```python
import torch

def _softmax(x, axis=-1):
    return torch.softmax(x, dim=axis)

def _gelu(x):
    return torch.nn.functional.gelu(x, approximate='tanh')

def _silu(x):
    return torch.nn.functional.silu(x)

def _sigmoid(x):
    return torch.sigmoid(x)

def _layernorm(x, axis=-1, eps=1e-05):
    m = x.mean(dim=axis, keepdim=True)
    v = x.var(dim=axis, keepdim=True, unbiased=False)
    return (x - m) / torch.sqrt(v + eps)

def _scatter_add(tgt, idx, src, axis=0):
    return tgt.clone().index_add_(axis, idx, src)

def _segment_sum(src, seg, n):
    out = torch.zeros((n,) + src.shape[1:], dtype=src.dtype)
    return out.index_add_(0, seg, src)

def _pad(x, pw):
    flat = tuple((v for pair in reversed(pw) for v in pair))
    return torch.nn.functional.pad(x, flat)
kernel_name = 'adaLN_cond__softmax_full_mha__mixer_gmlp__prompt'

def setup_inputs():

    _w = lambda a: (lambda t: t.to(torch.bfloat16) if t.is_floating_point() else t)(torch.as_tensor(a))

    def _A_setup():
        B, S, D = (1, 2048, 512)
        return {'x': _w((lambda *s: torch.randn(*s, dtype=torch.bfloat16) * 0.05)(B, S, D)), 'wqkv': _w((lambda *s: torch.randn(*s, dtype=torch.bfloat16) * 0.05)(D, 3 * D)), 'wo': _w((lambda *s: torch.randn(*s, dtype=torch.bfloat16) * 0.05)(D, D))}
    _A_in = _A_setup()

    def _B_setup():
        B, S, D, F = (1, 2048, 512, 1792)
        Fh = F // 2
        return {'x': _w((lambda *s: torch.randn(*s, dtype=torch.bfloat16) * 0.05)(B, S, D)), 'w_in': _w((lambda *s: torch.randn(*s, dtype=torch.bfloat16) * 0.4)(D, 2 * Fh)), 'w_sp': _w(0.01 * (lambda *s: torch.randn(*s, dtype=torch.bfloat16) * 0.4)(S, S)), 'b_sp': _w(1.0 + 0.01 * (lambda *s: torch.randn(*s, dtype=torch.bfloat16) * 0.05)(S)), 'w_out': _w((lambda *s: torch.randn(*s, dtype=torch.bfloat16) * 0.05)(Fh, D))}
    _B_in = _B_setup()
    return {'A_x': _A_in['x'], 'A_wqkv': _A_in['wqkv'], 'A_wo': _A_in['wo'], 'B_x': _B_in['x'], 'B_w_in': _B_in['w_in'], 'B_w_sp': _B_in['w_sp'], 'B_b_sp': _B_in['b_sp'], 'B_w_out': _B_in['w_out'], 'c': _w((lambda *s: torch.randn(*s, dtype=torch.bfloat16) * 0.1)(1, 512) * 8.0), 'w_mod': _w((lambda *s: torch.randn(*s, dtype=torch.bfloat16) * 0.1)(512, 6 * 512) * 512 ** (-0.5) * 8.0)}

def reference(A_x, A_wqkv, A_wo, B_x, B_w_in, B_w_sp, B_b_sp, B_w_out, c, w_mod):

    def _A_ref(x, wqkv, wo):
        B, S, D = x.shape
        H = 32
        Dh = D // H
        qkv = (lambda x, *d: x.permute(*d))((x @ wqkv).reshape(B, S, 3, H, Dh), 2, 0, 3, 1, 4)
        q, k, v = (qkv[0], qkv[1], qkv[2])
        scale = Dh ** (-0.5)
        scores = torch.einsum('bhqd,bhkd->bhqk', q, k) * scale
        p = _softmax(scores, axis=-1)
        o = torch.einsum('bhqk,bhkd->bhqd', p, v)
        return (lambda x, *d: x.permute(*d))(o, 0, 2, 1, 3).reshape(B, S, D) @ wo

    def _B_ref(x, w_in, w_sp, b_sp, w_out):
        B, S, D = x.shape
        F = w_in.shape[-1]
        Fh = F // 2
        z = _gelu(torch.einsum('bsd,df->bsf', x, w_in))
        z1 = z[..., :Fh].clone()
        z2 = _layernorm(z[..., Fh:], axis=-1)
        g = torch.einsum('ts,bsf->btf', w_sp, z2) + b_sp[None, :, None]
        return torch.einsum('bsf,fd->bsd', z1 * g, w_out)
    x = A_x
    mod = (_silu(c) @ w_mod).reshape(c.shape[0], 6, 1, -1)
    sa, ca, ga, sb, cb, gb = (mod[:, 0].clone(), mod[:, 1].clone(), mod[:, 2].clone(), mod[:, 3].clone(), mod[:, 4].clone(), mod[:, 5].clone())
    h = _layernorm(x, axis=-1) * (1 + ca) + sa
    x = x + ga * _A_ref(h, A_wqkv, A_wo)
    h = _layernorm(x, axis=-1) * (1 + cb) + sb
    x = x + gb * _B_ref(h, B_w_in, B_w_sp, B_b_sp, B_w_out)
    return x
```

```cpp
#include <hip/hip_runtime.h>
#include <hip/hip_bf16.h>
#include <stdint.h>
#define NEXTRA 0
typedef unsigned short u16;
__device__ __forceinline__ float b2f(u16 v) { return __uint_as_float(((unsigned)v) << 16); }
__device__ __forceinline__ u16 f2b(float f) { unsigned u = __float_as_uint(f); u += 0x7FFF + ((u >> 16) & 1); return (u16)(u >> 16); }
constexpr int S = 2048, D = 512, H = 32, DH = 16, F = 1792, FH = 896;

__global__ void k_mod(const u16* c, const u16* w, float* mod) {
    int n = blockIdx.x * 64 + (threadIdx.x & 63); int kq = threadIdx.x >> 6;
    __shared__ float red[4][64];
    float a = 0;
    for (int k = kq; k < D; k += 4) { float cv = b2f(c[k]); float s = cv / (1.f + __expf(-cv)); a += s * b2f(w[k * 3072 + n]); }
    red[kq][threadIdx.x & 63] = a; __syncthreads();
    if (kq == 0) mod[n] = red[0][threadIdx.x] + red[1][threadIdx.x] + red[2][threadIdx.x] + red[3][threadIdx.x];
}
template <typename T>
__global__ void k_ln(const T* x, int ld, int n, const float* sh, const float* sc, u16* out, int ldo) {
    int r = blockIdx.x; __shared__ float red[8];
    float v[4]; float s = 0;
    for (int i = 0; i < 4; ++i) { int c = threadIdx.x + i * 256; float t = 0; if (c < n) { if constexpr (sizeof(T) == 2) t = b2f(x[r * ld + c]); else t = x[r * ld + c]; } v[i] = t; s += t; }
    for (int o = 32; o; o >>= 1) s += __shfl_xor(s, o);
    if ((threadIdx.x & 63) == 0) red[threadIdx.x >> 6] = s; __syncthreads();
    float m = (red[0] + red[1] + red[2] + red[3]) / n; float q = 0;
    for (int i = 0; i < 4; ++i) { int c = threadIdx.x + i * 256; if (c < n) q += (v[i] - m) * (v[i] - m); }
    for (int o = 32; o; o >>= 1) q += __shfl_xor(q, o);
    if ((threadIdx.x & 63) == 0) red[4 + (threadIdx.x >> 6)] = q; __syncthreads();
    float rs = rsqrtf((red[4] + red[5] + red[6] + red[7]) / n + 1e-5f);
    for (int i = 0; i < 4; ++i) { int c = threadIdx.x + i * 256; if (c < n) { float t = (v[i] - m) * rs; if (sc) t = t * (1.f + sc[c]) + sh[c]; out[r * ldo + c] = f2b(t); } }
}
template <int MODE>
__global__ void k_gemm(const u16* A, int lda, const u16* B, int ldb, int K, void* C, int ldc, const void* e0, const void* e1) {
    __shared__ float As[16][65], Bs[16][65];
    int tx = threadIdx.x & 15, ty = threadIdx.x >> 4; int m0 = blockIdx.y * 64, n0 = blockIdx.x * 64;
    float acc[4][4] = {};
    for (int k0 = 0; k0 < K; k0 += 16) {
        for (int i = threadIdx.x; i < 1024; i += 256) { int r = i >> 4, k = i & 15; As[k][r] = b2f(A[(m0 + r) * lda + k0 + k]); int kk = i >> 6, c = i & 63; Bs[kk][c] = b2f(B[(k0 + kk) * ldb + n0 + c]); }
        __syncthreads();
        for (int k = 0; k < 16; ++k) for (int i = 0; i < 4; ++i) for (int j = 0; j < 4; ++j) acc[i][j] += As[k][ty * 4 + i] * Bs[k][tx * 4 + j];
        __syncthreads();
    }
    for (int i = 0; i < 4; ++i) for (int j = 0; j < 4; ++j) {
        int r = m0 + ty * 4 + i, c = n0 + tx * 4 + j; float a = acc[i][j];
        if (MODE == 0) ((u16*)C)[r * ldc + c] = f2b(a);
        if (MODE == 1) ((float*)C)[r * ldc + c] = b2f(((const u16*)e0)[r * D + c]) + ((const float*)e1)[c] * a;
        if (MODE == 2) { float t = 0.7978845608f * (a + 0.044715f * a * a * a); ((u16*)C)[r * ldc + c] = f2b(0.5f * a * (1.f + tanhf(t))); }
        if (MODE == 3) ((u16*)C)[r * ldc + c] = f2b((a + b2f(((const u16*)e0)[r])) * b2f(((const u16*)e1)[r * F + c]));
        if (MODE == 4) ((u16*)C)[r * ldc + c] = f2b(((const float*)e0)[r * D + c] + ((const float*)e1)[c] * a);
    }
}
__global__ void k_attn(const u16* qkv, u16* o) {
    int h = blockIdx.y, q = blockIdx.x * 256 + threadIdx.x;
    __shared__ float Ks[64][16], Vs[64][16];
    float qv[16], acc[16]; for (int d = 0; d < 16; ++d) { qv[d] = b2f(qkv[q * 1536 + h * 16 + d]) * 0.25f; acc[d] = 0; }
    float m = -1e30f, l = 0;
    for (int k0 = 0; k0 < S; k0 += 64) {
        for (int i = threadIdx.x; i < 1024; i += 256) { int r = i >> 4, d = i & 15; Ks[r][d] = b2f(qkv[(k0 + r) * 1536 + 512 + h * 16 + d]); Vs[r][d] = b2f(qkv[(k0 + r) * 1536 + 1024 + h * 16 + d]); }
        __syncthreads();
        for (int r = 0; r < 64; ++r) { float s = 0; for (int d = 0; d < 16; ++d) s += qv[d] * Ks[r][d];
            float mn = fmaxf(m, s); float al = __expf(m - mn), p = __expf(s - mn); l = l * al + p; for (int d = 0; d < 16; ++d) acc[d] = acc[d] * al + p * Vs[r][d]; m = mn; }
        __syncthreads();
    }
    for (int d = 0; d < 16; ++d) o[q * D + h * 16 + d] = f2b(acc[d] / l);
}
__global__ void k_empty(float* p) { if (p == nullptr) return; }

extern "C" void kernel_launch(void* const* d_in, const int* in_sizes, int n_in, void* d_out, int out_size, void* d_ws, size_t ws_size, hipStream_t stream) {
    const u16* x = (const u16*)d_in[0]; const u16* wqkv = (const u16*)d_in[1]; const u16* wo = (const u16*)d_in[2];
    const u16* w_in = (const u16*)d_in[4]; const u16* w_sp = (const u16*)d_in[5]; const u16* b_sp = (const u16*)d_in[6]; const u16* w_out = (const u16*)d_in[7];
    const u16* c = (const u16*)d_in[8]; const u16* w_mod = (const u16*)d_in[9];
    char* ws = (char*)d_ws;
    float* mod = (float*)ws; u16* h = (u16*)(ws + (1 << 20)); u16* qkv = (u16*)(ws + (4 << 20)); u16* o = (u16*)(ws + (12 << 20));
    float* x1 = (float*)(ws + (16 << 20)); u16* z = (u16*)(ws + (24 << 20)); u16* z2n = (u16*)(ws + (32 << 20)); u16* u = (u16*)(ws + (40 << 20));
    k_mod<<<48, 256, 0, stream>>>(c, w_mod, mod);
    k_ln<u16><<<S, 256, 0, stream>>>(x, D, D, mod, mod + D, h, D);
    k_gemm<0><<<dim3(24, 32), 256, 0, stream>>>(h, D, wqkv, 1536, D, qkv, 1536, nullptr, nullptr);
    k_attn<<<dim3(8, 32), 256, 0, stream>>>(qkv, o);
    k_gemm<1><<<dim3(8, 32), 256, 0, stream>>>(o, D, wo, D, D, x1, D, x, mod + 2 * D);
    k_ln<float><<<S, 256, 0, stream>>>(x1, D, D, mod + 3 * D, mod + 4 * D, h, D);
    k_gemm<2><<<dim3(28, 32), 256, 0, stream>>>(h, D, w_in, F, D, z, F, nullptr, nullptr);
    k_ln<u16><<<S, 256, 0, stream>>>(z + FH, F, FH, nullptr, nullptr, z2n, FH);
    k_gemm<3><<<dim3(14, 32), 256, 0, stream>>>(w_sp, S, z2n, FH, S, u, FH, b_sp, z);
    k_gemm<4><<<dim3(8, 32), 256, 0, stream>>>(u, FH, w_out, D, FH, d_out, D, x1, mod + 5 * D);
    for (int i = 0; i < NEXTRA; ++i) k_empty<<<256, 256, 0, stream>>>(mod);
}
```

```cpp
#include <hip/hip_runtime.h>
#include <hip/hip_bf16.h>

__global__ __launch_bounds__(512, 2) void fused_block_kernel(const void* c, const void* w_mod, const void* x, const void* wqkv, void* ws,
        const void* w_in, const void* wo, const void* w_sp, const void* w_out, const void* b_sp, void* out) {
#if defined(__HIP_DEVICE_COMPILE__)
    __shared__ __attribute__((aligned(16))) char lds[163840];
    unsigned tid = threadIdx.x;
    asm volatile(
"s_getpc_b64 s[44:45]\n"
"LSTART_%=:\n"
"s_load_dwordx16 s[48:63], %1, 0\n"
"s_load_dwordx4 s[64:67], %1, 64\n"
"s_load_dwordx2 s[68:69], %1, 80\n"
"s_mov_b32 s98, %2\n"
"v_mov_b32 v3, %0\n"
"v_lshrrev_b32 v4, 6, v3\n"
"v_and_b32 v1, 63, v3\n"
"v_readfirstlane_b32 s74, v4\n"
"s_lshr_b32 s77, s98, 3\n"
"s_and_b32 s82, s98, 7\n"
"s_and_b32 s45, s45, 0xffff\n"
"s_mov_b32 s46, LEND_%=-LSTART_%=\n"
"s_mov_b32 s47, 0x20000\n"
"s_and_b32 s83, s77, 7\n"
"v_lshlrev_b32 v5, 7, v3\n"
"s_cmp_lg_u32 s83, s74\n"
"s_cbranch_scc1 L1_%=\n"
"buffer_load_dword v199, v5, s[44:47], 0 offen\n"
"L1_%=:\n"
"s_lshl_b32 s86, s82, 3\n"
"s_lshl_b32 s87, s82, 4\n"
"s_add_u32 s87, s87, 56\n"
"s_cmp_lt_u32 s77, 8\n"
"s_cselect_b32 s86, s86, s87\n"
"s_add_u32 s86, s86, s77\n"
"s_lshl_b32 s89, s77, 6\n"
"v_lshrrev_b32 v4, 1, v3\n"
"v_add_u32 v4, s89, v4\n"
"v_and_b32 v4, 255, v4\n"
"v_and_b32 v5, 1, v3\n"
"v_mul_u32_u24 v6, 12288, v4\n"
"v_lshl_or_b32 v204, v5, 4, v6\n"
"v_lshlrev_b32 v205, 2, v4\n"
"s_mov_b32 s42, 1024\n"
"s_mov_b32 s43, 0x20000\n"
"s_mov_b32 s10, 2097152\n"
"s_mov_b32 s11, 0x20000\n"
"s_and_b32 s84, s82, 3\n"
"s_lshl_b32 s84, s84, 3\n"
"s_or_b32 s75, s84, s83\n"
"s_lshr_b32 s84, s82, 2\n"
"s_lshl_b32 s84, s84, 2\n"
"s_lshr_b32 s85, s77, 3\n"
"s_or_b32 s76, s84, s85\n"
"s_and_b32 s88, s75, 7\n"
"v_lshlrev_b32 v2, 4, v1\n"
"s_lshl_b32 s83, s88, 7\n"
"v_add_u32 v3, s83, v2\n"
"v_and_b32 v3, 0x3ff, v3\n"
"s_lshl_b32 s95, s76, 1\n"
"s_and_b32 s95, s95, 2\n"
"s_bfe_u32 s87, s76, 0x10001\n"
"s_or_b32 s95, s95, s87\n"
"s_lshl_b32 s95, s95, 1\n"
"s_and_b32 s95, s95, 6\n"
"s_xor_b32 s95, s95, s74\n"
"s_lshl_b32 s82, s75, 3\n"
"s_add_u32 s82, s82, s95\n"
"s_lshl_b32 s82, s82, 13\n"
"s_mov_b32 s85, s86\n"
"s_waitcnt lgkmcnt(0)\n"
"s_load_dwordx2 s[72:73], s[56:57], 0\n"
"s_cmp_ge_u32 s77, 24\n"
"s_cbranch_scc1 L2_%=\n"
"s_mov_b32 s44, s50\n"
"s_and_b32 s45, s51, 0xffff\n"
"s_mov_b32 s40, s48\n"
"s_and_b32 s41, s49, 0xffff\n"
"s_mov_b32 s46, 3145728\n"
"s_lshl_b32 s90, s85, 5\n"
"buffer_load_dword v0, v205, s[40:43], 0 offen\n"
"buffer_load_dwordx4 v[200:203], v204, s[44:47], s90 offen\n"
"s_add_u32 s90, s90, 6144\n"
"buffer_load_dwordx4 v[204:207], v204, s[44:47], s90 offen\n"
"s_barrier\n"
"L2_%=:\n"
"s_mov_b32 s8, s52\n"
"s_and_b32 s9, s53, 0xffff\n"
"buffer_load_dwordx4 v[16:19], v3, s[8:11], s82 offen offset:0\n"
"buffer_load_dwordx4 v[20:23], v3, s[8:11], s82 offen offset:1024\n"
"buffer_load_dwordx4 v[24:27], v3, s[8:11], s82 offen offset:2048\n"
"s_mov_b32 s12, s54\n"
"s_and_b32 s13, s55, 0xffff\n"
"s_mov_b32 s32, s56\n"
"s_and_b32 s33, s57, 0xffff\n"
"s_mov_b32 s20, s58\n"
"s_and_b32 s21, s59, 0xffff\n"
"s_mov_b32 s16, s60\n"
"s_and_b32 s17, s61, 0xffff\n"
"s_mov_b32 s24, s62\n"
"s_and_b32 s25, s63, 0xffff\n"
"buffer_load_dwordx4 v[28:31], v3, s[8:11], s82 offen offset:3072\n"
"s_mov_b32 s28, s64\n"
"s_and_b32 s29, s65, 0xffff\n"
"s_mov_b32 s36, s68\n"
"s_and_b32 s37, s69, 0xffff\n"
"s_mov_b32 s14, 1572864\n"
"s_mov_b32 s15, 0x20000\n"
"s_mov_b32 s34, 21102592\n"
"s_mov_b32 s35, 0x20000\n"
"s_mov_b32 s26, 8388608\n"
"s_mov_b32 s27, 0x20000\n"
"s_add_u32 s82, s82, 4096\n"
"buffer_load_dwordx4 v[32:35], v3, s[8:11], s82 offen offset:0\n"
"s_mov_b32 s38, 2097152\n"
"s_mov_b32 s39, 0x20000\n"
"s_mov_b32 s22, 1835008\n"
"s_mov_b32 s18, 524288\n"
"s_mov_b32 s30, 917504\n"
"s_mov_b32 s96, s66\n"
"s_and_b32 s97, s67, 0xffff\n"
"s_mov_b32 s78, 20000\n"
"s_mov_b32 s80, 0xffff\n"
"s_mov_b32 s81, 0xffff\n"
"s_lshl_b32 s99, s76, 2\n"
"s_and_b32 s90, s74, 3\n"
"s_add_u32 s90, s99, s90\n"
"s_lshr_b32 s91, s74, 2\n"
"buffer_load_dwordx4 v[36:39], v3, s[8:11], s82 offen offset:1024\n"
"s_lshl_b32 s69, s90, 1\n"
"s_add_u32 s69, s69, s91\n"
"s_lshl_b32 s69, s69, 8\n"
"s_add_u32 s69, s69, 32768\n"
"s_lshl_b32 s99, s90, 5\n"
"s_add_u32 s99, s99, s75\n"
"s_lshl_b32 s99, s99, 1\n"
"s_add_u32 s99, s99, s91\n"
"s_lshl_b32 s99, s99, 11\n"
"s_add_u32 s99, s99, 1048576\n"
"v_lshrrev_b32 v11, 3, v1\n"
"v_and_b32 v12, 7, v1\n"
"v_and_b32 v13, 2, v11\n"
"v_xor_b32 v13, v13, v12\n"
"s_and_b32 s83, s74, 1\n"
"s_lshl_b32 s83, s83, 2\n"
"v_xor_b32 v13, s83, v13\n"
"v_mul_u32_u24 v14, 3072, v11\n"
"v_lshl_or_b32 v4, v13, 4, v14\n"
"s_mul_i32 s83, s74, 24576\n"
"s_lshl_b32 s84, s76, 7\n"
"s_add_u32 s83, s83, s84\n"
"s_mul_i32 s84, s74, 3072\n"
"v_lshrrev_b32 v14, 4, v1\n"
"v_mul_u32_u24 v14, 3072, v14\n"
"v_bfe_u32 v15, v1, 2, 2\n"
"v_lshl_or_b32 v14, v15, 7, v14\n"
"v_xor_b32 v15, s74, v11\n"
"v_and_b32 v15, 3, v15\n"
"v_lshl_or_b32 v14, v15, 5, v14\n"
"buffer_load_dwordx4 v[40:43], v3, s[8:11], s82 offen offset:2048\n"
"v_and_b32 v15, 3, v1\n"
"v_lshl_or_b32 v14, v15, 3, v14\n"
"v_add_u32 v5, 65536, v14\n"
"v_add_u32 v6, 114688, v14\n"
"v_lshrrev_b32 v13, 2, v1\n"
"v_lshlrev_b32 v13, 12, v13\n"
"v_lshl_or_b32 v13, v15, 8, v13\n"
"v_lshl_or_b32 v13, v12, 4, v13\n"
"s_lshr_b32 s90, s95, 1\n"
"s_lshl_b32 s90, s90, 10\n"
"s_and_b32 s91, s74, 1\n"
"s_lshl_b32 s91, s91, 7\n"
"s_or_b32 s90, s90, s91\n"
"v_or_b32 v8, s90, v13\n"
"v_lshrrev_b32 v13, 4, v1\n"
"v_xor_b32 v13, v13, v1\n"
"s_lshr_b32 s90, s74, 2\n"
"s_lshl_b32 s90, s90, 11\n"
"v_lshlrev_b32 v13, 4, v13\n"
"v_add_u32 v9, s90, v13\n"
"v_xor_b32 v10, 64, v9\n"
"s_add_u32 s90, s84, 139264\n"
"v_add_u32 v7, s90, v2\n"
"s_lshl_b32 s79, s98, 2\n"
"s_sub_u32 s79, 1016, s79\n"
"s_add_u32 s79, s79, s74\n"
"s_cmp_ge_u32 s74, 4\n"
"s_cselect_b32 s79, s79, 0x7fff\n"
"s_cmp_ge_u32 s79, 800\n"
"s_cbranch_scc1 L3_%=\n"
"s_cmp_ge_u32 s79, 128\n"
"s_cbranch_scc1 L4_%=\n"
"s_mov_b64 s[40:41], s[16:17]\n"
"s_mov_b32 s42, s18\n"
"s_lshr_b32 s90, s79, 3\n"
"s_and_b32 s91, s79, 7\n"
"s_lshr_b32 s89, s90, 1\n"
"s_xor_b32 s89, s89, s91\n"
"s_and_b32 s89, s89, 4\n"
"s_and_b32 s92, s90, 3\n"
"s_or_b32 s89, s89, s92\n"
"s_and_b32 s92, s90, 4\n"
"s_lshr_b32 s94, s92, 1\n"
"s_lshl_b32 s92, s92, 1\n"
"s_or_b32 s89, s89, s92\n"
"s_mul_i32 s44, s91, 400\n"
"s_lshl_b32 s89, s89, 2\n"
"s_add_u32 s44, s44, s89\n"
"s_lshl_b32 s90, s90, 15\n"
"s_lshl_b32 s91, s91, 7\n"
"s_add_u32 s90, s90, s91\n"
"s_add_u32 s91, s90, 16384\n"
"s_mov_b32 s92, 0x08100410\n"
"s_mov_b32 s93, 1024\n"
"s_branch L7_%=\n"
"L4_%=:\n"
"s_cmp_ge_u32 s79, 576\n"
"s_cbranch_scc1 L5_%=\n"
"s_mov_b64 s[40:41], s[20:21]\n"
"s_mov_b32 s42, s22\n"
"s_sub_u32 s91, s79, 128\n"
"s_mul_i32 s90, s91, 2341\n"
"s_lshr_b32 s90, s90, 16\n"
"s_mul_i32 s89, s90, 28\n"
"s_sub_u32 s91, s91, s89\n"
"s_cmp_lt_u32 s91, 14\n"
"s_cselect_b32 s94, 176, 64\n"
"s_cselect_b32 s93, 0, 14\n"
"s_sub_u32 s93, s91, s93\n"
"s_cmp_ge_u32 s93, 7\n"
"s_cselect_b32 s92, 8, 0\n"
"s_xor_b32 s92, s92, s90\n"
"s_mul_i32 s92, s92, 7\n"
"s_add_u32 s92, s92, s94\n"
"s_lshl_b32 s93, s93, 2\n"
"s_add_u32 s44, s93, 0\n"
"s_mul_i32 s89, s44, 37\n"
"s_lshr_b32 s89, s89, 8\n"
"s_mul_i32 s89, s89, 393\n"
"s_add_u32 s44, s44, s89\n"
"s_add_u32 s44, s44, s92\n"
"s_add_u32 s45, s93, 1\n"
"s_mul_i32 s89, s45, 37\n"
"s_lshr_b32 s89, s89, 8\n"
"s_mul_i32 s89, s89, 393\n"
"s_add_u32 s45, s45, s89\n"
"s_add_u32 s45, s45, s92\n"
"s_add_u32 s46, s93, 2\n"
"s_mul_i32 s89, s46, 37\n"
"s_lshr_b32 s89, s89, 8\n"
"s_mul_i32 s89, s89, 393\n"
"s_add_u32 s46, s46, s89\n"
"s_add_u32 s46, s46, s92\n"
"s_add_u32 s47, s93, 3\n"
"s_mul_i32 s89, s47, 37\n"
"s_lshr_b32 s89, s89, 8\n"
"s_mul_i32 s89, s89, 393\n"
"s_add_u32 s47, s47, s89\n"
"s_add_u32 s47, s47, s92\n"
"s_lshl_b32 s67, s90, 7\n"
"s_lshl_b32 s66, s91, 13\n"
"s_add_u32 s66, s66, s67\n"
"s_add_u32 s66, s66, 262144\n"
"s_lshr_b32 s67, s67, 1\n"
"s_mul_i32 s90, s90, 114688\n"
"s_lshl_b32 s91, s91, 7\n"
"s_add_u32 s90, s90, s91\n"
"s_add_u32 s91, s90, 57344\n"
"s_mov_b32 s92, 0x20080810\n"
"s_mov_b32 s93, 512\n"
"s_mov_b32 s89, 3584\n"
"s_branch L6_%=\n"
"L5_%=:\n"
"s_mov_b64 s[40:41], s[28:29]\n"
"s_mov_b32 s42, s30\n"
"s_sub_u32 s91, s79, 576\n"
"s_lshr_b32 s90, s91, 3\n"
"s_and_b32 s91, s91, 7\n"
"s_mul_i32 s93, s90, 37\n"
"s_lshr_b32 s93, s93, 8\n"
"s_mul_i32 s89, s93, 7\n"
"s_sub_u32 s90, s90, s89\n"
"s_and_b32 s94, s93, 1\n"
"s_lshl_b32 s94, s94, 1\n"
"s_lshr_b32 s89, s91, 1\n"
"s_xor_b32 s89, s89, s93\n"
"s_lshr_b32 s89, s89, 1\n"
"s_add_u32 s89, s89, s94\n"
"s_mul_i32 s89, s89, 7\n"
"s_add_u32 s89, s89, s90\n"
"s_lshl_b32 s89, s89, 2\n"
"s_mul_i32 s44, s91, 400\n"
"s_add_u32 s44, s44, s89\n"
"s_add_u32 s44, s44, 288\n"
"s_cmp_ge_u32 s90, 3\n"
"s_cselect_b32 s89, 16, 0\n"
"s_lshl_b32 s92, s90, 5\n"
"s_add_u32 s89, s89, s92\n"
"s_cmp_lt_u32 s90, 6\n"
"s_cselect_b32 s89, s89, 0x60\n"
"s_cselect_b32 s92, 16, 0x70\n"
"s_cselect_b32 s66, 0, 1\n"
"s_mul_i32 s93, s93, 224\n"
"s_add_u32 s89, s89, s93\n"
"s_lshl_b32 s90, s89, 10\n"
"s_lshl_b32 s91, s91, 7\n"
"s_add_u32 s90, s90, s91\n"
"s_lshl_b32 s92, s92, 10\n"
"s_add_u32 s91, s90, s92\n"
"s_mov_b32 s92, 0x08100408\n"
"s_mov_b32 s93, 2048\n"
"s_cmp_eq_u32 s66, 1\n"
"s_cselect_b32 s92, 0x08100810, s92\n"
"s_cselect_b32 s93, 512, s93\n"
"L7_%=:\n"
"s_mov_b32 s89, 1024\n"
"s_mov_b32 s66, 2146435072\n"
"s_mov_b32 s67, 0\n"
"s_or_b32 s44, s44, s94\n"
"s_xor_b32 s45, s44, 1\n"
"s_xor_b32 s46, s44, 2\n"
"s_xor_b32 s47, s44, 3\n"
"L6_%=:\n"
"s_add_u32 s67, s67, 10240\n"
"s_mov_b32 s16, s89\n"
"s_mov_b32 s17, s92\n"
"s_mov_b32 s18, s93\n"
"s_mov_b32 s0, s90\n"
"s_mov_b32 s1, s91\n"
"s_nop 3\n"
"L3_%=:\n"
"buffer_load_dwordx4 v[44:47], v3, s[8:11], s82 offen offset:3072\n"
"s_barrier\n"
"s_add_u32 m0, s84, 65536\n"
"s_add_u32 s86, s88, 0\n"
"s_and_b32 s86, s86, 7\n"
"s_mul_i32 s86, s86, 196608\n"
"s_add_u32 s86, s86, s83\n"
"buffer_load_dwordx4 v4, s[12:15], s86 offen offset:0 lds\n"
"buffer_load_dwordx4 v4, s[12:15], s86 offen offset:1024 lds\n"
"buffer_load_dwordx4 v4, s[12:15], s86 offen offset:2048 lds\n"
"s_waitcnt lgkmcnt(0)\n"
"s_add_u32 s72, s72, 1\n"
"s_addc_u32 s73, s73, 0\n"
"v_mov_b32 v14, s72\n"
"v_mov_b32 v15, s73\n"
"s_cmp_ge_u32 s77, 24\n"
"s_cbranch_scc1 L8_%=\n"
"s_waitcnt vmcnt(13)\n"
"v_lshlrev_b32 v64, 16, v0\n"
"v_and_b32 v65, 0xffff0000, v0\n"
"v_mul_f32 v66, 0xbfb8aa3b, v64\n"
"v_mul_f32 v67, 0xbfb8aa3b, v65\n"
"v_exp_f32 v66, v66\n"
"v_exp_f32 v67, v67\n"
"v_and_b32 v68, 1, v1\n"
"v_lshlrev_b32 v68, 5, v68\n"
"v_lshrrev_b32 v69, 4, v1\n"
"v_lshl_or_b32 v68, v69, 3, v68\n"
"v_add_f32 v66, 1.0, v66\n"
"v_add_f32 v67, 1.0, v67\n"
"v_rcp_f32 v66, v66\n"
"v_rcp_f32 v67, v67\n"
"s_lshl_b32 s90, s74, 6\n"
"s_add_u32 s90, s90, 0\n"
"v_add_u32 v68, s90, v68\n"
"v_mul_f32 v66, v64, v66\n"
"v_mul_f32 v67, v65, v67\n"
"v_cvt_pk_bf16_f32 v66, v66, v67\n"
"v_lshlrev_b32 v64, 16, v66\n"
"v_and_b32 v65, 0xffff0000, v66\n"
"s_waitcnt vmcnt(12)\n"
"v_lshlrev_b32 v48, 16, v200\n"
"v_and_b32 v49, 0xffff0000, v200\n"
"v_lshlrev_b32 v50, 16, v201\n"
"v_and_b32 v51, 0xffff0000, v201\n"
"v_lshlrev_b32 v52, 16, v202\n"
"v_and_b32 v53, 0xffff0000, v202\n"
"v_lshlrev_b32 v54, 16, v203\n"
"v_and_b32 v55, 0xffff0000, v203\n"
"v_pk_mul_f32 v[48:49], v[48:49], v[64:65] op_sel_hi:[1,0]\n"
"v_pk_mul_f32 v[50:51], v[50:51], v[64:65] op_sel_hi:[1,0]\n"
"v_pk_mul_f32 v[52:53], v[52:53], v[64:65] op_sel_hi:[1,0]\n"
"v_pk_mul_f32 v[54:55], v[54:55], v[64:65] op_sel_hi:[1,0]\n"
"s_waitcnt vmcnt(11)\n"
"v_lshlrev_b32 v56, 16, v204\n"
"v_and_b32 v57, 0xffff0000, v204\n"
"v_lshlrev_b32 v58, 16, v205\n"
"v_and_b32 v59, 0xffff0000, v205\n"
"v_lshlrev_b32 v60, 16, v206\n"
"v_and_b32 v61, 0xffff0000, v206\n"
"v_lshlrev_b32 v62, 16, v207\n"
"v_and_b32 v63, 0xffff0000, v207\n"
"v_pk_fma_f32 v[48:49], v[64:65], v[56:57], v[48:49] op_sel:[1,0,0] op_sel_hi:[1,1,1]\n"
"v_pk_fma_f32 v[50:51], v[64:65], v[58:59], v[50:51] op_sel:[1,0,0] op_sel_hi:[1,1,1]\n"
"v_pk_fma_f32 v[52:53], v[64:65], v[60:61], v[52:53] op_sel:[1,0,0] op_sel_hi:[1,1,1]\n"
"v_pk_fma_f32 v[54:55], v[64:65], v[62:63], v[54:55] op_sel:[1,0,0] op_sel_hi:[1,1,1]\n"
"s_nop 0\n"
"v_permlane32_swap_b32 v48, v52\n"
"v_permlane32_swap_b32 v49, v53\n"
"v_permlane32_swap_b32 v50, v54\n"
"v_permlane32_swap_b32 v51, v55\n"
"v_pk_add_f32 v[48:49], v[48:49], v[52:53]\n"
"v_pk_add_f32 v[50:51], v[50:51], v[54:55]\n"
"s_nop 1\n"
"v_permlane16_swap_b32 v48, v50\n"
"v_permlane16_swap_b32 v49, v51\n"
"v_pk_add_f32 v[48:49], v[48:49], v[50:51]\n"
"s_nop 1\n"
"v_add_f32_dpp v48, v48, v48 row_ror:8 row_mask:0xf bank_mask:0xf\n"
"v_add_f32_dpp v49, v49, v49 row_ror:8 row_mask:0xf bank_mask:0xf\n"
"s_nop 0\n"
"v_add_f32_dpp v48, v48, v48 row_ror:4 row_mask:0xf bank_mask:0xf\n"
"v_add_f32_dpp v49, v49, v49 row_ror:4 row_mask:0xf bank_mask:0xf\n"
"s_nop 0\n"
"v_add_f32_dpp v48, v48, v48 quad_perm:[2,3,0,1] row_mask:0xf bank_mask:0xf\n"
"v_add_f32_dpp v49, v49, v49 quad_perm:[2,3,0,1] row_mask:0xf bank_mask:0xf\n"
"ds_write_b64 v68, v[48:49]\n"
"s_waitcnt lgkmcnt(0)\n"
"s_barrier\n"
"s_cmp_lg_u32 s74, 0\n"
"s_cbranch_scc1 L9_%=\n"
"v_and_b32 v60, 0xf0, v2\n"
"v_add_u32 v60, 0, v60\n"
"ds_read_b128 v[48:51], v60\n"
"ds_read_b128 v[52:55], v60 offset:256\n"
"s_lshr_b32 s91, s85, 5\n"
"s_lshl_b32 s90, s91, 10\n"
"s_lshl_b32 s92, s85, 5\n"
"s_add_u32 s90, s90, s92\n"
"s_add_u32 s90, s90, 4096\n"
"s_bitcmp1_b32 0x12, s91\n"
"s_cselect_b32 s92, 1.0, 0\n"
"v_and_b32 v60, 1, v1\n"
"v_lshlrev_b32 v60, 10, v60\n"
"v_lshrrev_b32 v56, 1, v1\n"
"v_lshl_or_b32 v60, v56, 4, v60\n"
"v_mov_b32 v57, s72\n"
"v_mov_b32 v59, s73\n"
"s_waitcnt lgkmcnt(0)\n"
"v_pk_add_f32 v[48:49], v[48:49], v[52:53]\n"
"v_pk_add_f32 v[50:51], v[50:51], v[54:55]\n"
"s_nop 0\n"
"v_add_f32_dpp v48, v48, v48 row_ror:8 row_mask:0xf bank_mask:0xf\n"
"v_add_f32_dpp v49, v49, v49 row_ror:8 row_mask:0xf bank_mask:0xf\n"
"v_add_f32_dpp v50, v50, v50 row_ror:8 row_mask:0xf bank_mask:0xf\n"
"v_add_f32_dpp v51, v51, v51 row_ror:8 row_mask:0xf bank_mask:0xf\n"
"v_add_f32_dpp v48, v48, v48 row_ror:4 row_mask:0xf bank_mask:0xf\n"
"v_add_f32_dpp v49, v49, v49 row_ror:4 row_mask:0xf bank_mask:0xf\n"
"v_add_f32_dpp v50, v50, v50 row_ror:4 row_mask:0xf bank_mask:0xf\n"
"v_add_f32_dpp v51, v51, v51 row_ror:4 row_mask:0xf bank_mask:0xf\n"
"v_cvt_pk_bf16_f32 v48, 0, v48\n"
"v_cvt_pk_bf16_f32 v49, 0, v49\n"
"v_cvt_pk_bf16_f32 v50, 0, v50\n"
"v_cvt_pk_bf16_f32 v51, 0, v51\n"
"v_pk_add_f32 v[48:49], v[48:49], s[92:93] op_sel_hi:[1,0]\n"
"v_pk_add_f32 v[50:51], v[50:51], s[92:93] op_sel_hi:[1,0]\n"
"v_cvt_pk_bf16_f32 v56, v48, v49\n"
"v_cvt_pk_bf16_f32 v58, v50, v51\n"
"s_mov_b64 exec, 15\n"
"buffer_store_dwordx4 v[56:59], v60, s[32:35], s90 offen sc1\n"
"s_mov_b64 exec, -1\n"
"s_nop 3\n"
"L9_%=:\n"
"s_nop 3\n"
"L8_%=:\n"
"s_cmp_lt_u32 s77, 24\n"
"s_cbranch_scc1 L10_%=\n"
"s_sleep 20\n"
"s_nop 3\n"
"L10_%=:\n"
"v_bfe_u32 v12, v1, 3, 2\n"
"v_and_b32 v13, 0x70, v2\n"
"v_lshl_or_b32 v11, v12, 10, v13\n"
"s_lshl_b32 s90, s74, 7\n"
"s_add_u32 s87, s90, 4096\n"
"v_and_b32 v12, 8, v1\n"
"v_or_b32 v13, v13, v12\n"
"v_bfe_u32 v12, v1, 4, 1\n"
"v_lshl_or_b32 v13, v12, 10, v13\n"
"v_add_u32 v13, s90, v13\n"
"s_waitcnt vmcnt(10)\n"
"v_lshlrev_b32 v48, 16, v16\n"
"v_and_b32 v49, 0xffff0000, v16\n"
"v_lshlrev_b32 v50, 16, v17\n"
"v_and_b32 v51, 0xffff0000, v17\n"
"v_lshlrev_b32 v52, 16, v18\n"
"v_and_b32 v53, 0xffff0000, v18\n"
"v_lshlrev_b32 v54, 16, v19\n"
"v_and_b32 v55, 0xffff0000, v19\n"
"v_pk_mul_f32 v[16:17], v[48:49], v[48:49]\n"
"v_pk_add_f32 v[18:19], v[48:49], v[50:51]\n"
"v_pk_fma_f32 v[16:17], v[50:51], v[50:51], v[16:17]\n"
"v_pk_add_f32 v[18:19], v[18:19], v[52:53]\n"
"v_pk_fma_f32 v[16:17], v[52:53], v[52:53], v[16:17]\n"
"v_pk_add_f32 v[18:19], v[18:19], v[54:55]\n"
"v_pk_fma_f32 v[16:17], v[54:55], v[54:55], v[16:17]\n"
"v_add_f32 v112, v18, v19\n"
"v_add_f32 v120, v16, v17\n"
"s_waitcnt vmcnt(9)\n"
"v_lshlrev_b32 v56, 16, v20\n"
"v_and_b32 v57, 0xffff0000, v20\n"
"v_lshlrev_b32 v58, 16, v21\n"
"s_add_u32 m0, s84, 90112\n"
"s_add_u32 s86, s88, 1\n"
"s_and_b32 s86, s86, 7\n"
"s_mul_i32 s86, s86, 196608\n"
"s_add_u32 s86, s86, s83\n"
"buffer_load_dwordx4 v4, s[12:15], s86 offen offset:0 lds\n"
"v_and_b32 v59, 0xffff0000, v21\n"
"v_lshlrev_b32 v60, 16, v22\n"
"v_and_b32 v61, 0xffff0000, v22\n"
"v_lshlrev_b32 v62, 16, v23\n"
"v_and_b32 v63, 0xffff0000, v23\n"
"v_pk_mul_f32 v[20:21], v[56:57], v[56:57]\n"
"v_pk_add_f32 v[22:23], v[56:57], v[58:59]\n"
"v_pk_fma_f32 v[20:21], v[58:59], v[58:59], v[20:21]\n"
"v_pk_add_f32 v[22:23], v[22:23], v[60:61]\n"
"v_pk_fma_f32 v[20:21], v[60:61], v[60:61], v[20:21]\n"
"v_pk_add_f32 v[22:23], v[22:23], v[62:63]\n"
"v_pk_fma_f32 v[20:21], v[62:63], v[62:63], v[20:21]\n"
"v_add_f32 v113, v22, v23\n"
"v_add_f32 v121, v20, v21\n"
"s_waitcnt vmcnt(9)\n"
"v_lshlrev_b32 v64, 16, v24\n"
"v_and_b32 v65, 0xffff0000, v24\n"
"v_lshlrev_b32 v66, 16, v25\n"
"v_and_b32 v67, 0xffff0000, v25\n"
"v_lshlrev_b32 v68, 16, v26\n"
"buffer_load_dwordx4 v4, s[12:15], s86 offen offset:1024 lds\n"
"v_and_b32 v69, 0xffff0000, v26\n"
"v_lshlrev_b32 v70, 16, v27\n"
"v_and_b32 v71, 0xffff0000, v27\n"
"v_pk_mul_f32 v[24:25], v[64:65], v[64:65]\n"
"v_pk_add_f32 v[26:27], v[64:65], v[66:67]\n"
"v_pk_fma_f32 v[24:25], v[66:67], v[66:67], v[24:25]\n"
"v_pk_add_f32 v[26:27], v[26:27], v[68:69]\n"
"v_pk_fma_f32 v[24:25], v[68:69], v[68:69], v[24:25]\n"
"v_pk_add_f32 v[26:27], v[26:27], v[70:71]\n"
"v_pk_fma_f32 v[24:25], v[70:71], v[70:71], v[24:25]\n"
"v_add_f32 v114, v26, v27\n"
"v_add_f32 v122, v24, v25\n"
"s_waitcnt vmcnt(9)\n"
"v_lshlrev_b32 v72, 16, v28\n"
"v_and_b32 v73, 0xffff0000, v28\n"
"v_lshlrev_b32 v74, 16, v29\n"
"v_and_b32 v75, 0xffff0000, v29\n"
"v_lshlrev_b32 v76, 16, v30\n"
"v_and_b32 v77, 0xffff0000, v30\n"
"v_lshlrev_b32 v78, 16, v31\n"
"v_and_b32 v79, 0xffff0000, v31\n"
"buffer_load_dwordx4 v4, s[12:15], s86 offen offset:2048 lds\n"
"v_pk_mul_f32 v[28:29], v[72:73], v[72:73]\n"
"v_pk_add_f32 v[30:31], v[72:73], v[74:75]\n"
"v_pk_fma_f32 v[28:29], v[74:75], v[74:75], v[28:29]\n"
"v_pk_add_f32 v[30:31], v[30:31], v[76:77]\n"
"v_pk_fma_f32 v[28:29], v[76:77], v[76:77], v[28:29]\n"
"v_pk_add_f32 v[30:31], v[30:31], v[78:79]\n"
"v_pk_fma_f32 v[28:29], v[78:79], v[78:79], v[28:29]\n"
"v_add_f32 v115, v30, v31\n"
"v_add_f32 v123, v28, v29\n"
"s_waitcnt vmcnt(9)\n"
"v_lshlrev_b32 v80, 16, v32\n"
"v_and_b32 v81, 0xffff0000, v32\n"
"v_lshlrev_b32 v82, 16, v33\n"
"v_and_b32 v83, 0xffff0000, v33\n"
"v_lshlrev_b32 v84, 16, v34\n"
"v_and_b32 v85, 0xffff0000, v34\n"
"v_lshlrev_b32 v86, 16, v35\n"
"v_and_b32 v87, 0xffff0000, v35\n"
"v_pk_mul_f32 v[32:33], v[80:81], v[80:81]\n"
"v_pk_add_f32 v[34:35], v[80:81], v[82:83]\n"
"s_add_u32 m0, s84, 114688\n"
"s_add_u32 s86, s88, 2\n"
"s_and_b32 s86, s86, 7\n"
"s_mul_i32 s86, s86, 196608\n"
"s_add_u32 s86, s86, s83\n"
"buffer_load_dwordx4 v4, s[12:15], s86 offen offset:0 lds\n"
"v_pk_fma_f32 v[32:33], v[82:83], v[82:83], v[32:33]\n"
"v_pk_add_f32 v[34:35], v[34:35], v[84:85]\n"
"v_pk_fma_f32 v[32:33], v[84:85], v[84:85], v[32:33]\n"
"v_pk_add_f32 v[34:35], v[34:35], v[86:87]\n"
"v_pk_fma_f32 v[32:33], v[86:87], v[86:87], v[32:33]\n"
"v_add_f32 v116, v34, v35\n"
"v_add_f32 v124, v32, v33\n"
"s_waitcnt vmcnt(9)\n"
"v_lshlrev_b32 v88, 16, v36\n"
"v_and_b32 v89, 0xffff0000, v36\n"
"v_lshlrev_b32 v90, 16, v37\n"
"v_and_b32 v91, 0xffff0000, v37\n"
"v_lshlrev_b32 v92, 16, v38\n"
"v_and_b32 v93, 0xffff0000, v38\n"
"v_lshlrev_b32 v94, 16, v39\n"
"v_and_b32 v95, 0xffff0000, v39\n"
"v_pk_mul_f32 v[36:37], v[88:89], v[88:89]\n"
"v_pk_add_f32 v[38:39], v[88:89], v[90:91]\n"
"v_pk_fma_f32 v[36:37], v[90:91], v[90:91], v[36:37]\n"
"v_pk_add_f32 v[38:39], v[38:39], v[92:93]\n"
"v_pk_fma_f32 v[36:37], v[92:93], v[92:93], v[36:37]\n"
"buffer_load_dwordx4 v4, s[12:15], s86 offen offset:1024 lds\n"
"v_pk_add_f32 v[38:39], v[38:39], v[94:95]\n"
"v_pk_fma_f32 v[36:37], v[94:95], v[94:95], v[36:37]\n"
"v_add_f32 v117, v38, v39\n"
"v_add_f32 v125, v36, v37\n"
"s_waitcnt vmcnt(9)\n"
"v_lshlrev_b32 v96, 16, v40\n"
"v_and_b32 v97, 0xffff0000, v40\n"
"v_lshlrev_b32 v98, 16, v41\n"
"v_and_b32 v99, 0xffff0000, v41\n"
"v_lshlrev_b32 v100, 16, v42\n"
"v_and_b32 v101, 0xffff0000, v42\n"
"v_lshlrev_b32 v102, 16, v43\n"
"v_and_b32 v103, 0xffff0000, v43\n"
"v_pk_mul_f32 v[40:41], v[96:97], v[96:97]\n"
"v_pk_add_f32 v[42:43], v[96:97], v[98:99]\n"
"v_pk_fma_f32 v[40:41], v[98:99], v[98:99], v[40:41]\n"
"v_pk_add_f32 v[42:43], v[42:43], v[100:101]\n"
"v_pk_fma_f32 v[40:41], v[100:101], v[100:101], v[40:41]\n"
"v_pk_add_f32 v[42:43], v[42:43], v[102:103]\n"
"v_pk_fma_f32 v[40:41], v[102:103], v[102:103], v[40:41]\n"
"buffer_load_dwordx4 v4, s[12:15], s86 offen offset:2048 lds\n"
"v_add_f32 v118, v42, v43\n"
"v_add_f32 v126, v40, v41\n"
"s_waitcnt vmcnt(9)\n"
"v_lshlrev_b32 v104, 16, v44\n"
"v_and_b32 v105, 0xffff0000, v44\n"
"v_lshlrev_b32 v106, 16, v45\n"
"v_and_b32 v107, 0xffff0000, v45\n"
"v_lshlrev_b32 v108, 16, v46\n"
"v_and_b32 v109, 0xffff0000, v46\n"
"v_lshlrev_b32 v110, 16, v47\n"
"v_and_b32 v111, 0xffff0000, v47\n"
"v_pk_mul_f32 v[44:45], v[104:105], v[104:105]\n"
"v_pk_add_f32 v[46:47], v[104:105], v[106:107]\n"
"v_pk_fma_f32 v[44:45], v[106:107], v[106:107], v[44:45]\n"
"v_pk_add_f32 v[46:47], v[46:47], v[108:109]\n"
"v_pk_fma_f32 v[44:45], v[108:109], v[108:109], v[44:45]\n"
"v_pk_add_f32 v[46:47], v[46:47], v[110:111]\n"
"v_pk_fma_f32 v[44:45], v[110:111], v[110:111], v[44:45]\n"
"v_add_f32 v119, v46, v47\n"
"v_add_f32 v127, v44, v45\n"
"v_mov_b64 v[192:193], -1\n"
"v_mov_b64 v[194:195], -1\n"
"buffer_store_dwordx4 v[192:195], v2, s[32:35], s99 offen offset:0 sc1\n"
"buffer_store_dwordx4 v[192:195], v2, s[32:35], s99 offen offset:1024 sc1\n"
"v_permlane32_swap_b32 v112, v120\n"
"v_permlane32_swap_b32 v113, v121\n"
"v_permlane32_swap_b32 v114, v122\n"
"v_permlane32_swap_b32 v115, v123\n"
"v_permlane32_swap_b32 v116, v124\n"
"v_permlane32_swap_b32 v117, v125\n"
"v_permlane32_swap_b32 v118, v126\n"
"v_permlane32_swap_b32 v119, v127\n"
"v_pk_add_f32 v[112:113], v[112:113], v[120:121]\n"
"v_pk_add_f32 v[116:117], v[116:117], v[124:125]\n"
"v_pk_add_f32 v[114:115], v[114:115], v[122:123]\n"
"v_pk_add_f32 v[118:119], v[118:119], v[126:127]\n"
"v_permlane16_swap_b32 v112, v116\n"
"v_permlane16_swap_b32 v113, v117\n"
"v_permlane16_swap_b32 v114, v118\n"
"v_permlane16_swap_b32 v115, v119\n"
"v_pk_add_f32 v[112:113], v[112:113], v[116:117]\n"
"v_pk_add_f32 v[114:115], v[114:115], v[118:119]\n"
"s_nop 0\n"
"v_add_f32_dpp v112, v112, v112 row_ror:8 row_mask:0xf bank_mask:0x3\n"
"v_add_f32_dpp v113, v113, v113 row_ror:8 row_mask:0xf bank_mask:0x3\n"
"s_waitcnt vmcnt(2)\n"
"s_barrier\n"
"v_add_f32_dpp v112, v114, v114 row_ror:8 row_mask:0xf bank_mask:0xc\n"
"v_add_f32_dpp v113, v115, v115 row_ror:8 row_mask:0xf bank_mask:0xc\n"
"ds_read_b64_tr_b16 v[128:129], v5 offset:0\n"
"v_add_f32_dpp v112, v112, v112 row_half_mirror row_mask:0xf bank_mask:0x5\n"
"s_nop 1\n"
"v_add_f32_dpp v112, v113, v113 row_half_mirror row_mask:0xf bank_mask:0xa\n"
"ds_read_b64_tr_b16 v[130:131], v5 offset:512\n"
"s_nop 0\n"
"v_add_f32_dpp v112, v112, v112 quad_perm:[1,0,3,2] row_mask:0xf bank_mask:0xf\n"
"ds_read_b64_tr_b16 v[132:133], v5 offset:1024\n"
"s_nop 0\n"
"v_add_f32_dpp v112, v112, v112 quad_perm:[2,3,0,1] row_mask:0xf bank_mask:0xf\n"
"v_mul_f32 v112, 0x3b000000, v112\n"
"ds_read_b64_tr_b16 v[134:135], v5 offset:1536\n"
"s_add_u32 m0, s84, 139264\n"
"s_add_u32 s86, s88, 3\n"
"s_and_b32 s86, s86, 7\n"
"s_mul_i32 s86, s86, 196608\n"
"s_add_u32 s86, s86, s83\n"
"buffer_load_dwordx4 v4, s[12:15], s86 offen offset:0 lds\n"
"v_mov_b32 v113, v112\n"
"ds_read_b64_tr_b16 v[136:137], v5 offset:2048\n"
"s_nop 0\n"
"v_permlane32_swap_b32 v112, v113\n"
"v_fma_f32 v113, -v112, v112, v113\n"
"ds_read_b64_tr_b16 v[138:139], v5 offset:2560\n"
"v_max_f32 v113, 0, v113\n"
"v_cvt_pk_bf16_f32 v113, 0, v113\n"
"ds_read_b64_tr_b16 v[140:141], v5 offset:12288\n"
"v_add_f32 v113, 0x3727c5ac, v113\n"
"ds_read_b64_tr_b16 v[142:143], v5 offset:12800\n"
"buffer_load_dwordx4 v4, s[12:15], s86 offen offset:1024 lds\n"
"v_cvt_pk_bf16_f32 v113, 0, v113\n"
"v_sqrt_f32 v113, v113\n"
"ds_read_b64_tr_b16 v[144:145], v5 offset:13312\n"
"v_cvt_pk_bf16_f32 v112, 0, v112\n"
"ds_read_b64_tr_b16 v[146:147], v5 offset:13824\n"
"v_cvt_pk_bf16_f32 v113, 0, v113\n"
"v_rcp_f32 v113, v113\n"
"ds_read_b64_tr_b16 v[148:149], v5 offset:14336\n"
"v_mul_f32 v112, v112, v113\n"
"ds_read_b64_tr_b16 v[150:151], v5 offset:14848\n"
"buffer_load_dwordx4 v4, s[12:15], s86 offen offset:2048 lds\n"
"buffer_load_dwordx4 v[16:19], v11, s[32:35], s87 offen sc1\n"
"v_readlane_b32 s28, v113, 0\n"
"v_readlane_b32 s29, v112, 0\n"
"ds_read_b64_tr_b16 v[152:153], v5 offset:24576\n"
"v_readlane_b32 s30, v113, 4\n"
"v_readlane_b32 s31, v112, 4\n"
"ds_read_b64_tr_b16 v[154:155], v5 offset:25088\n"
"v_pk_fma_f32 v[48:49], s[28:29], v[48:49], s[28:29] op_sel:[0,0,1] op_sel_hi:[0,1,1] neg_lo:[0,0,1] neg_hi:[0,0,1]\n"
"ds_read_b64_tr_b16 v[156:157], v5 offset:25600\n"
"v_pk_fma_f32 v[50:51], s[28:29], v[50:51], s[28:29] op_sel:[0,0,1] op_sel_hi:[0,1,1] neg_lo:[0,0,1] neg_hi:[0,0,1]\n"
"v_pk_fma_f32 v[52:53], s[28:29], v[52:53], s[28:29] op_sel:[0,0,1] op_sel_hi:[0,1,1] neg_lo:[0,0,1] neg_hi:[0,0,1]\n"
"ds_read_b64_tr_b16 v[158:159], v5 offset:26112\n"
"v_pk_fma_f32 v[54:55], s[28:29], v[54:55], s[28:29] op_sel:[0,0,1] op_sel_hi:[0,1,1] neg_lo:[0,0,1] neg_hi:[0,0,1]\n"
"v_readlane_b32 s28, v113, 8\n"
"ds_read_b64_tr_b16 v[160:161], v5 offset:26624\n"
"v_readlane_b32 s29, v112, 8\n"
"ds_read_b64_tr_b16 v[162:163], v5 offset:27136\n"
"v_pk_fma_f32 v[56:57], s[30:31], v[56:57], s[30:31] op_sel:[0,0,1] op_sel_hi:[0,1,1] neg_lo:[0,0,1] neg_hi:[0,0,1]\n"
"v_pk_fma_f32 v[58:59], s[30:31], v[58:59], s[30:31] op_sel:[0,0,1] op_sel_hi:[0,1,1] neg_lo:[0,0,1] neg_hi:[0,0,1]\n"
"ds_read_b64_tr_b16 v[164:165], v5 offset:36864\n"
"v_pk_fma_f32 v[60:61], s[30:31], v[60:61], s[30:31] op_sel:[0,0,1] op_sel_hi:[0,1,1] neg_lo:[0,0,1] neg_hi:[0,0,1]\n"
"s_add_u32 s86, s88, 7\n"
"s_and_b32 s86, s86, 7\n"
"s_mul_i32 s86, s86, 196608\n"
"s_add_u32 s86, s86, s83\n"
"buffer_load_dwordx4 v[208:211], v4, s[12:15], s86 offen offset:0\n"
"v_pk_fma_f32 v[62:63], s[30:31], v[62:63], s[30:31] op_sel:[0,0,1] op_sel_hi:[0,1,1] neg_lo:[0,0,1] neg_hi:[0,0,1]\n"
"ds_read_b64_tr_b16 v[166:167], v5 offset:37376\n"
"v_readlane_b32 s30, v113, 12\n"
"ds_read_b64_tr_b16 v[168:169], v5 offset:37888\n"
"v_readlane_b32 s31, v112, 12\n"
"v_pk_fma_f32 v[64:65], s[28:29], v[64:65], s[28:29] op_sel:[0,0,1] op_sel_hi:[0,1,1] neg_lo:[0,0,1] neg_hi:[0,0,1]\n"
"ds_read_b64_tr_b16 v[170:171], v5 offset:38400\n"
"v_pk_fma_f32 v[66:67], s[28:29], v[66:67], s[28:29] op_sel:[0,0,1] op_sel_hi:[0,1,1] neg_lo:[0,0,1] neg_hi:[0,0,1]\n"
"v_pk_fma_f32 v[68:69], s[28:29], v[68:69], s[28:29] op_sel:[0,0,1] op_sel_hi:[0,1,1] neg_lo:[0,0,1] neg_hi:[0,0,1]\n"
"ds_read_b64_tr_b16 v[172:173], v5 offset:38912\n"
"v_pk_fma_f32 v[70:71], s[28:29], v[70:71], s[28:29] op_sel:[0,0,1] op_sel_hi:[0,1,1] neg_lo:[0,0,1] neg_hi:[0,0,1]\n"
"ds_read_b64_tr_b16 v[174:175], v5 offset:39424\n"
"v_readlane_b32 s28, v113, 16\n"
"v_readlane_b32 s29, v112, 16\n"
"ds_read_b64_tr_b16 v[176:177], v6 offset:0\n"
"v_pk_fma_f32 v[72:73], s[30:31], v[72:73], s[30:31] op_sel:[0,0,1] op_sel_hi:[0,1,1] neg_lo:[0,0,1] neg_hi:[0,0,1]\n"
"v_pk_fma_f32 v[74:75], s[30:31], v[74:75], s[30:31] op_sel:[0,0,1] op_sel_hi:[0,1,1] neg_lo:[0,0,1] neg_hi:[0,0,1]\n"
"ds_read_b64_tr_b16 v[178:179], v6 offset:512\n"
"v_pk_fma_f32 v[76:77], s[30:31], v[76:77], s[30:31] op_sel:[0,0,1] op_sel_hi:[0,1,1] neg_lo:[0,0,1] neg_hi:[0,0,1]\n"
"ds_read_b64_tr_b16 v[180:181], v6 offset:1024\n"
"buffer_load_dwordx4 v[212:215], v4, s[12:15], s86 offen offset:1024\n"
"v_pk_fma_f32 v[78:79], s[30:31], v[78:79], s[30:31] op_sel:[0,0,1] op_sel_hi:[0,1,1] neg_lo:[0,0,1] neg_hi:[0,0,1]\n"
"v_readlane_b32 s30, v113, 20\n"
"ds_read_b64_tr_b16 v[182:183], v6 offset:1536\n"
"v_readlane_b32 s31, v112, 20\n"
"v_pk_fma_f32 v[80:81], s[28:29], v[80:81], s[28:29] op_sel:[0,0,1] op_sel_hi:[0,1,1] neg_lo:[0,0,1] neg_hi:[0,0,1]\n"
"ds_read_b64_tr_b16 v[184:185], v6 offset:2048\n"
"v_pk_fma_f32 v[82:83], s[28:29], v[82:83], s[28:29] op_sel:[0,0,1] op_sel_hi:[0,1,1] neg_lo:[0,0,1] neg_hi:[0,0,1]\n"
"ds_read_b64_tr_b16 v[186:187], v6 offset:2560\n"
"v_pk_fma_f32 v[84:85], s[28:29], v[84:85], s[28:29] op_sel:[0,0,1] op_sel_hi:[0,1,1] neg_lo:[0,0,1] neg_hi:[0,0,1]\n"
"v_pk_fma_f32 v[86:87], s[28:29], v[86:87], s[28:29] op_sel:[0,0,1] op_sel_hi:[0,1,1] neg_lo:[0,0,1] neg_hi:[0,0,1]\n"
"ds_read_b64_tr_b16 v[188:189], v6 offset:12288\n"
"v_readlane_b32 s28, v113, 24\n"
"v_readlane_b32 s29, v112, 24\n"
"ds_read_b64_tr_b16 v[190:191], v6 offset:12800\n"
"v_pk_fma_f32 v[88:89], s[30:31], v[88:89], s[30:31] op_sel:[0,0,1] op_sel_hi:[0,1,1] neg_lo:[0,0,1] neg_hi:[0,0,1]\n"
"ds_read_b64_tr_b16 v[192:193], v6 offset:13312\n"
"v_pk_fma_f32 v[90:91], s[30:31], v[90:91], s[30:31] op_sel:[0,0,1] op_sel_hi:[0,1,1] neg_lo:[0,0,1] neg_hi:[0,0,1]\n"
"v_pk_fma_f32 v[92:93], s[30:31], v[92:93], s[30:31] op_sel:[0,0,1] op_sel_hi:[0,1,1] neg_lo:[0,0,1] neg_hi:[0,0,1]\n"
"ds_read_b64_tr_b16 v[194:195], v6 offset:13824\n"
"v_pk_fma_f32 v[94:95], s[30:31], v[94:95], s[30:31] op_sel:[0,0,1] op_sel_hi:[0,1,1] neg_lo:[0,0,1] neg_hi:[0,0,1]\n"
"buffer_load_dwordx4 v[216:219], v4, s[12:15], s86 offen offset:2048\n"
"v_readlane_b32 s30, v113, 28\n"
"ds_read_b64_tr_b16 v[196:197], v6 offset:14336\n"
"v_readlane_b32 s31, v112, 28\n"
"ds_read_b64_tr_b16 v[198:199], v6 offset:14848\n"
"v_pk_fma_f32 v[96:97], s[28:29], v[96:97], s[28:29] op_sel:[0,0,1] op_sel_hi:[0,1,1] neg_lo:[0,0,1] neg_hi:[0,0,1]\n"
"v_pk_fma_f32 v[98:99], s[28:29], v[98:99], s[28:29] op_sel:[0,0,1] op_sel_hi:[0,1,1] neg_lo:[0,0,1] neg_hi:[0,0,1]\n"
"v_pk_fma_f32 v[100:101], s[28:29], v[100:101], s[28:29] op_sel:[0,0,1] op_sel_hi:[0,1,1] neg_lo:[0,0,1] neg_hi:[0,0,1]\n"
"v_pk_fma_f32 v[102:103], s[28:29], v[102:103], s[28:29] op_sel:[0,0,1] op_sel_hi:[0,1,1] neg_lo:[0,0,1] neg_hi:[0,0,1]\n"
"v_pk_fma_f32 v[104:105], s[30:31], v[104:105], s[30:31] op_sel:[0,0,1] op_sel_hi:[0,1,1] neg_lo:[0,0,1] neg_hi:[0,0,1]\n"
"v_pk_fma_f32 v[106:107], s[30:31], v[106:107], s[30:31] op_sel:[0,0,1] op_sel_hi:[0,1,1] neg_lo:[0,0,1] neg_hi:[0,0,1]\n"
"v_pk_fma_f32 v[108:109], s[30:31], v[108:109], s[30:31] op_sel:[0,0,1] op_sel_hi:[0,1,1] neg_lo:[0,0,1] neg_hi:[0,0,1]\n"
"v_pk_fma_f32 v[110:111], s[30:31], v[110:111], s[30:31] op_sel:[0,0,1] op_sel_hi:[0,1,1] neg_lo:[0,0,1] neg_hi:[0,0,1]\n"
"s_waitcnt vmcnt(3)\n"
"v_cmp_ne_u32 vcc, s72, v17\n"
"v_cmp_ne_u32 s[92:93], s73, v19\n"
"s_nop 0\n"
"s_or_b64 vcc, vcc, s[92:93]\n"
"s_cbranch_vccnz RT11_%=\n"
"BK12_%=:\n"
"ds_write2_b32 v13, v16, v18 offset0:128 offset1:129\n"
"s_waitcnt lgkmcnt(0)\n"
"s_barrier\n"
"ds_read_b128 v[16:19], v3 offset:512\n"
"ds_read_b128 v[20:23], v3 offset:1536\n"
"s_add_u32 m0, s84, 65536\n"
"s_add_u32 s86, s88, 4\n"
"s_and_b32 s86, s86, 7\n"
"s_mul_i32 s86, s86, 196608\n"
"s_add_u32 s86, s86, s83\n"
"buffer_load_dwordx4 v4, s[12:15], s86 offen offset:0 lds\n"
"buffer_load_dwordx4 v4, s[12:15], s86 offen offset:1024 lds\n"
"s_waitcnt lgkmcnt(0)\n"
"s_barrier\n"
"v_lshlrev_b32 v112, 16, v16\n"
"v_and_b32 v113, 0xffff0000, v16\n"
"v_lshlrev_b32 v114, 16, v17\n"
"v_and_b32 v115, 0xffff0000, v17\n"
"v_lshlrev_b32 v116, 16, v18\n"
"v_and_b32 v117, 0xffff0000, v18\n"
"v_lshlrev_b32 v118, 16, v19\n"
"v_and_b32 v119, 0xffff0000, v19\n"
"v_lshlrev_b32 v120, 16, v20\n"
"v_and_b32 v121, 0xffff0000, v20\n"
"v_lshlrev_b32 v122, 16, v21\n"
"v_and_b32 v123, 0xffff0000, v21\n"
"v_lshlrev_b32 v124, 16, v22\n"
"v_and_b32 v125, 0xffff0000, v22\n"
"v_lshlrev_b32 v126, 16, v23\n"
"v_and_b32 v127, 0xffff0000, v23\n"
"v_pk_fma_f32 v[48:49], v[48:49], v[120:121], v[112:113]\n"
"v_pk_fma_f32 v[50:51], v[50:51], v[122:123], v[114:115]\n"
"v_pk_fma_f32 v[52:53], v[52:53], v[124:125], v[116:117]\n"
"v_pk_fma_f32 v[54:55], v[54:55], v[126:127], v[118:119]\n"
"v_cvt_pk_bf16_f32 v48, v48, v49\n"
"v_cvt_pk_bf16_f32 v49, v50, v51\n"
"v_cvt_pk_bf16_f32 v50, v52, v53\n"
"v_cvt_pk_bf16_f32 v51, v54, v55\n"
"ds_write_b128 v8, v[48:51] offset:0\n"
"v_pk_fma_f32 v[56:57], v[56:57], v[120:121], v[112:113]\n"
"v_pk_fma_f32 v[58:59], v[58:59], v[122:123], v[114:115]\n"
"v_pk_fma_f32 v[60:61], v[60:61], v[124:125], v[116:117]\n"
"v_pk_fma_f32 v[62:63], v[62:63], v[126:127], v[118:119]\n"
"v_xor_b32 v8, 16, v8\n"
"v_cvt_pk_bf16_f32 v56, v56, v57\n"
"v_cvt_pk_bf16_f32 v57, v58, v59\n"
"v_cvt_pk_bf16_f32 v58, v60, v61\n"
"v_cvt_pk_bf16_f32 v59, v62, v63\n"
"buffer_load_dwordx4 v4, s[12:15], s86 offen offset:2048 lds\n"
"ds_write_b128 v8, v[56:59] offset:0\n"
"v_pk_fma_f32 v[64:65], v[64:65], v[120:121], v[112:113]\n"
"v_pk_fma_f32 v[66:67], v[66:67], v[122:123], v[114:115]\n"
"v_pk_fma_f32 v[68:69], v[68:69], v[124:125], v[116:117]\n"
"v_pk_fma_f32 v[70:71], v[70:71], v[126:127], v[118:119]\n"
"v_xor_b32 v8, 48, v8\n"
"v_cvt_pk_bf16_f32 v64, v64, v65\n"
"v_cvt_pk_bf16_f32 v65, v66, v67\n"
"v_cvt_pk_bf16_f32 v66, v68, v69\n"
"v_cvt_pk_bf16_f32 v67, v70, v71\n"
"ds_write_b128 v8, v[64:67] offset:0\n"
"v_pk_fma_f32 v[72:73], v[72:73], v[120:121], v[112:113]\n"
"v_pk_fma_f32 v[74:75], v[74:75], v[122:123], v[114:115]\n"
"v_pk_fma_f32 v[76:77], v[76:77], v[124:125], v[116:117]\n"
"v_pk_fma_f32 v[78:79], v[78:79], v[126:127], v[118:119]\n"
"v_xor_b32 v8, 16, v8\n"
"v_cvt_pk_bf16_f32 v72, v72, v73\n"
"s_add_u32 m0, s84, 90112\n"
"s_add_u32 s86, s88, 5\n"
"s_and_b32 s86, s86, 7\n"
"s_mul_i32 s86, s86, 196608\n"
"s_add_u32 s86, s86, s83\n"
"buffer_load_dwordx4 v4, s[12:15], s86 offen offset:0 lds\n"
"v_cvt_pk_bf16_f32 v73, v74, v75\n"
"v_cvt_pk_bf16_f32 v74, v76, v77\n"
"v_cvt_pk_bf16_f32 v75, v78, v79\n"
"ds_write_b128 v8, v[72:75] offset:0\n"
"v_pk_fma_f32 v[80:81], v[80:81], v[120:121], v[112:113]\n"
"v_pk_fma_f32 v[82:83], v[82:83], v[122:123], v[114:115]\n"
"v_pk_fma_f32 v[84:85], v[84:85], v[124:125], v[116:117]\n"
"v_pk_fma_f32 v[86:87], v[86:87], v[126:127], v[118:119]\n"
"v_xor_b32 v8, 112, v8\n"
"v_cvt_pk_bf16_f32 v80, v80, v81\n"
"v_cvt_pk_bf16_f32 v81, v82, v83\n"
"v_cvt_pk_bf16_f32 v82, v84, v85\n"
"v_cvt_pk_bf16_f32 v83, v86, v87\n"
"ds_write_b128 v8, v[80:83] offset:0\n"
"v_pk_fma_f32 v[88:89], v[88:89], v[120:121], v[112:113]\n"
"v_pk_fma_f32 v[90:91], v[90:91], v[122:123], v[114:115]\n"
"v_pk_fma_f32 v[92:93], v[92:93], v[124:125], v[116:117]\n"
"v_pk_fma_f32 v[94:95], v[94:95], v[126:127], v[118:119]\n"
"buffer_load_dwordx4 v4, s[12:15], s86 offen offset:1024 lds\n"
"v_xor_b32 v8, 16, v8\n"
"v_cvt_pk_bf16_f32 v88, v88, v89\n"
"v_cvt_pk_bf16_f32 v89, v90, v91\n"
"v_cvt_pk_bf16_f32 v90, v92, v93\n"
"v_cvt_pk_bf16_f32 v91, v94, v95\n"
"ds_write_b128 v8, v[88:91] offset:0\n"
"v_pk_fma_f32 v[96:97], v[96:97], v[120:121], v[112:113]\n"
"v_pk_fma_f32 v[98:99], v[98:99], v[122:123], v[114:115]\n"
"v_pk_fma_f32 v[100:101], v[100:101], v[124:125], v[116:117]\n"
"v_pk_fma_f32 v[102:103], v[102:103], v[126:127], v[118:119]\n"
"v_xor_b32 v8, 48, v8\n"
"v_cvt_pk_bf16_f32 v96, v96, v97\n"
"v_cvt_pk_bf16_f32 v97, v98, v99\n"
"v_cvt_pk_bf16_f32 v98, v100, v101\n"
"v_cvt_pk_bf16_f32 v99, v102, v103\n"
"ds_write_b128 v8, v[96:99] offset:0\n"
"v_pk_fma_f32 v[104:105], v[104:105], v[120:121], v[112:113]\n"
"buffer_load_dwordx4 v4, s[12:15], s86 offen offset:2048 lds\n"
"v_pk_fma_f32 v[106:107], v[106:107], v[122:123], v[114:115]\n"
"v_pk_fma_f32 v[108:109], v[108:109], v[124:125], v[116:117]\n"
"v_pk_fma_f32 v[110:111], v[110:111], v[126:127], v[118:119]\n"
"v_xor_b32 v8, 16, v8\n"
"v_cvt_pk_bf16_f32 v104, v104, v105\n"
"v_cvt_pk_bf16_f32 v105, v106, v107\n"
"v_cvt_pk_bf16_f32 v106, v108, v109\n"
"v_cvt_pk_bf16_f32 v107, v110, v111\n"
"ds_write_b128 v8, v[104:107] offset:0\n"
"s_waitcnt lgkmcnt(0)\n"
"s_barrier\n"
"ds_read_b128 v[40:43], v9 offset:0\n"
"ds_read_b128 v[44:47], v9 offset:1024\n"
"ds_read_b128 v[48:51], v10 offset:4096\n"
"ds_read_b128 v[52:55], v10 offset:5120\n"
"s_waitcnt lgkmcnt(3)\n"
"v_mfma_f32_16x16x32_bf16 v[20:23], v[132:135], v[40:43], 0\n"
"v_mfma_f32_16x16x32_bf16 v[24:27], v[40:43], v[136:139], 0\n"
"ds_read_b128 v[56:59], v9 offset:8192\n"
"v_mfma_f32_16x16x32_bf16 v[16:19], v[128:131], v[40:43], 0\n"
"ds_read_b128 v[60:63], v9 offset:9216\n"
"s_waitcnt lgkmcnt(4)\n"
"v_mfma_f32_16x16x32_bf16 v[32:35], v[132:135], v[44:47], 0\n"
"s_add_u32 m0, s84, 114688\n"
"s_add_u32 s86, s88, 6\n"
"s_and_b32 s86, s86, 7\n"
"s_mul_i32 s86, s86, 196608\n"
"s_add_u32 s86, s86, s83\n"
"buffer_load_dwordx4 v4, s[12:15], s86 offen offset:0 lds\n"
"v_mfma_f32_16x16x32_bf16 v[36:39], v[44:47], v[136:139], 0\n"
"buffer_load_dwordx4 v4, s[12:15], s86 offen offset:1024 lds\n"
"v_mfma_f32_16x16x32_bf16 v[28:31], v[128:131], v[44:47], 0\n"
"buffer_load_dwordx4 v4, s[12:15], s86 offen offset:2048 lds\n"
"s_waitcnt lgkmcnt(3)\n"
"v_mfma_f32_16x16x32_bf16 v[20:23], v[144:147], v[48:51], v[20:23]\n"
"v_mfma_f32_16x16x32_bf16 v[24:27], v[48:51], v[148:151], v[24:27]\n"
"ds_read_b128 v[40:43], v10 offset:12288\n"
"v_mfma_f32_16x16x32_bf16 v[16:19], v[140:143], v[48:51], v[16:19]\n"
"s_waitcnt lgkmcnt(3)\n"
"v_mfma_f32_16x16x32_bf16 v[32:35], v[144:147], v[52:55], v[32:35]\n"
"ds_read_b128 v[44:47], v10 offset:13312\n"
"v_mfma_f32_16x16x32_bf16 v[36:39], v[52:55], v[148:151], v[36:39]\n"
"v_mfma_f32_16x16x32_bf16 v[28:31], v[140:143], v[52:55], v[28:31]\n"
"s_lshl_b32 s23, s76, 12\n"
"s_add_u32 s23, s23, 196608\n"
"s_lshl_b32 s90, s75, 7\n"
"s_add_u32 s90, s90, s23\n"
"s_cmp_eq_u32 s74, 4\n"
"s_cselect_b32 s90, s90, 2146435072\n"
"v_and_b32 v8, 31, v1\n"
"v_lshlrev_b32 v8, 3, v8\n"
"s_mov_b64 exec, 0xffff\n"
"buffer_store_dwordx2 v[14:15], v8, s[32:35], s90 offen sc1\n"
"s_mov_b64 exec, -1\n"
"s_waitcnt lgkmcnt(3)\n"
"v_mfma_f32_16x16x32_bf16 v[20:23], v[156:159], v[56:59], v[20:23]\n"
"ds_read_b128 v[48:51], v9 offset:16384\n"
"v_mfma_f32_16x16x32_bf16 v[24:27], v[56:59], v[160:163], v[24:27]\n"
"ds_read_b128 v[52:55], v9 offset:17408\n"
"ds_read_b64_tr_b16 v[128:129], v6 offset:24576\n"
"v_mfma_f32_16x16x32_bf16 v[16:19], v[152:155], v[56:59], v[16:19]\n"
"ds_read_b64_tr_b16 v[130:131], v6 offset:25088\n"
"s_waitcnt lgkmcnt(6)\n"
"v_mfma_f32_16x16x32_bf16 v[32:35], v[156:159], v[60:63], v[32:35]\n"
"ds_read_b64_tr_b16 v[132:133], v6 offset:25600\n"
"ds_read_b64_tr_b16 v[134:135], v6 offset:26112\n"
"v_mfma_f32_16x16x32_bf16 v[36:39], v[60:63], v[160:163], v[36:39]\n"
"ds_read_b64_tr_b16 v[136:137], v6 offset:26624\n"
"v_mfma_f32_16x16x32_bf16 v[28:31], v[152:155], v[60:63], v[28:31]\n"
"ds_read_b64_tr_b16 v[138:139], v6 offset:27136\n"
"s_cmp_ge_u32 s79, 800\n"
"s_cbranch_scc1 L15_%=\n"
"v_lshrrev_b32 v96, 3, v1\n"
"v_mul_u32_u24 v96, s16, v96\n"
"v_and_b32 v97, 0x70, v2\n"
"v_add_u32 v96, v96, v97\n"
"s_lshl_b32 s16, s16, 3\n"
"buffer_load_dwordx4 v[68:71], v96, s[40:43], s0 offen\n"
"s_add_u32 s0, s0, s16\n"
"buffer_load_dwordx4 v[84:87], v96, s[40:43], s0 offen\n"
"buffer_load_dwordx4 v[88:91], v96, s[40:43], s1 offen\n"
"s_add_u32 s1, s1, s16\n"
"buffer_load_dwordx4 v[92:95], v96, s[40:43], s1 offen\n"
"v_and_b32 v97, 1, v1\n"
"v_lshlrev_b32 v97, 11, v97\n"
"v_and_or_b32 v97, v1, 48, v97\n"
"buffer_load_dwordx4 v[200:203], v97, s[32:35], s67 offen offset:0 sc1\n"
"buffer_load_dwordx4 v[204:207], v97, s[32:35], s67 offen offset:1024 sc1\n"
"L15_%=:\n"
"s_waitcnt lgkmcnt(9)\n"
"v_mfma_f32_16x16x32_bf16 v[20:23], v[168:171], v[40:43], v[20:23]\n"
"ds_read_b128 v[56:59], v10 offset:20480\n"
"v_mfma_f32_16x16x32_bf16 v[24:27], v[40:43], v[172:175], v[24:27]\n"
"ds_read_b128 v[60:63], v10 offset:21504\n"
"v_mfma_f32_16x16x32_bf16 v[16:19], v[164:167], v[40:43], v[16:19]\n"
"ds_read_b64_tr_b16 v[140:141], v6 offset:36864\n"
"ds_read_b64_tr_b16 v[142:143], v6 offset:37376\n"
"s_waitcnt lgkmcnt(12)\n"
"v_mfma_f32_16x16x32_bf16 v[32:35], v[168:171], v[44:47], v[32:35]\n"
"ds_read_b64_tr_b16 v[144:145], v6 offset:37888\n"
"v_mfma_f32_16x16x32_bf16 v[36:39], v[44:47], v[172:175], v[36:39]\n"
"ds_read_b64_tr_b16 v[146:147], v6 offset:38400\n"
"v_mfma_f32_16x16x32_bf16 v[28:31], v[164:167], v[44:47], v[28:31]\n"
"ds_read_b64_tr_b16 v[148:149], v6 offset:38912\n"
"ds_read_b64_tr_b16 v[150:151], v6 offset:39424\n"
"s_cmp_ge_u32 s79, 800\n"
"s_cbranch_scc1 L16_%=\n"
"s_waitcnt vmcnt(7)\n"
"s_branch L17_%=\n"
"L16_%=:\n"
"s_waitcnt vmcnt(1)\n"
"L17_%=:\n"
"s_waitcnt lgkmcnt(0)\n"
"s_barrier\n"
"v_mfma_f32_16x16x32_bf16 v[20:23], v[180:183], v[48:51], v[20:23]\n"
"ds_read_b128 v[40:43], v9 offset:24576\n"
"v_mfma_f32_16x16x32_bf16 v[24:27], v[48:51], v[184:187], v[24:27]\n"
"ds_read_b128 v[44:47], v9 offset:25600\n"
"ds_read_b64_tr_b16 v[152:153], v5 offset:0\n"
"v_mfma_f32_16x16x32_bf16 v[16:19], v[176:179], v[48:51], v[16:19]\n"
"ds_read_b64_tr_b16 v[154:155], v5 offset:512\n"
"ds_read_b64_tr_b16 v[156:157], v5 offset:1024\n"
"v_mfma_f32_16x16x32_bf16 v[32:35], v[180:183], v[52:55], v[32:35]\n"
"ds_read_b64_tr_b16 v[158:159], v5 offset:1536\n"
"v_mfma_f32_16x16x32_bf16 v[36:39], v[52:55], v[184:187], v[36:39]\n"
"ds_read_b64_tr_b16 v[160:161], v5 offset:2048\n"
"ds_read_b64_tr_b16 v[162:163], v5 offset:2560\n"
"v_mfma_f32_16x16x32_bf16 v[28:31], v[176:179], v[52:55], v[28:31]\n"
"s_mov_b64 s[28:29], s[96:97]\n"
"s_mov_b32 s30, 4096\n"
"s_mov_b32 s31, 0x20000\n"
"s_lshl_b32 s90, s98, 8\n"
"s_add_u32 s91, s90, 32768\n"
"s_cmp_eq_u32 s74, 0\n"
"s_cselect_b32 s91, s91, 2146435072\n"
"s_cselect_b32 s90, s90, 2146435072\n"
"s_mov_b64 exec, 0x101\n"
"buffer_load_dword v222, v2, s[32:35], s91 offen sc1\n"
"buffer_load_dword v222, v2, s[28:31], s90 offen\n"
"s_mov_b64 exec, -1\n"
"s_lshl_b32 s91, s75, 3\n"
"s_add_u32 s91, s91, s76\n"
"s_lshl_b32 s91, s91, 3\n"
"s_add_u32 s91, s91, s74\n"
"s_lshl_b32 s91, s91, 12\n"
"v_lshlrev_b32 v11, 7, v1\n"
"s_mov_b32 exec_hi, 0\n"
"buffer_load_dword v11, v11, s[24:27], s91 offen\n"
"s_mov_b64 exec, -1\n"
"v_mfma_f32_16x16x32_bf16 v[20:23], v[192:195], v[56:59], v[20:23]\n"
"ds_read_b128 v[48:51], v10 offset:28672\n"
"v_mfma_f32_16x16x32_bf16 v[24:27], v[56:59], v[196:199], v[24:27]\n"
"ds_read_b128 v[52:55], v10 offset:29696\n"
"ds_write_b128 v7, v[208:211] offset:0\n"
"v_mfma_f32_16x16x32_bf16 v[16:19], v[188:191], v[56:59], v[16:19]\n"
"ds_write_b128 v7, v[212:215] offset:1024\n"
"ds_write_b128 v7, v[216:219] offset:2048\n"
"v_mfma_f32_16x16x32_bf16 v[32:35], v[192:195], v[60:63], v[32:35]\n"
"ds_read_b64_tr_b16 v[164:165], v5 offset:12288\n"
"ds_read_b64_tr_b16 v[166:167], v5 offset:12800\n"
"v_mfma_f32_16x16x32_bf16 v[36:39], v[60:63], v[196:199], v[36:39]\n"
"ds_read_b64_tr_b16 v[168:169], v5 offset:13312\n"
"ds_read_b64_tr_b16 v[170:171], v5 offset:13824\n"
"v_mfma_f32_16x16x32_bf16 v[28:31], v[188:191], v[60:63], v[28:31]\n"
"ds_read_b64_tr_b16 v[172:173], v5 offset:14336\n"
"ds_read_b64_tr_b16 v[174:175], v5 offset:14848\n"
"s_waitcnt lgkmcnt(15)\n"
"v_mfma_f32_16x16x32_bf16 v[20:23], v[132:135], v[40:43], v[20:23]\n"
"ds_read_b128 v[56:59], v9 offset:32768\n"
"v_mfma_f32_16x16x32_bf16 v[24:27], v[40:43], v[136:139], v[24:27]\n"
"ds_read_b128 v[60:63], v9 offset:33792\n"
"v_mfma_f32_16x16x32_bf16 v[16:19], v[128:131], v[40:43], v[16:19]\n"
"ds_read_b64_tr_b16 v[176:177], v5 offset:24576\n"
"ds_read_b64_tr_b16 v[178:179], v5 offset:25088\n"
"s_waitcnt lgkmcnt(15)\n"
"v_mfma_f32_16x16x32_bf16 v[32:35], v[132:135], v[44:47], v[32:35]\n"
"ds_read_b64_tr_b16 v[180:181], v5 offset:25600\n"
"v_mfma_f32_16x16x32_bf16 v[36:39], v[44:47], v[136:139], v[36:39]\n"
"ds_read_b64_tr_b16 v[182:183], v5 offset:26112\n"
"v_mfma_f32_16x16x32_bf16 v[28:31], v[128:131], v[44:47], v[28:31]\n"
"ds_read_b64_tr_b16 v[184:185], v5 offset:26624\n"
"ds_read_b64_tr_b16 v[186:187], v5 offset:27136\n"
"s_waitcnt lgkmcnt(15)\n"
"v_mfma_f32_16x16x32_bf16 v[20:23], v[144:147], v[48:51], v[20:23]\n"
"ds_read_b128 v[40:43], v10 offset:36864\n"
"v_mfma_f32_16x16x32_bf16 v[24:27], v[48:51], v[148:151], v[24:27]\n"
"ds_read_b128 v[44:47], v10 offset:37888\n"
"v_mfma_f32_16x16x32_bf16 v[16:19], v[140:143], v[48:51], v[16:19]\n"
"ds_read_b64_tr_b16 v[188:189], v5 offset:36864\n"
"ds_read_b64_tr_b16 v[190:191], v5 offset:37376\n"
"s_waitcnt lgkmcnt(15)\n"
"v_mfma_f32_16x16x32_bf16 v[32:35], v[144:147], v[52:55], v[32:35]\n"
"ds_read_b64_tr_b16 v[192:193], v5 offset:37888\n"
"v_mfma_f32_16x16x32_bf16 v[36:39], v[52:55], v[148:151], v[36:39]\n"
"ds_read_b64_tr_b16 v[194:195], v5 offset:38400\n"
"v_mfma_f32_16x16x32_bf16 v[28:31], v[140:143], v[52:55], v[28:31]\n"
"ds_read_b64_tr_b16 v[196:197], v5 offset:38912\n"
"ds_read_b64_tr_b16 v[198:199], v5 offset:39424\n"
"s_waitcnt lgkmcnt(15)\n"
"v_mfma_f32_16x16x32_bf16 v[20:23], v[156:159], v[56:59], v[20:23]\n"
"ds_read_b128 v[48:51], v9 offset:40960\n"
"ds_read_b128 v[52:55], v9 offset:41984\n"
"v_mfma_f32_16x16x32_bf16 v[24:27], v[56:59], v[160:163], v[24:27]\n"
"ds_read_b64_tr_b16 v[128:129], v6 offset:0\n"
"ds_read_b64_tr_b16 v[130:131], v6 offset:512\n"
"ds_read_b64_tr_b16 v[132:133], v6 offset:1024\n"
"v_mfma_f32_16x16x32_bf16 v[16:19], v[152:155], v[56:59], v[16:19]\n"
"ds_read_b64_tr_b16 v[134:135], v6 offset:1536\n"
"ds_read_b64_tr_b16 v[136:137], v6 offset:2048\n"
"s_waitcnt lgkmcnt(15)\n"
"v_mfma_f32_16x16x32_bf16 v[32:35], v[156:159], v[60:63], v[32:35]\n"
"ds_read_b64_tr_b16 v[138:139], v6 offset:2560\n"
"v_and_b32 v220, 11, v1\n"
"v_mov_b32 v221, 0x3f803f80\n"
"v_mfma_f32_16x16x32_bf16 v[36:39], v[60:63], v[160:163], v[36:39]\n"
"v_cmp_eq_u32 vcc, 0, v220\n"
"s_nop 1\n"
"v_cndmask_b32 v80, 0, v221, vcc\n"
"v_mfma_f32_16x16x32_bf16 v[28:31], v[152:155], v[60:63], v[28:31]\n"
"v_cndmask_b32 v81, 0, v221, vcc\n"
"v_mov_b64 v[82:83], v[80:81]\n"
"v_mov_b32 v0, 0xdddddddd\n"
"s_barrier\n"
"s_waitcnt lgkmcnt(15)\n"
"v_mfma_f32_16x16x32_bf16 v[20:23], v[168:171], v[40:43], v[20:23]\n"
"ds_read_b128 v[56:59], v10 offset:45056\n"
"v_mfma_f32_16x16x32_bf16 v[24:27], v[40:43], v[172:175], v[24:27]\n"
"ds_read_b128 v[60:63], v10 offset:46080\n"
"ds_read_b64_tr_b16 v[140:141], v6 offset:12288\n"
"v_mfma_f32_16x16x32_bf16 v[16:19], v[164:167], v[40:43], v[16:19]\n"
"ds_read_b64_tr_b16 v[142:143], v6 offset:12800\n"
"s_waitcnt lgkmcnt(15)\n"
"v_mfma_f32_16x16x32_bf16 v[32:35], v[168:171], v[44:47], v[32:35]\n"
"ds_read_b64_tr_b16 v[144:145], v6 offset:13312\n"
"ds_read_b64_tr_b16 v[146:147], v6 offset:13824\n"
"v_mfma_f32_16x16x32_bf16 v[36:39], v[44:47], v[172:175], v[36:39]\n"
"ds_read_b64_tr_b16 v[148:149], v6 offset:14336\n"
"v_mfma_f32_16x16x32_bf16 v[28:31], v[164:167], v[44:47], v[28:31]\n"
"ds_read_b64_tr_b16 v[150:151], v6 offset:14848\n"
"v_mov_b64 v[248:249], -1\n"
"v_mov_b64 v[250:251], -1\n"
"s_lshl_b32 s3, s98, 3\n"
"s_add_u32 s3, s3, s74\n"
"s_mul_i32 s2, s3, 5888\n"
"s_add_u32 s2, s2, 5242880\n"
"s_lshl_b32 s3, s3, 3\n"
"s_add_u32 s3, s3, 49152\n"
"s_waitcnt lgkmcnt(15)\n"
"v_mfma_f32_16x16x32_bf16 v[20:23], v[180:183], v[48:51], v[20:23]\n"
"ds_read_b128 v[40:43], v9 offset:49152\n"
"v_mfma_f32_16x16x32_bf16 v[24:27], v[48:51], v[184:187], v[24:27]\n"
"ds_read_b128 v[44:47], v9 offset:50176\n"
"v_mfma_f32_16x16x32_bf16 v[16:19], v[176:179], v[48:51], v[16:19]\n"
"ds_read_b64_tr_b16 v[152:153], v6 offset:24576\n"
"ds_read_b64_tr_b16 v[154:155], v6 offset:25088\n"
"s_waitcnt lgkmcnt(15)\n"
"v_mfma_f32_16x16x32_bf16 v[32:35], v[180:183], v[52:55], v[32:35]\n"
"ds_read_b64_tr_b16 v[156:157], v6 offset:25600\n"
"v_mfma_f32_16x16x32_bf16 v[36:39], v[52:55], v[184:187], v[36:39]\n"
"ds_read_b64_tr_b16 v[158:159], v6 offset:26112\n"
"v_mfma_f32_16x16x32_bf16 v[28:31], v[176:179], v[52:55], v[28:31]\n"
"ds_read_b64_tr_b16 v[160:161], v6 offset:26624\n"
"ds_read_b64_tr_b16 v[162:163], v6 offset:27136\n"
"s_waitcnt lgkmcnt(15)\n"
"v_mfma_f32_16x16x32_bf16 v[20:23], v[192:195], v[56:59], v[20:23]\n"
"ds_read_b128 v[48:51], v10 offset:53248\n"
"v_mfma_f32_16x16x32_bf16 v[24:27], v[56:59], v[196:199], v[24:27]\n"
"ds_read_b128 v[52:55], v10 offset:54272\n"
"v_mfma_f32_16x16x32_bf16 v[16:19], v[188:191], v[56:59], v[16:19]\n"
"ds_read_b64_tr_b16 v[164:165], v6 offset:36864\n"
"ds_read_b64_tr_b16 v[166:167], v6 offset:37376\n"
"s_waitcnt lgkmcnt(15)\n"
"v_mfma_f32_16x16x32_bf16 v[32:35], v[192:195], v[60:63], v[32:35]\n"
"ds_read_b64_tr_b16 v[168:169], v6 offset:37888\n"
"v_mfma_f32_16x16x32_bf16 v[36:39], v[60:63], v[196:199], v[36:39]\n"
"ds_read_b64_tr_b16 v[170:171], v6 offset:38400\n"
"v_mfma_f32_16x16x32_bf16 v[28:31], v[188:191], v[60:63], v[28:31]\n"
"ds_read_b64_tr_b16 v[172:173], v6 offset:38912\n"
"ds_read_b64_tr_b16 v[174:175], v6 offset:39424\n"
"s_waitcnt lgkmcnt(15)\n"
"v_mfma_f32_16x16x32_bf16 v[20:23], v[132:135], v[40:43], v[20:23]\n"
"ds_read_b128 v[56:59], v9 offset:57344\n"
"v_mfma_f32_16x16x32_bf16 v[24:27], v[40:43], v[136:139], v[24:27]\n"
"ds_read_b128 v[60:63], v9 offset:58368\n"
"v_mov_b64 v[228:229], v[80:81]\n"
"v_mfma_f32_16x16x32_bf16 v[16:19], v[128:131], v[40:43], v[16:19]\n"
"v_mov_b64 v[230:231], v[80:81]\n"
"v_mov_b64 v[232:233], v[80:81]\n"
"s_waitcnt lgkmcnt(15)\n"
"v_mfma_f32_16x16x32_bf16 v[32:35], v[132:135], v[44:47], v[32:35]\n"
"v_mov_b64 v[234:235], v[80:81]\n"
"v_mfma_f32_16x16x32_bf16 v[36:39], v[44:47], v[136:139], v[36:39]\n"
"v_mov_b64 v[240:241], v[80:81]\n"
"v_mov_b64 v[242:243], v[80:81]\n"
"v_mfma_f32_16x16x32_bf16 v[28:31], v[128:131], v[44:47], v[28:31]\n"
"v_mov_b64 v[244:245], v[80:81]\n"
"v_mov_b64 v[246:247], v[80:81]\n"
"s_waitcnt lgkmcnt(2)\n"
"s_barrier\n"
"v_mfma_f32_16x16x32_bf16 v[20:23], v[144:147], v[48:51], v[20:23]\n"
"ds_read_b128 v[40:43], v10 offset:61440\n"
"v_mfma_f32_16x16x32_bf16 v[24:27], v[48:51], v[148:151], v[24:27]\n"
"ds_read_b128 v[44:47], v10 offset:62464\n"
"v_mov_b64 v[96:97], 0\n"
"v_mfma_f32_16x16x32_bf16 v[16:19], v[140:143], v[48:51], v[16:19]\n"
"v_mov_b64 v[98:99], 0\n"
"v_mov_b64 v[100:101], 0\n"
"v_mfma_f32_16x16x32_bf16 v[32:35], v[144:147], v[52:55], v[32:35]\n"
"v_mov_b64 v[102:103], 0\n"
"v_mfma_f32_16x16x32_bf16 v[36:39], v[52:55], v[148:151], v[36:39]\n"
"v_mov_b64 v[104:105], 0\n"
"v_mov_b64 v[106:107], 0\n"
"v_mfma_f32_16x16x32_bf16 v[28:31], v[140:143], v[52:55], v[28:31]\n"
"v_mov_b64 v[108:109], 0\n"
"v_mov_b64 v[110:111], 0\n"
"s_waitcnt lgkmcnt(3)\n"
"v_mfma_f32_16x16x32_bf16 v[20:23], v[156:159], v[56:59], v[20:23]\n"
"v_mov_b64 v[112:113], 0\n"
"v_mfma_f32_16x16x32_bf16 v[24:27], v[56:59], v[160:163], v[24:27]\n"
"v_mov_b64 v[114:115], 0\n"
"v_mfma_f32_16x16x32_bf16 v[16:19], v[152:155], v[56:59], v[16:19]\n"
"v_mov_b64 v[116:117], 0\n"
"v_mov_b64 v[118:119], 0\n"
"s_waitcnt lgkmcnt(2)\n"
"v_mfma_f32_16x16x32_bf16 v[32:35], v[156:159], v[60:63], v[32:35]\n"
"v_mov_b64 v[120:121], 0\n"
"v_mfma_f32_16x16x32_bf16 v[36:39], v[60:63], v[160:163], v[36:39]\n"
"v_mov_b64 v[122:123], 0\n"
"v_mfma_f32_16x16x32_bf16 v[28:31], v[152:155], v[60:63], v[28:31]\n"
"v_mov_b64 v[124:125], 0\n"
"v_mov_b64 v[126:127], 0\n"
"s_waitcnt lgkmcnt(1)\n"
"v_mfma_f32_16x16x32_bf16 v[20:23], v[168:171], v[40:43], v[20:23]\n"
"v_mfma_f32_16x16x32_bf16 v[24:27], v[40:43], v[172:175], v[24:27]\n"
"v_add_u32 v10, s75, v1\n"
"v_and_b32 v10, 31, v10\n"
"v_lshlrev_b32 v10, 7, v10\n"
"v_mov_b64 v[12:13], v[14:15]\n"
"v_mfma_f32_16x16x32_bf16 v[16:19], v[164:167], v[40:43], v[16:19]\n"
"s_lshl_b32 s90, s74, 10\n"
"s_add_u32 s90, s90, 133120\n"
"v_add_u32 v3, s90, v2\n"
"s_waitcnt lgkmcnt(0)\n"
"v_mfma_f32_16x16x32_bf16 v[32:35], v[168:171], v[44:47], v[32:35]\n"
"v_mfma_f32_16x16x32_bf16 v[36:39], v[44:47], v[172:175], v[36:39]\n"
"s_xor_b32 s90, s74, 4\n"
"s_lshl_b32 s90, s90, 10\n"
"s_add_u32 s90, s90, 133120\n"
"v_add_u32 v4, s90, v2\n"
"v_mfma_f32_16x16x32_bf16 v[28:31], v[164:167], v[44:47], v[28:31]\n"
"s_mul_i32 s87, s74, 16640\n"
"v_add_u32 v7, s87, v2\n"
"s_mov_b32 s92, 0x3eb8aa3b\n"
"v_cvt_pk_bf16_f32 v72, v20, v21\n"
"v_cvt_pk_bf16_f32 v73, v22, v23\n"
"v_cvt_pk_bf16_f32 v74, v32, v33\n"
"v_cvt_pk_bf16_f32 v75, v34, v35\n"
"s_nop 0\n"
"v_permlane16_swap_b32 v72, v74\n"
"v_permlane16_swap_b32 v73, v75\n"
"buffer_store_dwordx4 v[72:75], v2, s[32:35], s99 offen offset:0 sc1\n"
"v_cvt_pk_bf16_f32 v76, v24, v25\n"
"v_cvt_pk_bf16_f32 v77, v26, v27\n"
"v_cvt_pk_bf16_f32 v78, v36, v37\n"
"v_cvt_pk_bf16_f32 v79, v38, v39\n"
"s_nop 0\n"
"v_permlane16_swap_b32 v76, v78\n"
"v_permlane16_swap_b32 v77, v79\n"
"buffer_store_dwordx4 v[76:79], v2, s[32:35], s99 offen offset:1024 sc1\n"
"v_pk_mul_f32 v[16:17], v[16:17], s[92:93] op_sel_hi:[1,0]\n"
"v_pk_mul_f32 v[18:19], v[18:19], s[92:93] op_sel_hi:[1,0]\n"
"v_cvt_pk_bf16_f32 v64, v16, v17\n"
"v_cvt_pk_bf16_f32 v65, v18, v19\n"
"v_pk_mul_f32 v[28:29], v[28:29], s[92:93] op_sel_hi:[1,0]\n"
"v_pk_mul_f32 v[30:31], v[30:31], s[92:93] op_sel_hi:[1,0]\n"
"v_cvt_pk_bf16_f32 v66, v28, v29\n"
"v_cvt_pk_bf16_f32 v67, v30, v31\n"
"s_nop 0\n"
"v_permlane16_swap_b32 v64, v66\n"
"v_permlane16_swap_b32 v65, v67\n"
"ds_write_b128 v3, v[64:67]\n"
"s_nop 0\n"
"v_mfma_f32_32x32x16_bf16 v[16:31], v[72:75], v[64:67], 0\n"
"v_permlane16_swap_b32 v76, v80\n"
"v_permlane16_swap_b32 v77, v81\n"
"v_permlane16_swap_b32 v78, v82\n"
"v_permlane16_swap_b32 v79, v83\n"
"s_waitcnt lgkmcnt(0)\n"
"s_barrier\n"
"s_setprio 3\n"
"s_cmp_ge_u32 s79, 800\n"
"s_cbranch_scc1 L18_%=\n"
"s_waitcnt vmcnt(5)\n"
"ds_write_b128 v7, v[68:71] offset:0\n"
"ds_write_b128 v7, v[84:87] offset:1024\n"
"ds_write_b128 v7, v[88:91] offset:2048\n"
"ds_write_b128 v7, v[92:95] offset:3072\n"
"s_waitcnt lgkmcnt(0)\n"
"s_nop 3\n"
"L18_%=:\n"
"ds_read_b128 v[68:71], v4\n"
"s_nop 1\n"
"v_exp_f32 v16, v16\n"
"v_exp_f32 v17, v17\n"
"v_exp_f32 v18, v18\n"
"v_exp_f32 v19, v19\n"
"v_exp_f32 v20, v20\n"
"s_waitcnt lgkmcnt(0)\n"
"v_mfma_f32_32x32x16_bf16 v[32:47], v[72:75], v[68:71], 0\n"
"v_exp_f32 v21, v21\n"
"v_exp_f32 v22, v22\n"
"v_exp_f32 v23, v23\n"
"v_exp_f32 v24, v24\n"
"v_exp_f32 v25, v25\n"
"v_smfmac_f32_32x32x32_bf16 v[96:111], v[76:79], v[16:23], v0\n"
"v_exp_f32 v26, v26\n"
"v_exp_f32 v27, v27\n"
"v_exp_f32 v28, v28\n"
"v_exp_f32 v29, v29\n"
"v_exp_f32 v30, v30\n"
"v_exp_f32 v31, v31\n"
"s_mov_b64 exec, 0x3fe\n"
"buffer_load_dwordx2 v[12:13], v10, s[32:35], s23 offen sc1\n"
"s_mov_b64 exec, -1\n"
"v_exp_f32 v32, v32\n"
"v_exp_f32 v33, v33\n"
"v_smfmac_f32_32x32x32_bf16 v[96:111], v[80:83], v[24:31], v0\n"
"v_exp_f32 v34, v34\n"
"v_exp_f32 v35, v35\n"
"v_exp_f32 v36, v36\n"
"v_exp_f32 v37, v37\n"
"v_exp_f32 v38, v38\n"
"v_exp_f32 v39, v39\n"
"v_exp_f32 v40, v40\n"
"v_exp_f32 v41, v41\n"
"v_smfmac_f32_32x32x32_bf16 v[112:127], v[76:79], v[32:39], v0\n"
"v_exp_f32 v42, v42\n"
"v_exp_f32 v43, v43\n"
"v_exp_f32 v44, v44\n"
"v_exp_f32 v45, v45\n"
"v_exp_f32 v46, v46\n"
"v_exp_f32 v47, v47\n"
"v_mov_b64 v[88:89], v[228:229]\n"
"v_mov_b64 v[90:91], v[228:229]\n"
"v_mov_b64 v[92:93], v[228:229]\n"
"v_mov_b64 v[94:95], v[228:229]\n"
"v_mov_b64 v[52:53], v[228:229]\n"
"v_mov_b64 v[54:55], v[228:229]\n"
"v_mov_b64 v[56:57], v[228:229]\n"
"v_mov_b64 v[58:59], v[228:229]\n"
"s_lshl_b32 s83, s75, 12\n"
"s_sub_u32 s89, s99, s83\n"
"v_add_u32 v4, s89, v2\n"
"s_lshl_b32 s89, s75, 3\n"
"s_add_u32 s89, s89, s69\n"
"s_waitcnt vmcnt(0)\n"
"v_cmp_ne_u64 vcc, s[72:73], v[12:13]\n"
"s_nop 0\n"
"s_cbranch_vccnz RT19_%=\n"
"BK20_%=:\n"
"s_add_u32 s83, s83, 4096\n"
"s_and_b32 s83, s83, 0x1ffff\n"
"buffer_load_dwordx4 v[84:87], v4, s[32:35], s83 offen sc1\n"
"s_mov_b64 exec, s[80:81]\n"
"buffer_load_dwordx4 v[88:91], v4, s[32:35], s83 offen offset:1024 sc1\n"
"buffer_load_dwordx4 v[92:95], v4, s[32:35], s83 offen offset:1280 sc1\n"
"s_mov_b64 exec, -1\n"
"s_add_u32 s83, s83, 4096\n"
"s_and_b32 s83, s83, 0x1ffff\n"
"buffer_load_dwordx4 v[48:51], v4, s[32:35], s83 offen sc1\n"
"s_mov_b64 exec, s[80:81]\n"
"buffer_load_dwordx4 v[52:55], v4, s[32:35], s83 offen offset:1024 sc1\n"
"buffer_load_dwordx4 v[56:59], v4, s[32:35], s83 offen offset:1280 sc1\n"
"s_mov_b64 exec, -1\n"
"s_mov_b64 exec, 1\n"
"buffer_store_dwordx2 v[14:15], v2, s[32:35], s89 offen sc1\n"
"s_mov_b64 exec, -1\n"
"s_cmp_ge_u32 s79, 800\n"
"s_cbranch_scc1 L23_%=\n"
"s_and_b32 s16, s17, 0xff\n"
"v_lshrrev_b32 v164, 5, v1\n"
"v_mul_u32_u24 v164, s16, v164\n"
"s_bfe_u32 s16, s17, 0x80008\n"
"v_bfe_u32 v165, v1, 4, 1\n"
"v_mad_u32_u24 v164, v165, s16, v164\n"
"v_bfe_u32 v165, v1, 2, 2\n"
"v_add_lshl_u32 v164, v164, v165, 7\n"
"s_bfe_u32 s16, s17, 0x80010\n"
"v_and_b32 v165, 3, v1\n"
"v_mad_u32_u24 v164, v165, s16, v164\n"
"v_add_u32 v160, s87, v164\n"
"v_add_u32 v161, s18, v160\n"
"s_lshr_b32 s16, s17, 24\n"
"v_add_u32 v162, s16, v160\n"
"v_add_u32 v163, s16, v161\n"
"ds_read_b64_tr_b16 v[128:129], v160 offset:0\n"
"ds_read_b64_tr_b16 v[130:131], v161 offset:0\n"
"ds_read_b64_tr_b16 v[132:133], v162 offset:0\n"
"ds_read_b64_tr_b16 v[134:135], v163 offset:0\n"
"ds_read_b64_tr_b16 v[136:137], v160 offset:64\n"
"ds_read_b64_tr_b16 v[138:139], v161 offset:64\n"
"ds_read_b64_tr_b16 v[140:141], v162 offset:64\n"
"ds_read_b64_tr_b16 v[142:143], v163 offset:64\n"
"v_xor_b32 v164, v201, v14\n"
"v_xor_b32 v165, v203, v15\n"
"v_bitop3_b32 v164, v205, v14, v164 bitop3:0xbe\n"
"v_bitop3_b32 v165, v207, v15, v165 bitop3:0xbe\n"
"v_cmp_ne_u64 vcc, 0, v[164:165]\n"
"s_nop 0\n"
"s_cbranch_vccnz RT24_%=\n"
"BK25_%=:\n"
"v_mov_b32 v201, v202\n"
"v_mov_b32 v202, v204\n"
"v_mov_b32 v203, v206\n"
"v_and_b32 v164, 15, v1\n"
"v_lshlrev_b32 v164, 3, v164\n"
"v_lshrrev_b32 v165, 4, v1\n"
"v_lshl_or_b32 v164, v165, 11, v164\n"
"s_waitcnt lgkmcnt(6)\n"
"v_mfma_f32_16x16x32_bf16 v[144:147], v[200:203], v[128:131], 0\n"
"s_lshl_b32 s89, s44, 3\n"
"v_writelane_b32 v9, s89, 0\n"
"s_lshl_b32 s89, s89, 7\n"
"s_add_u32 s44, s89, 17825792\n"
"s_waitcnt lgkmcnt(4)\n"
"v_mfma_f32_16x16x32_bf16 v[148:151], v[200:203], v[132:135], 0\n"
"s_lshl_b32 s89, s45, 3\n"
"v_writelane_b32 v9, s89, 1\n"
"s_lshl_b32 s89, s89, 7\n"
"s_add_u32 s45, s89, 17825792\n"
"s_waitcnt lgkmcnt(2)\n"
"v_mfma_f32_16x16x32_bf16 v[152:155], v[200:203], v[136:139], 0\n"
"s_lshl_b32 s89, s46, 3\n"
"v_writelane_b32 v9, s89, 2\n"
"s_lshl_b32 s89, s89, 7\n"
"s_add_u32 s46, s89, 17825792\n"
"s_waitcnt lgkmcnt(0)\n"
"v_mfma_f32_16x16x32_bf16 v[156:159], v[200:203], v[140:143], 0\n"
"s_lshl_b32 s89, s47, 3\n"
"v_writelane_b32 v9, s89, 3\n"
"s_lshl_b32 s89, s89, 7\n"
"s_add_u32 s47, s89, 17825792\n"
"v_mov_b32_dpp v144, v148 quad_perm:[0,1,2,3] row_mask:0x2 bank_mask:0xf\n"
"v_mov_b32_dpp v145, v149 quad_perm:[0,1,2,3] row_mask:0x2 bank_mask:0xf\n"
"s_nop 0\n"
"v_mov_b32_dpp v144, v152 quad_perm:[0,1,2,3] row_mask:0x4 bank_mask:0xf\n"
"v_mov_b32_dpp v145, v153 quad_perm:[0,1,2,3] row_mask:0x4 bank_mask:0xf\n"
"s_nop 0\n"
"v_mov_b32_dpp v144, v156 quad_perm:[0,1,2,3] row_mask:0x8 bank_mask:0xf\n"
"v_mov_b32_dpp v145, v157 quad_perm:[0,1,2,3] row_mask:0x8 bank_mask:0xf\n"
"s_nop 3\n"
"L23_%=:\n"
"v_mov_b64 v[196:197], v[228:229]\n"
"v_mov_b64 v[198:199], v[228:229]\n"
"v_mov_b64 v[200:201], v[228:229]\n"
"v_mov_b64 v[202:203], v[228:229]\n"
"v_lshrrev_b32 v62, 5, v1\n"
"v_mul_u32_u24 v62, 4736, v62\n"
"v_xor_b32 v5, v62, v2\n"
"v_bfe_u32 v62, v1, 4, 1\n"
"v_mul_u32_u24 v62, 1280, v62\n"
"v_xor_b32 v5, v62, v5\n"
"s_bfe_u32 s89, s75, 0x10003\n"
"s_lshl_b32 s89, s89, 10\n"
"v_xor_b32 v5, s89, v5\n"
"v_bfe_u32 v62, v1, 1, 3\n"
"v_mul_u32_u24 v62, 1040, v62\n"
"v_and_b32 v63, 0x30, v1\n"
"v_add3_u32 v62, v62, v63, s87\n"
"v_and_b32 v63, 1, v1\n"
"s_movk_i32 s89, 640\n"
"v_mad_u32_u24 v6, v63, s89, v62\n"
"s_movk_i32 s89, 384\n"
"v_mad_u32_u24 v7, v63, s89, v62\n"
"s_lshl_b32 s86, s75, 2\n"
"s_and_b32 s89, s74, 3\n"
"s_add_u32 s86, s86, s89\n"
"s_lshl_b32 s86, s86, 16\n"
"s_lshr_b32 s89, s75, 4\n"
"s_lshl_b32 s89, s89, 11\n"
"s_add_u32 s86, s86, s89\n"
"s_lshr_b32 s89, s74, 2\n"
"s_lshl_b32 s89, s89, 9\n"
"s_add_u32 s86, s86, s89\n"
"s_and_b32 s89, s74, 1\n"
"s_lshl_b32 s89, s89, 8\n"
"s_add_u32 s86, s86, s89\n"
"s_xor_b32 s14, s75, s76\n"
"s_and_b32 s14, s14, 0\n"
"s_xor_b32 s12, s75, s14\n"
"s_xor_b32 s13, s76, s14\n"
"s_lshr_b32 s14, s14, 2\n"
"s_xor_b32 s89, s74, 0\n"
"s_cmp_lt_u32 s74, 4\n"
"s_cselect_b32 s89, s89, s74\n"
"s_cselect_b32 s15, s13, s76\n"
"s_mul_i32 s15, s15, 25\n"
"s_add_u32 s15, s15, s89\n"
"s_lshl_b32 s88, s15, 14\n"
"s_add_u32 s88, s88, 17825792\n"
"s_waitcnt vmcnt(5)\n"
"v_max3_u32 v254, v85, v87, v89\n"
"s_waitcnt vmcnt(4)\n"
"v_max3_u32 v254, v91, v93, v254\n"
"v_max_u32 v254, v95, v254\n"
"v_cmp_eq_u32 vcc, -1, v254\n"
"s_nop 0\n"
"s_cbranch_vccnz RT28_%=\n"
"BK29_%=:\n"
"v_mfma_f32_32x32x16_bf16 v[16:31], v[84:87], v[64:67], 0\n"
"s_nop 11\n"
"v_exp_f32 v16, v16\n"
"v_exp_f32 v17, v17\n"
"v_smfmac_f32_32x32x32_bf16 v[112:127], v[80:83], v[40:47], v0\n"
"v_exp_f32 v18, v18\n"
"v_exp_f32 v19, v19\n"
"v_exp_f32 v20, v20\n"
"v_mfma_f32_32x32x16_bf16 v[32:47], v[84:87], v[68:71], 0\n"
"v_exp_f32 v21, v21\n"
"v_exp_f32 v22, v22\n"
"v_exp_f32 v23, v23\n"
"v_exp_f32 v24, v24\n"
"v_exp_f32 v25, v25\n"
"v_smfmac_f32_32x32x32_bf16 v[96:111], v[88:91], v[16:23], v0\n"
"v_exp_f32 v26, v26\n"
"s_add_u32 s83, s83, 4096\n"
"s_and_b32 s83, s83, 0x1ffff\n"
"buffer_load_dwordx4 v[192:195], v4, s[32:35], s83 offen sc1\n"
"v_exp_f32 v27, v27\n"
"v_exp_f32 v28, v28\n"
"v_exp_f32 v29, v29\n"
"v_exp_f32 v30, v30\n"
"v_exp_f32 v31, v31\n"
"v_exp_f32 v32, v32\n"
"v_exp_f32 v33, v33\n"
"s_mov_b64 exec, s[80:81]\n"
"buffer_load_dwordx4 v[196:199], v4, s[32:35], s83 offen offset:1024 sc1\n"
"buffer_load_dwordx4 v[200:203], v4, s[32:35], s83 offen offset:1280 sc1\n"
"s_mov_b64 exec, -1\n"
"v_smfmac_f32_32x32x32_bf16 v[96:111], v[92:95], v[24:31], v0\n"
"v_exp_f32 v34, v34\n"
"v_exp_f32 v35, v35\n"
"v_exp_f32 v36, v36\n"
"s_waitcnt vmcnt(5)\n"
"v_max3_u32 v254, v49, v51, v53\n"
"s_waitcnt vmcnt(4)\n"
"v_max3_u32 v254, v55, v57, v254\n"
"v_max_u32 v254, v59, v254\n"
"v_cmp_eq_u32 vcc, -1, v254\n"
"s_nop 0\n"
"s_cbranch_vccnz RT32_%=\n"
"BK33_%=:\n"
"v_mfma_f32_32x32x16_bf16 v[16:31], v[48:51], v[64:67], 0\n"
"v_exp_f32 v37, v37\n"
"v_exp_f32 v38, v38\n"
"s_add_u32 s83, s83, 4096\n"
"s_and_b32 s83, s83, 0x1ffff\n"
"buffer_load_dwordx4 v[224:227], v4, s[32:35], s83 offen sc1\n"
"v_exp_f32 v39, v39\n"
"v_exp_f32 v40, v40\n"
"v_exp_f32 v41, v41\n"
"v_smfmac_f32_32x32x32_bf16 v[112:127], v[88:91], v[32:39], v0\n"
"v_exp_f32 v42, v42\n"
"v_exp_f32 v43, v43\n"
"v_exp_f32 v44, v44\n"
"s_mov_b64 exec, s[80:81]\n"
"buffer_load_dwordx4 v[228:231], v4, s[32:35], s83 offen offset:1024 sc1\n"
"buffer_load_dwordx4 v[232:235], v4, s[32:35], s83 offen offset:1280 sc1\n"
"s_mov_b64 exec, -1\n"
"v_exp_f32 v45, v45\n"
"v_exp_f32 v46, v46\n"
"v_exp_f32 v47, v47\n"
"v_exp_f32 v16, v16\n"
"v_exp_f32 v17, v17\n"
"v_smfmac_f32_32x32x32_bf16 v[112:127], v[92:95], v[40:47], v0\n"
"v_exp_f32 v18, v18\n"
"v_exp_f32 v19, v19\n"
"v_exp_f32 v20, v20\n"
"v_mfma_f32_32x32x16_bf16 v[32:47], v[48:51], v[68:71], 0\n"
"v_exp_f32 v21, v21\n"
"v_exp_f32 v22, v22\n"
"v_exp_f32 v23, v23\n"
"v_exp_f32 v24, v24\n"
"s_add_u32 s83, s83, 4096\n"
"s_and_b32 s83, s83, 0x1ffff\n"
"buffer_load_dwordx4 v[236:239], v4, s[32:35], s83 offen sc1\n"
"v_exp_f32 v25, v25\n"
"v_smfmac_f32_32x32x32_bf16 v[96:111], v[52:55], v[16:23], v0\n"
"v_exp_f32 v26, v26\n"
"v_exp_f32 v27, v27\n"
"s_mov_b64 exec, s[80:81]\n"
"buffer_load_dwordx4 v[240:243], v4, s[32:35], s83 offen offset:1024 sc1\n"
"buffer_load_dwordx4 v[244:247], v4, s[32:35], s83 offen offset:1280 sc1\n"
"s_mov_b64 exec, -1\n"
"v_exp_f32 v28, v28\n"
"v_exp_f32 v29, v29\n"
"v_exp_f32 v30, v30\n"
"v_exp_f32 v31, v31\n"
"s_add_u32 m0, s87, 0\n"
"s_xor_b32 s84, s86, 0\n"
"buffer_load_dwordx4 v5, s[24:27], s84 offen lds\n"
"v_exp_f32 v32, v32\n"
"v_exp_f32 v33, v33\n"
"v_smfmac_f32_32x32x32_bf16 v[96:111], v[56:59], v[24:31], v0\n"
"v_exp_f32 v34, v34\n"
"v_exp_f32 v35, v35\n"
"s_add_u32 s83, s83, 4096\n"
"s_and_b32 s83, s83, 0x1ffff\n"
"buffer_load_dwordx4 v[72:75], v4, s[32:35], s83 offen sc1\n"
"v_exp_f32 v36, v36\n"
"s_waitcnt vmcnt(9)\n"
"v_max3_u32 v254, v193, v195, v197\n"
"s_waitcnt vmcnt(8)\n"
"v_max3_u32 v254, v199, v201, v254\n"
"v_max_u32 v254, v203, v254\n"
"v_cmp_eq_u32 vcc, -1, v254\n"
"s_nop 0\n"
"s_cbranch_vccnz RT36_%=\n"
"BK37_%=:\n"
"v_mfma_f32_32x32x16_bf16 v[16:31], v[192:195], v[64:67], 0\n"
"v_exp_f32 v37, v37\n"
"s_add_u32 m0, s87, 1040\n"
"s_add_u32 s84, s84, 8192\n"
"buffer_load_dwordx4 v5, s[24:27], s84 offen lds\n"
"v_exp_f32 v38, v38\n"
"v_exp_f32 v39, v39\n"
"v_exp_f32 v40, v40\n"
"v_exp_f32 v41, v41\n"
"s_mov_b64 exec, s[80:81]\n"
"buffer_load_dwordx4 v[76:79], v4, s[32:35], s83 offen offset:1024 sc1\n"
"buffer_load_dwordx4 v[80:83], v4, s[32:35], s83 offen offset:1280 sc1\n"
"s_mov_b64 exec, -1\n"
"v_smfmac_f32_32x32x32_bf16 v[112:127], v[52:55], v[32:39], v0\n"
"v_exp_f32 v42, v42\n"
"v_exp_f32 v43, v43\n"
"v_exp_f32 v44, v44\n"
"s_cmp_ge_u32 s79, 800\n"
"s_cbranch_scc1 L40_%=\n"
"buffer_store_dwordx4 v[128:131], v2, s[32:35], s44 offen sc1\n"
"L40_%=:\n"
"v_exp_f32 v45, v45\n"
"v_exp_f32 v46, v46\n"
"v_exp_f32 v47, v47\n"
"v_exp_f32 v16, v16\n"
"v_exp_f32 v17, v17\n"
"v_smfmac_f32_32x32x32_bf16 v[112:127], v[56:59], v[40:47], v0\n"
"v_exp_f32 v18, v18\n"
"v_exp_f32 v19, v19\n"
"v_exp_f32 v20, v20\n"
"v_mfma_f32_32x32x16_bf16 v[32:47], v[192:195], v[68:71], 0\n"
"v_exp_f32 v21, v21\n"
"v_exp_f32 v22, v22\n"
"v_exp_f32 v23, v23\n"
"v_exp_f32 v24, v24\n"
"s_add_u32 s83, s83, 4096\n"
"s_and_b32 s83, s83, 0x1ffff\n"
"buffer_load_dwordx4 v[84:87], v4, s[32:35], s83 offen sc1\n"
"v_exp_f32 v25, v25\n"
"v_smfmac_f32_32x32x32_bf16 v[96:111], v[196:199], v[16:23], v0\n"
"v_exp_f32 v26, v26\n"
"v_exp_f32 v27, v27\n"
"s_mov_b64 exec, s[80:81]\n"
"buffer_load_dwordx4 v[88:91], v4, s[32:35], s83 offen offset:1024 sc1\n"
"buffer_load_dwordx4 v[92:95], v4, s[32:35], s83 offen offset:1280 sc1\n"
"s_mov_b64 exec, -1\n"
"v_exp_f32 v28, v28\n"
"v_exp_f32 v29, v29\n"
"v_exp_f32 v30, v30\n"
"v_exp_f32 v31, v31\n"
"s_add_u32 m0, s87, 2080\n"
"s_add_u32 s84, s84, 8192\n"
"buffer_load_dwordx4 v5, s[24:27], s84 offen lds\n"
"v_exp_f32 v32, v32\n"
"v_exp_f32 v33, v33\n"
"v_smfmac_f32_32x32x32_bf16 v[96:111], v[200:203], v[24:31], v0\n"
"v_exp_f32 v34, v34\n"
"v_exp_f32 v35, v35\n"
"s_add_u32 s83, s83, 4096\n"
"s_and_b32 s83, s83, 0x1ffff\n"
"buffer_load_dwordx4 v[48:51], v4, s[32:35], s83 offen sc1\n"
"v_exp_f32 v36, v36\n"
"s_waitcnt vmcnt(14)\n"
"v_max3_u32 v254, v225, v227, v229\n"
"s_waitcnt vmcnt(13)\n"
"v_max3_u32 v254, v231, v233, v254\n"
"v_max_u32 v254, v235, v254\n"
"v_cmp_eq_u32 vcc, -1, v254\n"
"s_nop 0\n"
"s_cbranch_vccnz RT41_%=\n"
"BK42_%=:\n"
"v_mfma_f32_32x32x16_bf16 v[16:31], v[224:227], v[64:67], 0\n"
"v_exp_f32 v37, v37\n"
"s_add_u32 m0, s87, 3120\n"
"s_add_u32 s84, s84, 8192\n"
"buffer_load_dwordx4 v5, s[24:27], s84 offen lds\n"
"v_exp_f32 v38, v38\n"
"v_exp_f32 v39, v39\n"
"v_exp_f32 v40, v40\n"
"v_exp_f32 v41, v41\n"
"s_mov_b64 exec, s[80:81]\n"
"buffer_load_dwordx4 v[52:55], v4, s[32:35], s83 offen offset:1024 sc1\n"
"buffer_load_dwordx4 v[56:59], v4, s[32:35], s83 offen offset:1280 sc1\n"
"s_mov_b64 exec, -1\n"
"v_smfmac_f32_32x32x32_bf16 v[112:127], v[196:199], v[32:39], v0\n"
"v_exp_f32 v42, v42\n"
"v_exp_f32 v43, v43\n"
"v_exp_f32 v44, v44\n"
"s_cmp_ge_u32 s79, 800\n"
"s_cbranch_scc1 L45_%=\n"
"buffer_store_dwordx4 v[132:135], v2, s[32:35], s45 offen sc1\n"
"L45_%=:\n"
"v_exp_f32 v45, v45\n"
"v_exp_f32 v46, v46\n"
"v_exp_f32 v47, v47\n"
"v_exp_f32 v16, v16\n"
"v_exp_f32 v17, v17\n"
"v_smfmac_f32_32x32x32_bf16 v[112:127], v[200:203], v[40:47], v0\n"
"v_exp_f32 v18, v18\n"
"v_exp_f32 v19, v19\n"
"v_exp_f32 v20, v20\n"
"v_mfma_f32_32x32x16_bf16 v[32:47], v[224:227], v[68:71], 0\n"
"v_exp_f32 v21, v21\n"
"v_exp_f32 v22, v22\n"
"v_exp_f32 v23, v23\n"
"v_exp_f32 v24, v24\n"
"v_exp_f32 v25, v25\n"
"s_add_u32 s83, s83, 4096\n"
"s_and_b32 s83, s83, 0x1ffff\n"
"buffer_load_dwordx4 v[192:195], v4, s[32:35], s83 offen sc1\n"
"v_smfmac_f32_32x32x32_bf16 v[96:111], v[228:231], v[16:23], v0\n"
"v_exp_f32 v26, v26\n"
"v_exp_f32 v27, v27\n"
"v_exp_f32 v28, v28\n"
"v_exp_f32 v29, v29\n"
"v_exp_f32 v30, v30\n"
"s_add_u32 m0, s87, 4160\n"
"s_add_u32 s84, s84, 8192\n"
"buffer_load_dwordx4 v5, s[24:27], s84 offen lds\n"
"v_exp_f32 v31, v31\n"
"v_exp_f32 v32, v32\n"
"v_exp_f32 v33, v33\n"
"v_smfmac_f32_32x32x32_bf16 v[96:111], v[232:235], v[24:31], v0\n"
"v_exp_f32 v34, v34\n"
"v_exp_f32 v35, v35\n"
"s_mov_b64 exec, s[80:81]\n"
"buffer_load_dwordx4 v[196:199], v4, s[32:35], s83 offen offset:1024 sc1\n"
"buffer_load_dwordx4 v[200:203], v4, s[32:35], s83 offen offset:1280 sc1\n"
"s_mov_b64 exec, -1\n"
"v_exp_f32 v36, v36\n"
"s_waitcnt vmcnt(18)\n"
"v_max3_u32 v254, v237, v239, v241\n"
"s_waitcnt vmcnt(17)\n"
"v_max3_u32 v254, v243, v245, v254\n"
"v_max_u32 v254, v247, v254\n"
"v_cmp_eq_u32 vcc, -1, v254\n"
"s_nop 0\n"
"s_cbranch_vccnz RT46_%=\n"
"BK47_%=:\n"
"v_mfma_f32_32x32x16_bf16 v[16:31], v[236:239], v[64:67], 0\n"
"v_exp_f32 v37, v37\n"
"v_exp_f32 v38, v38\n"
"v_exp_f32 v39, v39\n"
"s_add_u32 m0, s87, 5200\n"
"s_add_u32 s84, s84, 8192\n"
"buffer_load_dwordx4 v5, s[24:27], s84 offen lds\n"
"v_exp_f32 v40, v40\n"
"v_exp_f32 v41, v41\n"
"v_smfmac_f32_32x32x32_bf16 v[112:127], v[228:231], v[32:39], v0\n"
"v_exp_f32 v42, v42\n"
"v_exp_f32 v43, v43\n"
"v_exp_f32 v44, v44\n"
"s_cmp_ge_u32 s79, 800\n"
"s_cbranch_scc1 L50_%=\n"
"buffer_store_dwordx4 v[136:139], v2, s[32:35], s46 offen sc1\n"
"L50_%=:\n"
"v_exp_f32 v45, v45\n"
"v_exp_f32 v46, v46\n"
"v_exp_f32 v47, v47\n"
"v_exp_f32 v16, v16\n"
"v_exp_f32 v17, v17\n"
"v_smfmac_f32_32x32x32_bf16 v[112:127], v[232:235], v[40:47], v0\n"
"v_exp_f32 v18, v18\n"
"v_exp_f32 v19, v19\n"
"v_exp_f32 v20, v20\n"
"v_mfma_f32_32x32x16_bf16 v[32:47], v[236:239], v[68:71], 0\n"
"v_exp_f32 v21, v21\n"
"v_exp_f32 v22, v22\n"
"v_exp_f32 v23, v23\n"
"v_exp_f32 v24, v24\n"
"v_exp_f32 v25, v25\n"
"s_cmp_lg_u32 s74, 0\n"
"s_cbranch_scc1 L51_%=\n"
"s_lshl_b32 s68, s13, 7\n"
"s_add_u32 s68, s68, 4096\n"
"v_lshrrev_b32 v60, 2, v1\n"
"v_and_b32 v60, 12, v60\n"
"v_mov_b32 v62, 0x5542\n"
"v_bfe_u32 v60, v62, v60, 4\n"
"v_and_b32 v61, 1, v1\n"
"v_lshl_or_b32 v60, v60, 1, v61\n"
"v_bfe_u32 v61, v1, 1, 3\n"
"v_lshlrev_b32 v61, 4, v61\n"
"v_lshl_or_b32 v60, v60, 10, v61\n"
"buffer_load_dwordx4 v[180:183], v60, s[32:35], s68 offen sc1\n"
"s_nop 3\n"
"L51_%=:\n"
"v_smfmac_f32_32x32x32_bf16 v[96:111], v[240:243], v[16:23], v0\n"
"v_exp_f32 v26, v26\n"
"v_exp_f32 v27, v27\n"
"v_exp_f32 v28, v28\n"
"v_exp_f32 v29, v29\n"
"v_exp_f32 v30, v30\n"
"buffer_load_dwordx2 v[12:13], v8, s[32:35], s69 offen sc1\n"
"v_exp_f32 v31, v31\n"
"v_exp_f32 v32, v32\n"
"v_exp_f32 v33, v33\n"
"v_smfmac_f32_32x32x32_bf16 v[96:111], v[244:247], v[24:31], v0\n"
"v_exp_f32 v34, v34\n"
"v_exp_f32 v35, v35\n"
"s_add_u32 m0, s87, 6240\n"
"s_add_u32 s84, s84, 8192\n"
"buffer_load_dwordx4 v5, s[24:27], s84 offen lds\n"
"v_exp_f32 v36, v36\n"
"s_waitcnt vmcnt(16)\n"
"v_max3_u32 v254, v73, v75, v77\n"
"s_waitcnt vmcnt(15)\n"
"v_max3_u32 v254, v79, v81, v254\n"
"v_max_u32 v254, v83, v254\n"
"v_cmp_eq_u32 vcc, -1, v254\n"
"s_nop 0\n"
"s_cbranch_vccnz RT52_%=\n"
"BK53_%=:\n"
"v_mfma_f32_32x32x16_bf16 v[16:31], v[72:75], v[64:67], 0\n"
"v_exp_f32 v37, v37\n"
"v_exp_f32 v38, v38\n"
"v_exp_f32 v39, v39\n"
"s_add_u32 m0, s87, 7280\n"
"s_add_u32 s84, s84, 8192\n"
"buffer_load_dwordx4 v5, s[24:27], s84 offen lds\n"
"v_exp_f32 v40, v40\n"
"v_exp_f32 v41, v41\n"
"v_smfmac_f32_32x32x32_bf16 v[112:127], v[240:243], v[32:39], v0\n"
"v_exp_f32 v42, v42\n"
"v_exp_f32 v43, v43\n"
"v_exp_f32 v44, v44\n"
"s_cmp_ge_u32 s79, 800\n"
"s_cbranch_scc1 L56_%=\n"
"buffer_store_dwordx4 v[140:143], v2, s[32:35], s47 offen sc1\n"
"buffer_store_dwordx2 v[144:145], v164, s[32:35], s66 offen sc1\n"
"L56_%=:\n"
"v_exp_f32 v45, v45\n"
"v_exp_f32 v46, v46\n"
"v_exp_f32 v47, v47\n"
"v_exp_f32 v16, v16\n"
"v_exp_f32 v17, v17\n"
"v_smfmac_f32_32x32x32_bf16 v[112:127], v[244:247], v[40:47], v0\n"
"v_exp_f32 v18, v18\n"
"v_exp_f32 v19, v19\n"
"v_exp_f32 v20, v20\n"
"v_mfma_f32_32x32x16_bf16 v[32:47], v[72:75], v[68:71], 0\n"
"v_exp_f32 v21, v21\n"
"v_exp_f32 v22, v22\n"
"v_exp_f32 v23, v23\n"
"v_exp_f32 v24, v24\n"
"v_exp_f32 v25, v25\n"
"v_smfmac_f32_32x32x32_bf16 v[96:111], v[76:79], v[16:23], v0\n"
"v_exp_f32 v26, v26\n"
"v_exp_f32 v27, v27\n"
"v_exp_f32 v28, v28\n"
"v_exp_f32 v29, v29\n"
"v_exp_f32 v30, v30\n"
"v_exp_f32 v31, v31\n"
"v_exp_f32 v32, v32\n"
"v_exp_f32 v33, v33\n"
"s_waitcnt vmcnt(2)\n"
"v_cmp_ne_u64 vcc, s[72:73], v[12:13]\n"
"s_nop 0\n"
"s_cbranch_vccnz RT57_%=\n"
"BK58_%=:\n"
"v_smfmac_f32_32x32x32_bf16 v[96:111], v[80:83], v[24:31], v0\n"
"v_exp_f32 v34, v34\n"
"v_exp_f32 v35, v35\n"
"v_exp_f32 v36, v36\n"
"v_max3_u32 v254, v85, v87, v89\n"
"v_max3_u32 v254, v91, v93, v254\n"
"v_max_u32 v254, v95, v254\n"
"v_cmp_eq_u32 vcc, -1, v254\n"
"s_nop 0\n"
"s_cbranch_vccnz RT61_%=\n"
"BK62_%=:\n"
"v_mfma_f32_32x32x16_bf16 v[16:31], v[84:87], v[64:67], 0\n"
"v_exp_f32 v37, v37\n"
"v_exp_f32 v38, v38\n"
"v_exp_f32 v39, v39\n"
"v_exp_f32 v40, v40\n"
"v_exp_f32 v41, v41\n"
"v_smfmac_f32_32x32x32_bf16 v[112:127], v[76:79], v[32:39], v0\n"
"v_exp_f32 v42, v42\n"
"v_exp_f32 v43, v43\n"
"v_exp_f32 v44, v44\n"
"s_add_u32 m0, s87, 8320\n"
"s_xor_b32 s84, s86, 2048\n"
"buffer_load_dwordx4 v5, s[24:27], s84 offen lds\n"
"v_exp_f32 v45, v45\n"
"v_exp_f32 v46, v46\n"
"v_exp_f32 v47, v47\n"
"v_exp_f32 v16, v16\n"
"v_exp_f32 v17, v17\n"
"v_smfmac_f32_32x32x32_bf16 v[112:127], v[80:83], v[40:47], v0\n"
"v_exp_f32 v18, v18\n"
"v_exp_f32 v19, v19\n"
"v_exp_f32 v20, v20\n"
"v_mfma_f32_32x32x16_bf16 v[32:47], v[84:87], v[68:71], 0\n"
"v_exp_f32 v21, v21\n"
"v_exp_f32 v22, v22\n"
"v_exp_f32 v23, v23\n"
"v_exp_f32 v24, v24\n"
"s_add_u32 s83, s83, 4096\n"
"s_and_b32 s83, s83, 0x1ffff\n"
"buffer_load_dwordx4 v[224:227], v4, s[32:35], s83 offen sc1\n"
"v_exp_f32 v25, v25\n"
"v_smfmac_f32_32x32x32_bf16 v[96:111], v[88:91], v[16:23], v0\n"
"v_exp_f32 v26, v26\n"
"v_exp_f32 v27, v27\n"
"s_mov_b64 exec, s[80:81]\n"
"buffer_load_dwordx4 v[228:231], v4, s[32:35], s83 offen offset:1024 sc1\n"
"buffer_load_dwordx4 v[232:235], v4, s[32:35], s83 offen offset:1280 sc1\n"
"s_mov_b64 exec, -1\n"
"v_exp_f32 v28, v28\n"
"v_exp_f32 v29, v29\n"
"v_exp_f32 v30, v30\n"
"v_exp_f32 v31, v31\n"
"buffer_store_dwordx4 v[248:251], v2, s[32:35], s2 offen sc1\n"
"s_add_u32 s2, s2, 1024\n"
"v_exp_f32 v32, v32\n"
"v_exp_f32 v33, v33\n"
"v_smfmac_f32_32x32x32_bf16 v[96:111], v[92:95], v[24:31], v0\n"
"v_exp_f32 v34, v34\n"
"v_exp_f32 v35, v35\n"
"s_add_u32 s83, s83, 4096\n"
"s_and_b32 s83, s83, 0x1ffff\n"
"buffer_load_dwordx4 v[236:239], v4, s[32:35], s83 offen sc1\n"
"v_exp_f32 v36, v36\n"
"v_max3_u32 v254, v49, v51, v53\n"
"v_max3_u32 v254, v55, v57, v254\n"
"v_max_u32 v254, v59, v254\n"
"v_cmp_eq_u32 vcc, -1, v254\n"
"s_nop 0\n"
"s_cbranch_vccnz RT65_%=\n"
"BK66_%=:\n"
"v_mfma_f32_32x32x16_bf16 v[16:31], v[48:51], v[64:67], 0\n"
"v_exp_f32 v37, v37\n"
"s_add_u32 m0, s87, 9360\n"
"s_add_u32 s84, s84, 8192\n"
"buffer_load_dwordx4 v5, s[24:27], s84 offen lds\n"
"v_exp_f32 v38, v38\n"
"v_exp_f32 v39, v39\n"
"v_exp_f32 v40, v40\n"
"v_exp_f32 v41, v41\n"
"s_mov_b64 exec, s[80:81]\n"
"buffer_load_dwordx4 v[240:243], v4, s[32:35], s83 offen offset:1024 sc1\n"
"buffer_load_dwordx4 v[244:247], v4, s[32:35], s83 offen offset:1280 sc1\n"
"s_mov_b64 exec, -1\n"
"v_smfmac_f32_32x32x32_bf16 v[112:127], v[88:91], v[32:39], v0\n"
"v_exp_f32 v42, v42\n"
"v_exp_f32 v43, v43\n"
"v_exp_f32 v44, v44\n"
"s_add_u32 m0, s87, 10400\n"
"s_add_u32 s84, s84, 8192\n"
"buffer_load_dwordx4 v5, s[24:27], s84 offen lds\n"
"v_exp_f32 v45, v45\n"
"v_exp_f32 v46, v46\n"
"v_exp_f32 v47, v47\n"
"v_exp_f32 v16, v16\n"
"v_exp_f32 v17, v17\n"
"v_smfmac_f32_32x32x32_bf16 v[112:127], v[92:95], v[40:47], v0\n"
"v_exp_f32 v18, v18\n"
"v_exp_f32 v19, v19\n"
"v_exp_f32 v20, v20\n"
"v_mfma_f32_32x32x16_bf16 v[32:47], v[48:51], v[68:71], 0\n"
"v_exp_f32 v21, v21\n"
"v_exp_f32 v22, v22\n"
"v_exp_f32 v23, v23\n"
"s_add_u32 s83, s83, 4096\n"
"s_and_b32 s83, s83, 0x1ffff\n"
"buffer_load_dwordx4 v[72:75], v4, s[32:35], s83 offen sc1\n"
"v_exp_f32 v24, v24\n"
"v_exp_f32 v25, v25\n"
"v_smfmac_f32_32x32x32_bf16 v[96:111], v[52:55], v[16:23], v0\n"
"v_exp_f32 v26, v26\n"
"s_add_u32 m0, s87, 11440\n"
"s_add_u32 s84, s84, 8192\n"
"buffer_load_dwordx4 v5, s[24:27], s84 offen lds\n"
"v_exp_f32 v27, v27\n"
"v_exp_f32 v28, v28\n"
"v_exp_f32 v29, v29\n"
"v_exp_f32 v30, v30\n"
"s_mov_b64 exec, s[80:81]\n"
"buffer_load_dwordx4 v[76:79], v4, s[32:35], s83 offen offset:1024 sc1\n"
"buffer_load_dwordx4 v[80:83], v4, s[32:35], s83 offen offset:1280 sc1\n"
"s_mov_b64 exec, -1\n"
"v_exp_f32 v31, v31\n"
"v_exp_f32 v32, v32\n"
"v_exp_f32 v33, v33\n"
"s_add_u32 m0, s87, 12480\n"
"s_add_u32 s84, s84, 8192\n"
"buffer_load_dwordx4 v5, s[24:27], s84 offen lds\n"
"v_smfmac_f32_32x32x32_bf16 v[96:111], v[56:59], v[24:31], v0\n"
"v_exp_f32 v34, v34\n"
"v_exp_f32 v35, v35\n"
"v_exp_f32 v36, v36\n"
"s_add_u32 s83, s83, 4096\n"
"s_and_b32 s83, s83, 0x1ffff\n"
"buffer_load_dwordx4 v[84:87], v4, s[32:35], s83 offen sc1\n"
"v_max3_u32 v254, v193, v195, v197\n"
"v_max3_u32 v254, v199, v201, v254\n"
"v_max_u32 v254, v203, v254\n"
"v_cmp_eq_u32 vcc, -1, v254\n"
"s_nop 0\n"
"s_cbranch_vccnz RT69_%=\n"
"BK70_%=:\n"
"v_mfma_f32_32x32x16_bf16 v[16:31], v[192:195], v[64:67], 0\n"
"v_exp_f32 v37, v37\n"
"v_exp_f32 v38, v38\n"
"s_waitcnt vmcnt(16)\n"
"ds_read_b128 v[128:131], v6 offset:0\n"
"ds_read_b128 v[132:135], v6 offset:64\n"
"v_exp_f32 v39, v39\n"
"v_exp_f32 v40, v40\n"
"v_exp_f32 v41, v41\n"
"v_smfmac_f32_32x32x32_bf16 v[112:127], v[52:55], v[32:39], v0\n"
"s_mov_b64 exec, s[80:81]\n"
"buffer_load_dwordx4 v[88:91], v4, s[32:35], s83 offen offset:1024 sc1\n"
"buffer_load_dwordx4 v[92:95], v4, s[32:35], s83 offen offset:1280 sc1\n"
"s_mov_b64 exec, -1\n"
"v_exp_f32 v42, v42\n"
"v_exp_f32 v43, v43\n"
"v_exp_f32 v44, v44\n"
"s_cmp_ge_u32 s79, 800\n"
"s_cbranch_scc1 L73_%=\n"
"s_waitcnt vmcnt(18)\n"
"s_mov_b32 s89, 65536\n"
"s_mov_b64 exec, 15\n"
"buffer_store_dwordx2 v[14:15], v9, s[32:35], s89 offen sc1\n"
"s_mov_b64 exec, -1\n"
"s_waitcnt lgkmcnt(0)\n"
"s_nop 3\n"
"L73_%=:\n"
"v_exp_f32 v45, v45\n"
"v_exp_f32 v46, v46\n"
"v_exp_f32 v47, v47\n"
"v_exp_f32 v16, v16\n"
"v_exp_f32 v17, v17\n"
"v_smfmac_f32_32x32x32_bf16 v[112:127], v[56:59], v[40:47], v0\n"
"v_exp_f32 v18, v18\n"
"v_exp_f32 v19, v19\n"
"v_exp_f32 v20, v20\n"
"v_mfma_f32_32x32x16_bf16 v[32:47], v[192:195], v[68:71], 0\n"
"v_exp_f32 v21, v21\n"
"v_exp_f32 v22, v22\n"
"v_exp_f32 v23, v23\n"
"s_add_u32 s83, s83, 4096\n"
"s_and_b32 s83, s83, 0x1ffff\n"
"buffer_load_dwordx4 v[48:51], v4, s[32:35], s83 offen sc1\n"
"v_exp_f32 v24, v24\n"
"v_exp_f32 v25, v25\n"
"v_smfmac_f32_32x32x32_bf16 v[96:111], v[196:199], v[16:23], v0\n"
"v_exp_f32 v26, v26\n"
"buffer_store_dwordx4 v[248:251], v2, s[32:35], s2 offen sc1\n"
"s_add_u32 s2, s2, 1024\n"
"v_exp_f32 v27, v27\n"
"v_exp_f32 v28, v28\n"
"v_exp_f32 v29, v29\n"
"v_exp_f32 v30, v30\n"
"s_mov_b64 exec, s[80:81]\n"
"buffer_load_dwordx4 v[52:55], v4, s[32:35], s83 offen offset:1024 sc1\n"
"buffer_load_dwordx4 v[56:59], v4, s[32:35], s83 offen offset:1280 sc1\n"
"s_mov_b64 exec, -1\n"
"v_exp_f32 v31, v31\n"
"v_exp_f32 v32, v32\n"
"v_exp_f32 v33, v33\n"
"s_add_u32 m0, s87, 13520\n"
"s_add_u32 s84, s84, 8192\n"
"buffer_load_dwordx4 v5, s[24:27], s84 offen lds\n"
"v_smfmac_f32_32x32x32_bf16 v[96:111], v[200:203], v[24:31], v0\n"
"v_exp_f32 v34, v34\n"
"v_exp_f32 v35, v35\n"
"v_exp_f32 v36, v36\n"
"ds_read_b128 v[136:139], v7 offset:128\n"
"ds_read_b128 v[140:143], v7 offset:192\n"
"s_waitcnt vmcnt(21)\n"
"v_mfma_f32_32x32x16_bf16 v[16:31], v[224:227], v[64:67], 0\n"
"v_exp_f32 v37, v37\n"
"v_exp_f32 v38, v38\n"
"v_exp_f32 v39, v39\n"
"s_add_u32 s83, s83, 4096\n"
"s_and_b32 s83, s83, 0x1ffff\n"
"buffer_load_dwordx4 v[192:195], v4, s[32:35], s83 offen sc1\n"
"v_exp_f32 v40, v40\n"
"v_exp_f32 v41, v41\n"
"v_smfmac_f32_32x32x32_bf16 v[112:127], v[196:199], v[32:39], v0\n"
"v_exp_f32 v42, v42\n"
"s_add_u32 m0, s87, 14560\n"
"s_add_u32 s84, s84, 8192\n"
"buffer_load_dwordx4 v5, s[24:27], s84 offen lds\n"
"v_exp_f32 v43, v43\n"
"v_exp_f32 v44, v44\n"
"v_exp_f32 v45, v45\n"
"ds_read_b128 v[144:147], v6 offset:256\n"
"ds_read_b128 v[148:151], v6 offset:320\n"
"v_exp_f32 v46, v46\n"
"v_exp_f32 v47, v47\n"
"v_exp_f32 v16, v16\n"
"v_exp_f32 v17, v17\n"
"v_smfmac_f32_32x32x32_bf16 v[112:127], v[200:203], v[40:47], v0\n"
"v_exp_f32 v18, v18\n"
"v_exp_f32 v19, v19\n"
"v_exp_f32 v20, v20\n"
"v_mfma_f32_32x32x16_bf16 v[32:47], v[224:227], v[68:71], 0\n"
"v_exp_f32 v21, v21\n"
"v_exp_f32 v22, v22\n"
"v_exp_f32 v23, v23\n"
"v_exp_f32 v24, v24\n"
"v_exp_f32 v25, v25\n"
"s_waitcnt vmcnt(22)\n"
"v_smfmac_f32_32x32x32_bf16 v[96:111], v[228:231], v[16:23], v0\n"
"v_exp_f32 v26, v26\n"
"s_mov_b64 exec, s[80:81]\n"
"buffer_load_dwordx4 v[196:199], v4, s[32:35], s83 offen offset:1024 sc1\n"
"buffer_load_dwordx4 v[200:203], v4, s[32:35], s83 offen offset:1280 sc1\n"
"s_mov_b64 exec, -1\n"
"v_exp_f32 v27, v27\n"
"v_exp_f32 v28, v28\n"
"v_exp_f32 v29, v29\n"
"v_exp_f32 v30, v30\n"
"v_exp_f32 v31, v31\n"
"v_exp_f32 v32, v32\n"
"v_exp_f32 v33, v33\n"
"s_add_u32 m0, s87, 15600\n"
"s_add_u32 s84, s84, 8192\n"
"buffer_load_dwordx4 v5, s[24:27], s84 offen lds\n"
"s_waitcnt vmcnt(24)\n"
"v_smfmac_f32_32x32x32_bf16 v[96:111], v[232:235], v[24:31], v0\n"
"v_exp_f32 v34, v34\n"
"v_exp_f32 v35, v35\n"
"v_exp_f32 v36, v36\n"
"s_waitcnt vmcnt(22)\n"
"v_mfma_f32_32x32x16_bf16 v[16:31], v[236:239], v[64:67], 0\n"
"v_exp_f32 v37, v37\n"
"v_exp_f32 v38, v38\n"
"v_exp_f32 v39, v39\n"
"s_add_u32 s83, s83, 4096\n"
"s_and_b32 s83, s83, 0x1ffff\n"
"buffer_load_dwordx4 v[224:227], v4, s[32:35], s83 offen sc1\n"
"v_exp_f32 v40, v40\n"
"v_exp_f32 v41, v41\n"
"v_smfmac_f32_32x32x32_bf16 v[112:127], v[228:231], v[32:39], v0\n"
"v_exp_f32 v42, v42\n"
"v_exp_f32 v43, v43\n"
"v_exp_f32 v44, v44\n"
"v_exp_f32 v45, v45\n"
"ds_read_b128 v[152:155], v7 offset:384\n"
"ds_read_b128 v[156:159], v7 offset:448\n"
"v_exp_f32 v46, v46\n"
"v_exp_f32 v47, v47\n"
"v_exp_f32 v16, v16\n"
"v_exp_f32 v17, v17\n"
"v_smfmac_f32_32x32x32_bf16 v[112:127], v[232:235], v[40:47], v0\n"
"v_exp_f32 v18, v18\n"
"v_exp_f32 v19, v19\n"
"v_exp_f32 v20, v20\n"
"v_mfma_f32_32x32x16_bf16 v[32:47], v[236:239], v[68:71], 0\n"
"v_exp_f32 v21, v21\n"
"v_exp_f32 v22, v22\n"
"v_exp_f32 v23, v23\n"
"v_exp_f32 v24, v24\n"
"v_exp_f32 v25, v25\n"
"s_mov_b64 exec, s[80:81]\n"
"buffer_load_dwordx4 v[228:231], v4, s[32:35], s83 offen offset:1024 sc1\n"
"buffer_load_dwordx4 v[232:235], v4, s[32:35], s83 offen offset:1280 sc1\n"
"s_mov_b64 exec, -1\n"
"s_waitcnt vmcnt(23)\n"
"v_smfmac_f32_32x32x32_bf16 v[96:111], v[240:243], v[16:23], v0\n"
"v_exp_f32 v26, v26\n"
"v_exp_f32 v27, v27\n"
"v_exp_f32 v28, v28\n"
"v_exp_f32 v29, v29\n"
"v_exp_f32 v30, v30\n"
"buffer_store_dwordx4 v[248:251], v2, s[32:35], s2 offen sc1\n"
"s_add_u32 s2, s2, 1024\n"
"v_exp_f32 v31, v31\n"
"v_exp_f32 v32, v32\n"
"v_exp_f32 v33, v33\n"
"s_waitcnt vmcnt(23)\n"
"v_smfmac_f32_32x32x32_bf16 v[96:111], v[244:247], v[24:31], v0\n"
"v_exp_f32 v34, v34\n"
"v_exp_f32 v35, v35\n"
"s_add_u32 s83, s83, 4096\n"
"s_and_b32 s83, s83, 0x1ffff\n"
"buffer_load_dwordx4 v[236:239], v4, s[32:35], s83 offen sc1\n"
"v_exp_f32 v36, v36\n"
"s_waitcnt vmcnt(22)\n"
"v_mfma_f32_32x32x16_bf16 v[16:31], v[72:75], v[64:67], 0\n"
"v_exp_f32 v37, v37\n"
"v_exp_f32 v38, v38\n"
"v_exp_f32 v39, v39\n"
"v_exp_f32 v40, v40\n"
"s_waitcnt lgkmcnt(0)\n"
"s_add_u32 m0, s87, 0\n"
"s_xor_b32 s84, s86, 65536\n"
"buffer_load_dwordx4 v5, s[24:27], s84 offen lds\n"
"v_exp_f32 v41, v41\n"
"v_smfmac_f32_32x32x32_bf16 v[112:127], v[240:243], v[32:39], v0\n"
"v_exp_f32 v42, v42\n"
"v_exp_f32 v43, v43\n"
"v_exp_f32 v44, v44\n"
"v_exp_f32 v45, v45\n"
"s_add_u32 m0, s87, 1040\n"
"s_add_u32 s84, s84, 8192\n"
"buffer_load_dwordx4 v5, s[24:27], s84 offen lds\n"
"v_exp_f32 v46, v46\n"
"v_exp_f32 v47, v47\n"
"s_setprio 2\n"
"v_exp_f32 v16, v16\n"
"v_exp_f32 v17, v17\n"
"v_smfmac_f32_32x32x32_bf16 v[112:127], v[244:247], v[40:47], v0\n"
"v_exp_f32 v18, v18\n"
"v_exp_f32 v19, v19\n"
"v_exp_f32 v20, v20\n"
"v_mfma_f32_32x32x16_bf16 v[32:47], v[72:75], v[68:71], 0\n"
"v_exp_f32 v21, v21\n"
"v_exp_f32 v22, v22\n"
"v_exp_f32 v23, v23\n"
"v_exp_f32 v24, v24\n"
"v_exp_f32 v25, v25\n"
"s_mov_b64 exec, s[80:81]\n"
"buffer_load_dwordx4 v[240:243], v4, s[32:35], s83 offen offset:1024 sc1\n"
"buffer_load_dwordx4 v[244:247], v4, s[32:35], s83 offen offset:1280 sc1\n"
"s_mov_b64 exec, -1\n"
"s_waitcnt vmcnt(24)\n"
"v_smfmac_f32_32x32x32_bf16 v[96:111], v[76:79], v[16:23], v0\n"
"v_exp_f32 v26, v26\n"
"v_exp_f32 v27, v27\n"
"v_exp_f32 v28, v28\n"
"v_exp_f32 v29, v29\n"
"v_exp_f32 v30, v30\n"
"s_add_u32 m0, s87, 2080\n"
"s_add_u32 s84, s84, 8192\n"
"buffer_load_dwordx4 v5, s[24:27], s84 offen lds\n"
"v_exp_f32 v31, v31\n"
"v_exp_f32 v32, v32\n"
"v_exp_f32 v33, v33\n"
"s_waitcnt vmcnt(24)\n"
"v_smfmac_f32_32x32x32_bf16 v[96:111], v[80:83], v[24:31], v0\n"
"v_exp_f32 v34, v34\n"
"v_exp_f32 v35, v35\n"
"s_add_u32 s83, s83, 4096\n"
"s_and_b32 s83, s83, 0x1ffff\n"
"buffer_load_dwordx4 v[72:75], v4, s[32:35], s83 offen sc1\n"
"v_exp_f32 v36, v36\n"
"s_waitcnt vmcnt(23)\n"
"v_mfma_f32_32x32x16_bf16 v[16:31], v[84:87], v[64:67], 0\n"
"v_exp_f32 v37, v37\n"
"v_exp_f32 v38, v38\n"
"v_exp_f32 v39, v39\n"
"v_exp_f32 v40, v40\n"
"s_add_u32 m0, s87, 3120\n"
"s_add_u32 s84, s84, 8192\n"
"buffer_load_dwordx4 v5, s[24:27], s84 offen lds\n"
"v_exp_f32 v41, v41\n"
"v_smfmac_f32_32x32x32_bf16 v[112:127], v[76:79], v[32:39], v0\n"
"v_exp_f32 v42, v42\n"
"v_exp_f32 v43, v43\n"
"v_exp_f32 v44, v44\n"
"v_exp_f32 v45, v45\n"
"s_cmp_lg_u32 s74, 0\n"
"s_cbranch_scc1 L74_%=\n"
"s_waitcnt vmcnt(24)\n"
"v_cmp_ne_u32 vcc, s72, v181\n"
"v_cmp_ne_u32 s[92:93], s73, v183\n"
"s_nop 0\n"
"s_or_b64 vcc, vcc, s[92:93]\n"
"s_cbranch_vccnz RT75_%=\n"
"BK76_%=:\n"
"v_lshlrev_b32 v184, 16, v180\n"
"v_and_b32 v185, 0xffff0000, v180\n"
"v_lshlrev_b32 v186, 16, v182\n"
"v_and_b32 v187, 0xffff0000, v182\n"
"v_add_u32 v63, 157696, v2\n"
"ds_write_b128 v63, v[184:187]\n"
"s_waitcnt lgkmcnt(0)\n"
"s_nop 3\n"
"L74_%=:\n"
"v_exp_f32 v46, v46\n"
"v_exp_f32 v47, v47\n"
"v_exp_f32 v16, v16\n"
"v_exp_f32 v17, v17\n"
"v_smfmac_f32_32x32x32_bf16 v[112:127], v[80:83], v[40:47], v0\n"
"v_exp_f32 v18, v18\n"
"v_exp_f32 v19, v19\n"
"v_exp_f32 v20, v20\n"
"v_mfma_f32_32x32x16_bf16 v[32:47], v[84:87], v[68:71], 0\n"
"v_exp_f32 v21, v21\n"
"v_exp_f32 v22, v22\n"
"v_exp_f32 v23, v23\n"
"v_exp_f32 v24, v24\n"
"s_mov_b64 exec, s[80:81]\n"
"buffer_load_dwordx4 v[76:79], v4, s[32:35], s83 offen offset:1024 sc1\n"
"buffer_load_dwordx4 v[80:83], v4, s[32:35], s83 offen offset:1280 sc1\n"
"s_mov_b64 exec, -1\n"
"v_exp_f32 v25, v25\n"
"s_waitcnt vmcnt(25)\n"
"v_smfmac_f32_32x32x32_bf16 v[96:111], v[88:91], v[16:23], v0\n"
"v_exp_f32 v26, v26\n"
"v_exp_f32 v27, v27\n"
"buffer_store_dwordx4 v[248:251], v2, s[32:35], s2 offen sc1\n"
"s_add_u32 s2, s2, 1024\n"
"v_exp_f32 v28, v28\n"
"v_exp_f32 v29, v29\n"
"v_exp_f32 v30, v30\n"
"v_exp_f32 v31, v31\n"
"s_add_u32 m0, s87, 4160\n"
"s_add_u32 s84, s84, 8192\n"
"buffer_load_dwordx4 v5, s[24:27], s84 offen lds\n"
"v_exp_f32 v32, v32\n"
"v_exp_f32 v33, v33\n"
"s_waitcnt vmcnt(26)\n"
"v_smfmac_f32_32x32x32_bf16 v[96:111], v[92:95], v[24:31], v0\n"
"v_exp_f32 v34, v34\n"
"v_exp_f32 v35, v35\n"
"s_add_u32 s83, s83, 4096\n"
"s_and_b32 s83, s83, 0x1ffff\n"
"buffer_load_dwordx4 v[84:87], v4, s[32:35], s83 offen sc1\n"
"v_exp_f32 v36, v36\n"
"s_waitcnt vmcnt(26)\n"
"v_mfma_f32_32x32x16_bf16 v[16:31], v[48:51], v[64:67], 0\n"
"v_exp_f32 v37, v37\n"
"v_exp_f32 v38, v38\n"
"s_waitcnt vmcnt(17)\n"
"ds_read_b128 v[160:163], v6 offset:8320\n"
"ds_read_b128 v[164:167], v6 offset:8384\n"
"v_exp_f32 v39, v39\n"
"v_exp_f32 v40, v40\n"
"v_exp_f32 v41, v41\n"
"v_smfmac_f32_32x32x32_bf16 v[112:127], v[88:91], v[32:39], v0\n"
"s_add_u32 m0, s87, 5200\n"
"s_add_u32 s84, s84, 8192\n"
"buffer_load_dwordx4 v5, s[24:27], s84 offen lds\n"
"v_exp_f32 v42, v42\n"
"v_exp_f32 v43, v43\n"
"v_exp_f32 v44, v44\n"
"v_exp_f32 v45, v45\n"
"ds_read_b128 v[168:171], v7 offset:8448\n"
"ds_read_b128 v[172:175], v7 offset:8512\n"
"v_exp_f32 v46, v46\n"
"v_exp_f32 v47, v47\n"
"v_exp_f32 v16, v16\n"
"v_exp_f32 v17, v17\n"
"v_smfmac_f32_32x32x32_bf16 v[112:127], v[92:95], v[40:47], v0\n"
"v_exp_f32 v18, v18\n"
"v_exp_f32 v19, v19\n"
"v_exp_f32 v20, v20\n"
"v_mfma_f32_32x32x16_bf16 v[32:47], v[48:51], v[68:71], 0\n"
"v_exp_f32 v21, v21\n"
"v_exp_f32 v22, v22\n"
"v_exp_f32 v23, v23\n"
"v_exp_f32 v24, v24\n"
"s_mov_b64 exec, s[80:81]\n"
"buffer_load_dwordx4 v[88:91], v4, s[32:35], s83 offen offset:1024 sc1\n"
"buffer_load_dwordx4 v[92:95], v4, s[32:35], s83 offen offset:1280 sc1\n"
"s_mov_b64 exec, -1\n"
"v_exp_f32 v25, v25\n"
"v_smfmac_f32_32x32x32_bf16 v[96:111], v[52:55], v[16:23], v0\n"
"v_exp_f32 v26, v26\n"
"v_exp_f32 v27, v27\n"
"v_exp_f32 v28, v28\n"
"s_add_u32 m0, s87, 6240\n"
"s_add_u32 s84, s84, 8192\n"
"buffer_load_dwordx4 v5, s[24:27], s84 offen lds\n"
"v_exp_f32 v29, v29\n"
"v_exp_f32 v30, v30\n"
"v_exp_f32 v31, v31\n"
"v_exp_f32 v32, v32\n"
"v_exp_f32 v33, v33\n"
"ds_read_b128 v[176:179], v6 offset:8576\n"
"ds_read_b128 v[180:183], v6 offset:8640\n"
"v_smfmac_f32_32x32x32_bf16 v[96:111], v[56:59], v[24:31], v0\n"
"v_exp_f32 v34, v34\n"
"v_exp_f32 v35, v35\n"
"v_exp_f32 v36, v36\n"
"v_mfma_f32_32x32x16_bf16 v[16:31], v[192:195], v[64:67], 0\n"
"s_add_u32 s83, s83, 4096\n"
"s_and_b32 s83, s83, 0x1ffff\n"
"buffer_load_dwordx4 v[48:51], v4, s[32:35], s83 offen sc1\n"
"v_exp_f32 v37, v37\n"
"v_exp_f32 v38, v38\n"
"v_exp_f32 v39, v39\n"
"v_exp_f32 v40, v40\n"
"v_exp_f32 v41, v41\n"
"s_add_u32 m0, s87, 7280\n"
"s_add_u32 s84, s84, 8192\n"
"buffer_load_dwordx4 v5, s[24:27], s84 offen lds\n"
"v_smfmac_f32_32x32x32_bf16 v[112:127], v[52:55], v[32:39], v0\n"
"v_exp_f32 v42, v42\n"
"v_exp_f32 v43, v43\n"
"v_exp_f32 v44, v44\n"
"v_exp_f32 v45, v45\n"
"ds_read_b128 v[184:187], v7 offset:8704\n"
"ds_read_b128 v[188:191], v7 offset:8768\n"
"v_exp_f32 v46, v46\n"
"v_exp_f32 v47, v47\n"
"v_exp_f32 v16, v16\n"
"v_exp_f32 v17, v17\n"
"v_smfmac_f32_32x32x32_bf16 v[112:127], v[56:59], v[40:47], v0\n"
"v_exp_f32 v18, v18\n"
"v_exp_f32 v19, v19\n"
"v_exp_f32 v20, v20\n"
"v_mfma_f32_32x32x16_bf16 v[32:47], v[192:195], v[68:71], 0\n"
"v_exp_f32 v21, v21\n"
"v_exp_f32 v22, v22\n"
"v_exp_f32 v23, v23\n"
"v_exp_f32 v24, v24\n"
"v_exp_f32 v25, v25\n"
"v_smfmac_f32_32x32x32_bf16 v[96:111], v[196:199], v[16:23], v0\n"
"v_exp_f32 v26, v26\n"
"v_exp_f32 v27, v27\n"
"v_exp_f32 v28, v28\n"
"s_mov_b64 exec, s[80:81]\n"
"buffer_load_dwordx4 v[52:55], v4, s[32:35], s83 offen offset:1024 sc1\n"
"buffer_load_dwordx4 v[56:59], v4, s[32:35], s83 offen offset:1280 sc1\n"
"s_mov_b64 exec, -1\n"
"v_exp_f32 v29, v29\n"
"v_exp_f32 v30, v30\n"
"v_exp_f32 v31, v31\n"
"v_exp_f32 v32, v32\n"
"v_exp_f32 v33, v33\n"
"v_smfmac_f32_32x32x32_bf16 v[96:111], v[200:203], v[24:31], v0\n"
"v_exp_f32 v34, v34\n"
"v_exp_f32 v35, v35\n"
"v_exp_f32 v36, v36\n"
"s_waitcnt vmcnt(24)\n"
"v_mfma_f32_32x32x16_bf16 v[16:31], v[224:227], v[64:67], 0\n"
"buffer_store_dwordx4 v[248:251], v2, s[32:35], s2 offen sc1\n"
"s_add_u32 s2, s2, 1024\n"
"v_exp_f32 v37, v37\n"
"v_exp_f32 v38, v38\n"
"v_exp_f32 v39, v39\n"
"v_exp_f32 v40, v40\n"
"v_exp_f32 v41, v41\n"
"v_smfmac_f32_32x32x32_bf16 v[112:127], v[196:199], v[32:39], v0\n"
"v_exp_f32 v42, v42\n"
"v_exp_f32 v43, v43\n"
"v_exp_f32 v44, v44\n"
"v_exp_f32 v45, v45\n"
"s_waitcnt lgkmcnt(0)\n"
"s_add_u32 m0, s87, 8320\n"
"s_xor_b32 s84, s86, 67584\n"
"buffer_load_dwordx4 v5, s[24:27], s84 offen lds\n"
"v_exp_f32 v46, v46\n"
"v_exp_f32 v47, v47\n"
"v_exp_f32 v16, v16\n"
"v_exp_f32 v17, v17\n"
"v_smfmac_f32_32x32x32_bf16 v[112:127], v[200:203], v[40:47], v0\n"
"v_exp_f32 v18, v18\n"
"v_exp_f32 v19, v19\n"
"v_exp_f32 v20, v20\n"
"v_mfma_f32_32x32x16_bf16 v[32:47], v[224:227], v[68:71], 0\n"
"v_exp_f32 v21, v21\n"
"v_exp_f32 v22, v22\n"
"v_exp_f32 v23, v23\n"
"v_exp_f32 v24, v24\n"
"v_exp_f32 v25, v25\n"
"s_waitcnt vmcnt(25)\n"
"v_smfmac_f32_32x32x32_bf16 v[96:111], v[228:231], v[16:23], v0\n"
"v_exp_f32 v26, v26\n"
"s_add_u32 s83, s83, 4096\n"
"s_and_b32 s83, s83, 0x1ffff\n"
"buffer_load_dwordx4 v[224:227], v4, s[32:35], s83 offen sc1\n"
"v_exp_f32 v27, v27\n"
"v_exp_f32 v28, v28\n"
"v_exp_f32 v29, v29\n"
"v_exp_f32 v30, v30\n"
"v_exp_f32 v31, v31\n"
"v_exp_f32 v32, v32\n"
"v_exp_f32 v33, v33\n"
"s_mov_b32 exec_hi, 0xffff\n"
"buffer_store_dwordx4 v[248:251], v2, s[32:35], s2 offen sc1\n"
"s_mov_b32 exec_hi, -1\n"
"s_waitcnt vmcnt(26)\n"
"v_smfmac_f32_32x32x32_bf16 v[96:111], v[232:235], v[24:31], v0\n"
"v_exp_f32 v34, v34\n"
"v_exp_f32 v35, v35\n"
"v_exp_f32 v36, v36\n"
"s_waitcnt vmcnt(24)\n"
"v_mfma_f32_32x32x16_bf16 v[16:31], v[236:239], v[64:67], 0\n"
"v_exp_f32 v37, v37\n"
"v_exp_f32 v38, v38\n"
"v_exp_f32 v39, v39\n"
"s_add_u32 m0, s87, 9360\n"
"s_add_u32 s84, s84, 8192\n"
"buffer_load_dwordx4 v5, s[24:27], s84 offen lds\n"
"v_exp_f32 v40, v40\n"
"v_exp_f32 v41, v41\n"
"v_smfmac_f32_32x32x32_bf16 v[112:127], v[228:231], v[32:39], v0\n"
"v_exp_f32 v42, v42\n"
"v_exp_f32 v43, v43\n"
"v_exp_f32 v44, v44\n"
"v_exp_f32 v45, v45\n"
"s_add_u32 m0, s87, 10400\n"
"s_add_u32 s84, s84, 8192\n"
"buffer_load_dwordx4 v5, s[24:27], s84 offen lds\n"
"v_exp_f32 v46, v46\n"
"v_exp_f32 v47, v47\n"
"v_exp_f32 v16, v16\n"
"v_exp_f32 v17, v17\n"
"v_smfmac_f32_32x32x32_bf16 v[112:127], v[232:235], v[40:47], v0\n"
"v_exp_f32 v18, v18\n"
"v_exp_f32 v19, v19\n"
"v_exp_f32 v20, v20\n"
"v_mfma_f32_32x32x16_bf16 v[32:47], v[236:239], v[68:71], 0\n"
"v_exp_f32 v21, v21\n"
"v_exp_f32 v22, v22\n"
"v_exp_f32 v23, v23\n"
"v_exp_f32 v24, v24\n"
"v_exp_f32 v25, v25\n"
"s_waitcnt vmcnt(23)\n"
"v_smfmac_f32_32x32x32_bf16 v[96:111], v[240:243], v[16:23], v0\n"
"v_exp_f32 v26, v26\n"
"s_mov_b64 exec, s[80:81]\n"
"buffer_load_dwordx4 v[228:231], v4, s[32:35], s83 offen offset:1024 sc1\n"
"buffer_load_dwordx4 v[232:235], v4, s[32:35], s83 offen offset:1280 sc1\n"
"s_mov_b64 exec, -1\n"
"v_exp_f32 v27, v27\n"
"v_exp_f32 v28, v28\n"
"v_exp_f32 v29, v29\n"
"v_exp_f32 v30, v30\n"
"v_exp_f32 v31, v31\n"
"v_exp_f32 v32, v32\n"
"v_exp_f32 v33, v33\n"
"s_add_u32 m0, s87, 11440\n"
"s_add_u32 s84, s84, 8192\n"
"buffer_load_dwordx4 v5, s[24:27], s84 offen lds\n"
"s_waitcnt vmcnt(25)\n"
"v_smfmac_f32_32x32x32_bf16 v[96:111], v[244:247], v[24:31], v0\n"
"v_exp_f32 v34, v34\n"
"v_exp_f32 v35, v35\n"
"v_exp_f32 v36, v36\n"
"s_waitcnt vmcnt(23)\n"
"v_mfma_f32_32x32x16_bf16 v[16:31], v[72:75], v[64:67], 0\n"
"v_exp_f32 v37, v37\n"
"v_exp_f32 v38, v38\n"
"v_exp_f32 v39, v39\n"
"s_add_u32 m0, s87, 12480\n"
"s_add_u32 s84, s84, 8192\n"
"buffer_load_dwordx4 v5, s[24:27], s84 offen lds\n"
"v_exp_f32 v40, v40\n"
"v_exp_f32 v41, v41\n"
"v_smfmac_f32_32x32x32_bf16 v[112:127], v[240:243], v[32:39], v0\n"
"v_exp_f32 v42, v42\n"
"v_exp_f32 v43, v43\n"
"v_exp_f32 v44, v44\n"
"v_exp_f32 v45, v45\n"
"s_waitcnt vmcnt(12)\n"
"ds_read_b128 v[192:195], v6 offset:0\n"
"ds_read_b128 v[196:199], v6 offset:64\n"
"v_exp_f32 v46, v46\n"
"v_exp_f32 v47, v47\n"
"v_exp_f32 v16, v16\n"
"v_exp_f32 v17, v17\n"
"v_smfmac_f32_32x32x32_bf16 v[112:127], v[244:247], v[40:47], v0\n"
"v_exp_f32 v18, v18\n"
"v_exp_f32 v19, v19\n"
"v_exp_f32 v20, v20\n"
"v_mfma_f32_32x32x16_bf16 v[32:47], v[72:75], v[68:71], 0\n"
"v_exp_f32 v21, v21\n"
"v_exp_f32 v22, v22\n"
"v_exp_f32 v23, v23\n"
"v_exp_f32 v24, v24\n"
"s_add_u32 s83, s83, 4096\n"
"s_and_b32 s83, s83, 0x1ffff\n"
"buffer_load_dwordx4 v[72:75], v4, s[32:35], s83 offen sc1\n"
"v_exp_f32 v25, v25\n"
"v_smfmac_f32_32x32x32_bf16 v[96:111], v[76:79], v[16:23], v0\n"
"v_exp_f32 v26, v26\n"
"v_exp_f32 v27, v27\n"
"v_exp_f32 v28, v28\n"
"s_add_u32 m0, s87, 13520\n"
"s_add_u32 s84, s84, 8192\n"
"buffer_load_dwordx4 v5, s[24:27], s84 offen lds\n"
"v_exp_f32 v29, v29\n"
"v_exp_f32 v30, v30\n"
"v_exp_f32 v31, v31\n"
"v_exp_f32 v32, v32\n"
"v_exp_f32 v33, v33\n"
"ds_read_b128 v[200:203], v7 offset:128\n"
"ds_read_b128 v[204:207], v7 offset:192\n"
"v_smfmac_f32_32x32x32_bf16 v[96:111], v[80:83], v[24:31], v0\n"
"v_exp_f32 v34, v34\n"
"v_exp_f32 v35, v35\n"
"v_exp_f32 v36, v36\n"
"v_mfma_f32_32x32x16_bf16 v[16:31], v[84:87], v[64:67], 0\n"
"s_add_u32 m0, s87, 14560\n"
"s_add_u32 s84, s84, 8192\n"
"buffer_load_dwordx4 v5, s[24:27], s84 offen lds\n"
"v_exp_f32 v37, v37\n"
"v_exp_f32 v38, v38\n"
"v_exp_f32 v39, v39\n"
"v_exp_f32 v40, v40\n"
"v_exp_f32 v41, v41\n"
"ds_read_b128 v[208:211], v6 offset:256\n"
"ds_read_b128 v[212:215], v6 offset:320\n"
"v_smfmac_f32_32x32x32_bf16 v[112:127], v[76:79], v[32:39], v0\n"
"v_exp_f32 v42, v42\n"
"v_exp_f32 v43, v43\n"
"v_exp_f32 v44, v44\n"
"v_exp_f32 v45, v45\n"
"s_lshl_b32 s99, s15, 7\n"
"s_add_u32 s99, s99, 65536\n"
"v_and_b32 v60, 0x78, v8\n"
"buffer_load_dwordx2 v[62:63], v60, s[32:35], s99 offen sc1\n"
"v_exp_f32 v46, v46\n"
"v_exp_f32 v47, v47\n"
"v_exp_f32 v16, v16\n"
"v_exp_f32 v17, v17\n"
"v_smfmac_f32_32x32x32_bf16 v[112:127], v[80:83], v[40:47], v0\n"
"v_exp_f32 v18, v18\n"
"v_exp_f32 v19, v19\n"
"v_exp_f32 v20, v20\n"
"v_mfma_f32_32x32x16_bf16 v[32:47], v[84:87], v[68:71], 0\n"
"v_exp_f32 v21, v21\n"
"v_exp_f32 v22, v22\n"
"v_exp_f32 v23, v23\n"
"v_exp_f32 v24, v24\n"
"s_mov_b64 exec, s[80:81]\n"
"buffer_load_dwordx4 v[76:79], v4, s[32:35], s83 offen offset:1024 sc1\n"
"buffer_load_dwordx4 v[80:83], v4, s[32:35], s83 offen offset:1280 sc1\n"
"s_mov_b64 exec, -1\n"
"v_exp_f32 v25, v25\n"
"v_smfmac_f32_32x32x32_bf16 v[96:111], v[88:91], v[16:23], v0\n"
"v_exp_f32 v26, v26\n"
"v_exp_f32 v27, v27\n"
"v_exp_f32 v28, v28\n"
"s_add_u32 m0, s87, 15600\n"
"s_add_u32 s84, s84, 8192\n"
"buffer_load_dwordx4 v5, s[24:27], s84 offen lds\n"
"v_exp_f32 v29, v29\n"
"v_exp_f32 v30, v30\n"
"v_exp_f32 v31, v31\n"
"v_exp_f32 v32, v32\n"
"v_exp_f32 v33, v33\n"
"ds_read_b128 v[216:219], v7 offset:384\n"
"ds_read_b128 v[220:223], v7 offset:448\n"
"v_smfmac_f32_32x32x32_bf16 v[96:111], v[92:95], v[24:31], v0\n"
"v_exp_f32 v34, v34\n"
"v_exp_f32 v35, v35\n"
"v_exp_f32 v36, v36\n"
"v_mfma_f32_32x32x16_bf16 v[16:31], v[48:51], v[64:67], 0\n"
"s_add_u32 s83, s83, 4096\n"
"s_and_b32 s83, s83, 0x1ffff\n"
"buffer_load_dwordx4 v[84:87], v4, s[32:35], s83 offen sc1\n"
"v_exp_f32 v37, v37\n"
"v_exp_f32 v38, v38\n"
"v_exp_f32 v39, v39\n"
"v_exp_f32 v40, v40\n"
"v_exp_f32 v41, v41\n"
"s_waitcnt vmcnt(14)\n"
"s_mov_b64 exec, 1\n"
"buffer_store_dwordx2 v[14:15], v2, s[32:35], s3 offen sc1\n"
"s_mov_b64 exec, -1\n"
"v_smfmac_f32_32x32x32_bf16 v[112:127], v[88:91], v[32:39], v0\n"
"v_exp_f32 v42, v42\n"
"v_exp_f32 v43, v43\n"
"v_exp_f32 v44, v44\n"
"v_exp_f32 v45, v45\n"
"s_waitcnt vmcnt(5)\n"
"v_cmp_ne_u64 vcc, s[72:73], v[62:63]\n"
"s_nop 0\n"
"s_cbranch_vccnz RT79_%=\n"
"BK80_%=:\n"
"v_exp_f32 v46, v46\n"
"v_exp_f32 v47, v47\n"
"v_exp_f32 v16, v16\n"
"v_exp_f32 v17, v17\n"
"v_smfmac_f32_32x32x32_bf16 v[112:127], v[92:95], v[40:47], v0\n"
"v_exp_f32 v18, v18\n"
"v_exp_f32 v19, v19\n"
"v_exp_f32 v20, v20\n"
"v_mfma_f32_32x32x16_bf16 v[32:47], v[48:51], v[68:71], 0\n"
"v_exp_f32 v21, v21\n"
"v_exp_f32 v22, v22\n"
"v_exp_f32 v23, v23\n"
"v_exp_f32 v24, v24\n"
"v_exp_f32 v25, v25\n"
"v_smfmac_f32_32x32x32_bf16 v[96:111], v[52:55], v[16:23], v0\n"
"v_exp_f32 v26, v26\n"
"s_mov_b64 exec, s[80:81]\n"
"buffer_load_dwordx4 v[88:91], v4, s[32:35], s83 offen offset:1024 sc1\n"
"buffer_load_dwordx4 v[92:95], v4, s[32:35], s83 offen offset:1280 sc1\n"
"s_mov_b64 exec, -1\n"
"v_exp_f32 v27, v27\n"
"v_exp_f32 v28, v28\n"
"v_exp_f32 v29, v29\n"
"v_exp_f32 v30, v30\n"
"v_exp_f32 v31, v31\n"
"v_exp_f32 v32, v32\n"
"v_exp_f32 v33, v33\n"
"s_waitcnt lgkmcnt(0)\n"
"s_add_u32 m0, s87, 0\n"
"s_nop 0\n"
"buffer_load_dwordx4 v2, s[32:35], s88 offen offset:0 sc1 lds\n"
"v_smfmac_f32_32x32x32_bf16 v[96:111], v[56:59], v[24:31], v0\n"
"v_exp_f32 v34, v34\n"
"v_exp_f32 v35, v35\n"
"v_exp_f32 v36, v36\n"
"v_mfma_f32_32x32x16_bf16 v[16:31], v[224:227], v[64:67], 0\n"
"v_exp_f32 v37, v37\n"
"v_exp_f32 v38, v38\n"
"v_exp_f32 v39, v39\n"
"s_add_u32 s83, s83, 4096\n"
"s_and_b32 s83, s83, 0x1ffff\n"
"buffer_load_dwordx4 v[48:51], v4, s[32:35], s83 offen sc1\n"
"v_exp_f32 v40, v40\n"
"v_exp_f32 v41, v41\n"
"v_smfmac_f32_32x32x32_bf16 v[112:127], v[52:55], v[32:39], v0\n"
"v_exp_f32 v42, v42\n"
"v_exp_f32 v43, v43\n"
"v_exp_f32 v44, v44\n"
"v_exp_f32 v45, v45\n"
"buffer_load_dwordx4 v2, s[32:35], s88 offen offset:1024 sc1 lds\n"
"v_exp_f32 v46, v46\n"
"v_exp_f32 v47, v47\n"
"v_exp_f32 v16, v16\n"
"v_exp_f32 v17, v17\n"
"v_smfmac_f32_32x32x32_bf16 v[112:127], v[56:59], v[40:47], v0\n"
"v_exp_f32 v18, v18\n"
"v_exp_f32 v19, v19\n"
"v_exp_f32 v20, v20\n"
"v_mfma_f32_32x32x16_bf16 v[32:47], v[224:227], v[68:71], 0\n"
"v_exp_f32 v21, v21\n"
"v_exp_f32 v22, v22\n"
"v_exp_f32 v23, v23\n"
"v_exp_f32 v24, v24\n"
"v_exp_f32 v25, v25\n"
"v_smfmac_f32_32x32x32_bf16 v[96:111], v[228:231], v[16:23], v0\n"
"v_exp_f32 v26, v26\n"
"v_exp_f32 v27, v27\n"
"v_exp_f32 v28, v28\n"
"s_mov_b64 exec, s[80:81]\n"
"buffer_load_dwordx4 v[52:55], v4, s[32:35], s83 offen offset:1024 sc1\n"
"buffer_load_dwordx4 v[56:59], v4, s[32:35], s83 offen offset:1280 sc1\n"
"s_mov_b64 exec, -1\n"
"v_exp_f32 v29, v29\n"
"v_exp_f32 v30, v30\n"
"v_exp_f32 v31, v31\n"
"v_exp_f32 v32, v32\n"
"v_exp_f32 v33, v33\n"
"v_smfmac_f32_32x32x32_bf16 v[96:111], v[232:235], v[24:31], v0\n"
"v_exp_f32 v34, v34\n"
"v_exp_f32 v35, v35\n"
"v_exp_f32 v36, v36\n"
"v_mfma_f32_32x32x16_bf16 v[16:31], v[72:75], v[64:67], 0\n"
"buffer_load_dwordx4 v2, s[32:35], s88 offen offset:2048 sc1 lds\n"
"v_exp_f32 v37, v37\n"
"v_exp_f32 v38, v38\n"
"v_exp_f32 v39, v39\n"
"v_exp_f32 v40, v40\n"
"v_exp_f32 v41, v41\n"
"v_smfmac_f32_32x32x32_bf16 v[112:127], v[228:231], v[32:39], v0\n"
"v_exp_f32 v42, v42\n"
"v_exp_f32 v43, v43\n"
"v_exp_f32 v44, v44\n"
"v_exp_f32 v45, v45\n"
"buffer_load_dwordx4 v2, s[32:35], s88 offen offset:3072 sc1 lds\n"
"s_add_u32 s88, s88, 4096\n"
"v_exp_f32 v46, v46\n"
"v_exp_f32 v47, v47\n"
"s_setprio 1\n"
"v_exp_f32 v16, v16\n"
"v_exp_f32 v17, v17\n"
"v_smfmac_f32_32x32x32_bf16 v[112:127], v[232:235], v[40:47], v0\n"
"v_exp_f32 v18, v18\n"
"v_exp_f32 v19, v19\n"
"v_exp_f32 v20, v20\n"
"v_mfma_f32_32x32x16_bf16 v[32:47], v[72:75], v[68:71], 0\n"
"v_exp_f32 v21, v21\n"
"v_exp_f32 v22, v22\n"
"v_exp_f32 v23, v23\n"
"v_exp_f32 v24, v24\n"
"v_exp_f32 v25, v25\n"
"s_add_u32 s83, s83, 4096\n"
"s_and_b32 s83, s83, 0x1ffff\n"
"buffer_load_dwordx4 v[72:75], v4, s[32:35], s83 offen sc1\n"
"s_waitcnt vmcnt(14)\n"
"v_smfmac_f32_32x32x32_bf16 v[96:111], v[76:79], v[16:23], v0\n"
"v_exp_f32 v26, v26\n"
"v_exp_f32 v27, v27\n"
"v_exp_f32 v28, v28\n"
"v_exp_f32 v29, v29\n"
"v_exp_f32 v30, v30\n"
"s_add_u32 m0, s87, 4096\n"
"s_nop 0\n"
"buffer_load_dwordx4 v2, s[32:35], s88 offen offset:0 sc1 lds\n"
"v_exp_f32 v31, v31\n"
"v_exp_f32 v32, v32\n"
"v_exp_f32 v33, v33\n"
"s_waitcnt vmcnt(14)\n"
"v_smfmac_f32_32x32x32_bf16 v[96:111], v[80:83], v[24:31], v0\n"
"v_exp_f32 v34, v34\n"
"v_exp_f32 v35, v35\n"
"s_waitcnt vmcnt(13)\n"
"ds_read_b128 v[224:227], v6 offset:8320\n"
"ds_read_b128 v[228:231], v6 offset:8384\n"
"v_exp_f32 v36, v36\n"
"s_waitcnt vmcnt(12)\n"
"v_mfma_f32_32x32x16_bf16 v[16:31], v[84:87], v[64:67], 0\n"
"v_exp_f32 v37, v37\n"
"v_exp_f32 v38, v38\n"
"v_exp_f32 v39, v39\n"
"v_exp_f32 v40, v40\n"
"buffer_load_dwordx4 v2, s[32:35], s88 offen offset:1024 sc1 lds\n"
"v_exp_f32 v41, v41\n"
"v_smfmac_f32_32x32x32_bf16 v[112:127], v[76:79], v[32:39], v0\n"
"v_exp_f32 v42, v42\n"
"v_exp_f32 v43, v43\n"
"v_exp_f32 v44, v44\n"
"v_exp_f32 v45, v45\n"
"ds_read_b128 v[232:235], v7 offset:8448\n"
"ds_read_b128 v[236:239], v7 offset:8512\n"
"v_exp_f32 v46, v46\n"
"v_exp_f32 v47, v47\n"
"v_exp_f32 v16, v16\n"
"v_exp_f32 v17, v17\n"
"v_smfmac_f32_32x32x32_bf16 v[112:127], v[80:83], v[40:47], v0\n"
"v_exp_f32 v18, v18\n"
"v_exp_f32 v19, v19\n"
"v_exp_f32 v20, v20\n"
"v_mfma_f32_32x32x16_bf16 v[32:47], v[84:87], v[68:71], 0\n"
"v_exp_f32 v21, v21\n"
"v_exp_f32 v22, v22\n"
"v_exp_f32 v23, v23\n"
"v_exp_f32 v24, v24\n"
"s_mov_b64 exec, s[80:81]\n"
"buffer_load_dwordx4 v[76:79], v4, s[32:35], s83 offen offset:1024 sc1\n"
"buffer_load_dwordx4 v[80:83], v4, s[32:35], s83 offen offset:1280 sc1\n"
"s_mov_b64 exec, -1\n"
"v_exp_f32 v25, v25\n"
"s_waitcnt vmcnt(13)\n"
"v_smfmac_f32_32x32x32_bf16 v[96:111], v[88:91], v[16:23], v0\n"
"v_exp_f32 v26, v26\n"
"v_exp_f32 v27, v27\n"
"v_exp_f32 v28, v28\n"
"buffer_load_dwordx4 v2, s[32:35], s88 offen offset:2048 sc1 lds\n"
"v_exp_f32 v29, v29\n"
"v_exp_f32 v30, v30\n"
"v_exp_f32 v31, v31\n"
"v_exp_f32 v32, v32\n"
"v_exp_f32 v33, v33\n"
"ds_read_b128 v[240:243], v6 offset:8576\n"
"ds_read_b128 v[244:247], v6 offset:8640\n"
"s_waitcnt vmcnt(13)\n"
"v_smfmac_f32_32x32x32_bf16 v[96:111], v[92:95], v[24:31], v0\n"
"v_exp_f32 v34, v34\n"
"v_exp_f32 v35, v35\n"
"v_exp_f32 v36, v36\n"
"s_waitcnt vmcnt(11)\n"
"v_mfma_f32_32x32x16_bf16 v[16:31], v[48:51], v[64:67], 0\n"
"s_add_u32 s83, s83, 4096\n"
"s_and_b32 s83, s83, 0x1ffff\n"
"buffer_load_dwordx4 v[84:87], v4, s[32:35], s83 offen sc1\n"
"v_exp_f32 v37, v37\n"
"v_exp_f32 v38, v38\n"
"v_exp_f32 v39, v39\n"
"v_exp_f32 v40, v40\n"
"v_exp_f32 v41, v41\n"
"buffer_load_dwordx4 v2, s[32:35], s88 offen offset:3072 sc1 lds\n"
"s_add_u32 s88, s88, 4096\n"
"v_smfmac_f32_32x32x32_bf16 v[112:127], v[88:91], v[32:39], v0\n"
"v_exp_f32 v42, v42\n"
"v_exp_f32 v43, v43\n"
"v_exp_f32 v44, v44\n"
"v_exp_f32 v45, v45\n"
"ds_read_b128 v[248:251], v7 offset:8704\n"
"ds_read_b128 v[252:255], v7 offset:8768\n"
"v_exp_f32 v46, v46\n"
"v_exp_f32 v47, v47\n"
"v_exp_f32 v16, v16\n"
"v_exp_f32 v17, v17\n"
"v_smfmac_f32_32x32x32_bf16 v[112:127], v[92:95], v[40:47], v0\n"
"v_exp_f32 v18, v18\n"
"v_exp_f32 v19, v19\n"
"v_exp_f32 v20, v20\n"
"v_mfma_f32_32x32x16_bf16 v[32:47], v[48:51], v[68:71], 0\n"
"v_exp_f32 v21, v21\n"
"v_exp_f32 v22, v22\n"
"v_exp_f32 v23, v23\n"
"v_exp_f32 v24, v24\n"
"v_exp_f32 v25, v25\n"
"s_waitcnt vmcnt(11)\n"
"v_smfmac_f32_32x32x32_bf16 v[96:111], v[52:55], v[16:23], v0\n"
"v_exp_f32 v26, v26\n"
"v_exp_f32 v27, v27\n"
"v_exp_f32 v28, v28\n"
"s_mov_b64 exec, s[80:81]\n"
"buffer_load_dwordx4 v[88:91], v4, s[32:35], s83 offen offset:1024 sc1\n"
"buffer_load_dwordx4 v[92:95], v4, s[32:35], s83 offen offset:1280 sc1\n"
"s_mov_b64 exec, -1\n"
"v_exp_f32 v29, v29\n"
"v_exp_f32 v30, v30\n"
"v_exp_f32 v31, v31\n"
"v_exp_f32 v32, v32\n"
"v_exp_f32 v33, v33\n"
"s_waitcnt vmcnt(12)\n"
"v_smfmac_f32_32x32x32_bf16 v[96:111], v[56:59], v[24:31], v0\n"
"v_exp_f32 v34, v34\n"
"v_exp_f32 v35, v35\n"
"v_exp_f32 v36, v36\n"
"s_waitcnt vmcnt(9)\n"
"v_mfma_f32_32x32x16_bf16 v[16:31], v[72:75], v[64:67], 0\n"
"s_add_u32 s83, s83, 4096\n"
"s_and_b32 s83, s83, 0x1ffff\n"
"buffer_load_dwordx4 v[48:51], v4, s[32:35], s83 offen sc1\n"
"v_exp_f32 v37, v37\n"
"v_exp_f32 v38, v38\n"
"v_exp_f32 v39, v39\n"
"v_exp_f32 v40, v40\n"
"v_exp_f32 v41, v41\n"
"v_smfmac_f32_32x32x32_bf16 v[112:127], v[52:55], v[32:39], v0\n"
"v_exp_f32 v42, v42\n"
"v_exp_f32 v43, v43\n"
"v_exp_f32 v44, v44\n"
"v_exp_f32 v45, v45\n"
"s_waitcnt lgkmcnt(0)\n"
"s_add_u32 m0, s87, 8320\n"
"s_nop 0\n"
"buffer_load_dwordx4 v2, s[32:35], s88 offen offset:0 sc1 lds\n"
"v_exp_f32 v46, v46\n"
"v_exp_f32 v47, v47\n"
"v_exp_f32 v16, v16\n"
"v_exp_f32 v17, v17\n"
"v_smfmac_f32_32x32x32_bf16 v[112:127], v[56:59], v[40:47], v0\n"
"v_exp_f32 v18, v18\n"
"v_exp_f32 v19, v19\n"
"v_exp_f32 v20, v20\n"
"v_mfma_f32_32x32x16_bf16 v[32:47], v[72:75], v[68:71], 0\n"
"v_exp_f32 v21, v21\n"
"v_exp_f32 v22, v22\n"
"v_exp_f32 v23, v23\n"
"v_exp_f32 v24, v24\n"
"v_exp_f32 v25, v25\n"
"s_waitcnt vmcnt(8)\n"
"v_smfmac_f32_32x32x32_bf16 v[96:111], v[76:79], v[16:23], v0\n"
"v_exp_f32 v26, v26\n"
"s_mov_b64 exec, s[80:81]\n"
"buffer_load_dwordx4 v[52:55], v4, s[32:35], s83 offen offset:1024 sc1\n"
"buffer_load_dwordx4 v[56:59], v4, s[32:35], s83 offen offset:1280 sc1\n"
"s_mov_b64 exec, -1\n"
"v_exp_f32 v27, v27\n"
"v_exp_f32 v28, v28\n"
"v_exp_f32 v29, v29\n"
"v_exp_f32 v30, v30\n"
"v_exp_f32 v31, v31\n"
"v_exp_f32 v32, v32\n"
"v_exp_f32 v33, v33\n"
"buffer_load_dwordx4 v2, s[32:35], s88 offen offset:1024 sc1 lds\n"
"s_waitcnt vmcnt(10)\n"
"v_smfmac_f32_32x32x32_bf16 v[96:111], v[80:83], v[24:31], v0\n"
"v_exp_f32 v34, v34\n"
"v_exp_f32 v35, v35\n"
"v_exp_f32 v36, v36\n"
"s_waitcnt vmcnt(8)\n"
"v_mfma_f32_32x32x16_bf16 v[16:31], v[84:87], v[64:67], 0\n"
"v_exp_f32 v37, v37\n"
"v_exp_f32 v38, v38\n"
"v_exp_f32 v39, v39\n"
"s_add_u32 s83, s83, 4096\n"
"s_and_b32 s83, s83, 0x1ffff\n"
"buffer_load_dwordx4 v[72:75], v4, s[32:35], s83 offen sc1\n"
"v_exp_f32 v40, v40\n"
"v_exp_f32 v41, v41\n"
"v_smfmac_f32_32x32x32_bf16 v[112:127], v[76:79], v[32:39], v0\n"
"v_exp_f32 v42, v42\n"
"v_exp_f32 v43, v43\n"
"v_exp_f32 v44, v44\n"
"v_exp_f32 v45, v45\n"
"buffer_load_dwordx4 v2, s[32:35], s88 offen offset:2048 sc1 lds\n"
"v_exp_f32 v46, v46\n"
"v_exp_f32 v47, v47\n"
"v_exp_f32 v16, v16\n"
"v_exp_f32 v17, v17\n"
"v_smfmac_f32_32x32x32_bf16 v[112:127], v[80:83], v[40:47], v0\n"
"v_exp_f32 v18, v18\n"
"v_exp_f32 v19, v19\n"
"v_exp_f32 v20, v20\n"
"v_mfma_f32_32x32x16_bf16 v[32:47], v[84:87], v[68:71], 0\n"
"v_exp_f32 v21, v21\n"
"v_exp_f32 v22, v22\n"
"v_exp_f32 v23, v23\n"
"v_exp_f32 v24, v24\n"
"v_exp_f32 v25, v25\n"
"s_mov_b64 exec, s[80:81]\n"
"buffer_load_dwordx4 v[76:79], v4, s[32:35], s83 offen offset:1024 sc1\n"
"buffer_load_dwordx4 v[80:83], v4, s[32:35], s83 offen offset:1280 sc1\n"
"s_mov_b64 exec, -1\n"
"s_waitcnt vmcnt(10)\n"
"v_smfmac_f32_32x32x32_bf16 v[96:111], v[88:91], v[16:23], v0\n"
"v_exp_f32 v26, v26\n"
"v_exp_f32 v27, v27\n"
"v_exp_f32 v28, v28\n"
"v_exp_f32 v29, v29\n"
"v_exp_f32 v30, v30\n"
"buffer_load_dwordx4 v2, s[32:35], s88 offen offset:3072 sc1 lds\n"
"s_add_u32 s88, s88, 4096\n"
"v_exp_f32 v31, v31\n"
"v_exp_f32 v32, v32\n"
"v_exp_f32 v33, v33\n"
"s_waitcnt vmcnt(10)\n"
"v_smfmac_f32_32x32x32_bf16 v[96:111], v[92:95], v[24:31], v0\n"
"v_exp_f32 v34, v34\n"
"v_exp_f32 v35, v35\n"
"s_add_u32 s83, s83, 4096\n"
"s_and_b32 s83, s83, 0x1ffff\n"
"buffer_load_dwordx4 v[84:87], v4, s[32:35], s83 offen sc1\n"
"v_exp_f32 v36, v36\n"
"s_waitcnt vmcnt(10)\n"
"v_mfma_f32_32x32x16_bf16 v[16:31], v[48:51], v[64:67], 0\n"
"v_exp_f32 v37, v37\n"
"v_exp_f32 v38, v38\n"
"v_exp_f32 v39, v39\n"
"v_exp_f32 v40, v40\n"
"s_add_u32 m0, s87, 12416\n"
"s_nop 0\n"
"buffer_load_dwordx4 v2, s[32:35], s88 offen offset:0 sc1 lds\n"
"v_exp_f32 v41, v41\n"
"v_smfmac_f32_32x32x32_bf16 v[112:127], v[88:91], v[32:39], v0\n"
"v_exp_f32 v42, v42\n"
"v_exp_f32 v43, v43\n"
"v_exp_f32 v44, v44\n"
"v_exp_f32 v45, v45\n"
"s_lshl_b32 s89, s12, 2\n"
"s_and_b32 s90, s74, 3\n"
"s_add_u32 s89, s89, s90\n"
"s_lshl_b32 s89, s89, 14\n"
"s_lshl_b32 s90, s13, 7\n"
"s_add_u32 s89, s89, s90\n"
"s_lshr_b32 s90, s74, 2\n"
"s_lshl_b32 s90, s90, 6\n"
"s_add_u32 s99, s89, s90\n"
"v_and_b32 v10, 15, v1\n"
"v_lshlrev_b32 v10, 10, v10\n"
"v_and_b32 v11, 0x30, v1\n"
"v_or_b32 v10, v10, v11\n"
"buffer_load_dwordx4 v[60:63], v10, s[8:11], s99 offen\n"
"v_exp_f32 v46, v46\n"
"v_exp_f32 v47, v47\n"
"v_exp_f32 v16, v16\n"
"v_exp_f32 v17, v17\n"
"v_smfmac_f32_32x32x32_bf16 v[112:127], v[92:95], v[40:47], v0\n"
"v_exp_f32 v18, v18\n"
"v_exp_f32 v19, v19\n"
"v_exp_f32 v20, v20\n"
"v_mfma_f32_32x32x16_bf16 v[32:47], v[48:51], v[68:71], 0\n"
"v_exp_f32 v21, v21\n"
"v_exp_f32 v22, v22\n"
"v_exp_f32 v23, v23\n"
"v_exp_f32 v24, v24\n"
"v_exp_f32 v25, v25\n"
"s_mov_b64 exec, s[80:81]\n"
"buffer_load_dwordx4 v[88:91], v4, s[32:35], s83 offen offset:1024 sc1\n"
"buffer_load_dwordx4 v[92:95], v4, s[32:35], s83 offen offset:1280 sc1\n"
"s_mov_b64 exec, -1\n"
"s_waitcnt vmcnt(12)\n"
"v_smfmac_f32_32x32x32_bf16 v[96:111], v[52:55], v[16:23], v0\n"
"v_exp_f32 v26, v26\n"
"v_exp_f32 v27, v27\n"
"v_exp_f32 v28, v28\n"
"v_exp_f32 v29, v29\n"
"v_exp_f32 v30, v30\n"
"buffer_load_dwordx4 v2, s[32:35], s88 offen offset:1024 sc1 lds\n"
"v_exp_f32 v31, v31\n"
"v_exp_f32 v32, v32\n"
"v_exp_f32 v33, v33\n"
"s_waitcnt vmcnt(12)\n"
"v_smfmac_f32_32x32x32_bf16 v[96:111], v[56:59], v[24:31], v0\n"
"v_exp_f32 v34, v34\n"
"v_exp_f32 v35, v35\n"
"s_add_u32 s83, s83, 4096\n"
"s_and_b32 s83, s83, 0x1ffff\n"
"buffer_load_dwordx4 v[48:51], v4, s[32:35], s83 offen sc1\n"
"v_exp_f32 v36, v36\n"
"s_waitcnt vmcnt(11)\n"
"v_mfma_f32_32x32x16_bf16 v[16:31], v[72:75], v[64:67], 0\n"
"v_exp_f32 v37, v37\n"
"v_exp_f32 v38, v38\n"
"v_exp_f32 v39, v39\n"
"v_exp_f32 v40, v40\n"
"buffer_load_dwordx4 v2, s[32:35], s88 offen offset:2048 sc1 lds\n"
"v_exp_f32 v41, v41\n"
"v_smfmac_f32_32x32x32_bf16 v[112:127], v[52:55], v[32:39], v0\n"
"v_exp_f32 v42, v42\n"
"v_exp_f32 v43, v43\n"
"v_exp_f32 v44, v44\n"
"v_exp_f32 v45, v45\n"
"s_lshl_b32 s89, s74, 10\n"
"s_add_u32 s89, s89, 141312\n"
"v_add_u32 v11, s89, v2\n"
"s_lshl_b32 s89, s74, 8\n"
"s_add_u32 s89, s89, 158720\n"
"v_lshlrev_b32 v13, 2, v1\n"
"v_add_u32 v13, s89, v13\n"
"v_exp_f32 v46, v46\n"
"v_exp_f32 v47, v47\n"
"v_exp_f32 v16, v16\n"
"v_exp_f32 v17, v17\n"
"v_smfmac_f32_32x32x32_bf16 v[112:127], v[56:59], v[40:47], v0\n"
"v_exp_f32 v18, v18\n"
"v_exp_f32 v19, v19\n"
"v_exp_f32 v20, v20\n"
"v_mfma_f32_32x32x16_bf16 v[32:47], v[72:75], v[68:71], 0\n"
"v_exp_f32 v21, v21\n"
"v_exp_f32 v22, v22\n"
"v_exp_f32 v23, v23\n"
"v_exp_f32 v24, v24\n"
"s_mov_b64 exec, s[80:81]\n"
"buffer_load_dwordx4 v[52:55], v4, s[32:35], s83 offen offset:1024 sc1\n"
"buffer_load_dwordx4 v[56:59], v4, s[32:35], s83 offen offset:1280 sc1\n"
"s_mov_b64 exec, -1\n"
"v_exp_f32 v25, v25\n"
"s_waitcnt vmcnt(12)\n"
"v_smfmac_f32_32x32x32_bf16 v[96:111], v[76:79], v[16:23], v0\n"
"v_exp_f32 v26, v26\n"
"v_exp_f32 v27, v27\n"
"v_exp_f32 v28, v28\n"
"buffer_load_dwordx4 v2, s[32:35], s88 offen offset:3072 sc1 lds\n"
"s_add_u32 s88, s88, 4096\n"
"v_exp_f32 v29, v29\n"
"v_exp_f32 v30, v30\n"
"v_exp_f32 v31, v31\n"
"v_exp_f32 v32, v32\n"
"v_exp_f32 v33, v33\n"
"s_lshl_b32 s20, s75, 1\n"
"s_lshr_b32 s22, s74, 2\n"
"s_add_u32 s20, s20, s22\n"
"s_lshl_b32 s20, s20, 4\n"
"s_add_u32 s20, s20, s76\n"
"s_lshl_b32 s20, s20, 2\n"
"s_waitcnt vmcnt(12)\n"
"v_smfmac_f32_32x32x32_bf16 v[96:111], v[80:83], v[24:31], v0\n"
"v_exp_f32 v34, v34\n"
"v_exp_f32 v35, v35\n"
"v_exp_f32 v36, v36\n"
"s_waitcnt vmcnt(10)\n"
"v_mfma_f32_32x32x16_bf16 v[16:31], v[84:87], v[64:67], 0\n"
"s_add_u32 s83, s83, 4096\n"
"s_and_b32 s83, s83, 0x1ffff\n"
"buffer_load_dwordx4 v[72:75], v4, s[32:35], s83 offen sc1\n"
"v_exp_f32 v37, v37\n"
"v_exp_f32 v38, v38\n"
"v_exp_f32 v39, v39\n"
"v_exp_f32 v40, v40\n"
"v_exp_f32 v41, v41\n"
"s_and_b32 s21, s74, 3\n"
"s_add_u32 s20, s20, s21\n"
"s_lshl_b32 s20, s20, 9\n"
"s_add_u32 s20, s20, 5242880\n"
"s_lshl_b32 s22, s22, 11\n"
"s_sub_u32 s22, 1024, s22\n"
"v_smfmac_f32_32x32x32_bf16 v[112:127], v[76:79], v[32:39], v0\n"
"v_exp_f32 v42, v42\n"
"v_exp_f32 v43, v43\n"
"v_exp_f32 v44, v44\n"
"v_exp_f32 v45, v45\n"
"s_lshl_b32 s21, s75, 3\n"
"s_add_u32 s21, s21, s76\n"
"s_lshl_b32 s21, s21, 7\n"
"s_add_u32 s21, s21, 163840\n"
"s_cmp_eq_u32 s74, 0\n"
"s_cselect_b32 s21, s21, 2146435072\n"
"v_exp_f32 v46, v46\n"
"v_exp_f32 v47, v47\n"
"s_setprio 0\n"
"v_exp_f32 v16, v16\n"
"v_exp_f32 v17, v17\n"
"v_smfmac_f32_32x32x32_bf16 v[112:127], v[80:83], v[40:47], v0\n"
"v_exp_f32 v18, v18\n"
"v_exp_f32 v19, v19\n"
"v_exp_f32 v20, v20\n"
"v_mfma_f32_32x32x16_bf16 v[32:47], v[84:87], v[68:71], 0\n"
"v_exp_f32 v21, v21\n"
"v_exp_f32 v22, v22\n"
"v_exp_f32 v23, v23\n"
"v_exp_f32 v24, v24\n"
"v_exp_f32 v25, v25\n"
"s_waitcnt vmcnt(8)\n"
"v_smfmac_f32_32x32x32_bf16 v[96:111], v[88:91], v[16:23], v0\n"
"v_exp_f32 v26, v26\n"
"v_exp_f32 v27, v27\n"
"v_exp_f32 v28, v28\n"
"v_exp_f32 v29, v29\n"
"v_exp_f32 v30, v30\n"
"v_exp_f32 v31, v31\n"
"v_exp_f32 v32, v32\n"
"v_exp_f32 v33, v33\n"
"s_waitcnt vmcnt(7)\n"
"v_smfmac_f32_32x32x32_bf16 v[96:111], v[92:95], v[24:31], v0\n"
"v_exp_f32 v34, v34\n"
"v_exp_f32 v35, v35\n"
"v_exp_f32 v36, v36\n"
"s_waitcnt vmcnt(5)\n"
"v_mfma_f32_32x32x16_bf16 v[16:31], v[48:51], v[64:67], 0\n"
"v_exp_f32 v37, v37\n"
"v_exp_f32 v38, v38\n"
"v_exp_f32 v39, v39\n"
"v_exp_f32 v40, v40\n"
"v_exp_f32 v41, v41\n"
"v_smfmac_f32_32x32x32_bf16 v[112:127], v[88:91], v[32:39], v0\n"
"v_exp_f32 v42, v42\n"
"v_exp_f32 v43, v43\n"
"v_exp_f32 v44, v44\n"
"v_exp_f32 v45, v45\n"
"s_mov_b64 exec, s[80:81]\n"
"buffer_load_dwordx4 v[76:79], v4, s[32:35], s83 offen offset:1024 sc1\n"
"buffer_load_dwordx4 v[80:83], v4, s[32:35], s83 offen offset:1280 sc1\n"
"s_mov_b64 exec, -1\n"
"v_exp_f32 v46, v46\n"
"v_exp_f32 v47, v47\n"
"v_exp_f32 v16, v16\n"
"v_exp_f32 v17, v17\n"
"v_smfmac_f32_32x32x32_bf16 v[112:127], v[92:95], v[40:47], v0\n"
"v_exp_f32 v18, v18\n"
"v_exp_f32 v19, v19\n"
"v_exp_f32 v20, v20\n"
"v_mfma_f32_32x32x16_bf16 v[32:47], v[48:51], v[68:71], 0\n"
"v_exp_f32 v21, v21\n"
"v_exp_f32 v22, v22\n"
"v_exp_f32 v23, v23\n"
"v_exp_f32 v24, v24\n"
"v_exp_f32 v25, v25\n"
"s_waitcnt vmcnt(5)\n"
"v_smfmac_f32_32x32x32_bf16 v[96:111], v[52:55], v[16:23], v0\n"
"v_exp_f32 v26, v26\n"
"v_exp_f32 v27, v27\n"
"v_exp_f32 v28, v28\n"
"v_exp_f32 v29, v29\n"
"v_exp_f32 v30, v30\n"
"v_exp_f32 v31, v31\n"
"v_exp_f32 v32, v32\n"
"v_exp_f32 v33, v33\n"
"s_waitcnt vmcnt(4)\n"
"v_smfmac_f32_32x32x32_bf16 v[96:111], v[56:59], v[24:31], v0\n"
"v_exp_f32 v34, v34\n"
"v_exp_f32 v35, v35\n"
"v_exp_f32 v36, v36\n"
"s_waitcnt vmcnt(2)\n"
"v_mfma_f32_32x32x16_bf16 v[16:31], v[72:75], v[64:67], 0\n"
"v_exp_f32 v37, v37\n"
"v_exp_f32 v38, v38\n"
"v_exp_f32 v39, v39\n"
"v_exp_f32 v40, v40\n"
"v_exp_f32 v41, v41\n"
"v_smfmac_f32_32x32x32_bf16 v[112:127], v[52:55], v[32:39], v0\n"
"v_exp_f32 v42, v42\n"
"v_exp_f32 v43, v43\n"
"v_exp_f32 v44, v44\n"
"v_exp_f32 v45, v45\n"
"v_exp_f32 v46, v46\n"
"v_exp_f32 v47, v47\n"
"v_exp_f32 v16, v16\n"
"v_exp_f32 v17, v17\n"
"v_smfmac_f32_32x32x32_bf16 v[112:127], v[56:59], v[40:47], v0\n"
"v_exp_f32 v18, v18\n"
"v_exp_f32 v19, v19\n"
"v_exp_f32 v20, v20\n"
"v_mfma_f32_32x32x16_bf16 v[32:47], v[72:75], v[68:71], 0\n"
"v_exp_f32 v21, v21\n"
"v_exp_f32 v22, v22\n"
"v_exp_f32 v23, v23\n"
"v_exp_f32 v24, v24\n"
"v_exp_f32 v25, v25\n"
"s_waitcnt vmcnt(1)\n"
"v_smfmac_f32_32x32x32_bf16 v[96:111], v[76:79], v[16:23], v0\n"
"v_exp_f32 v26, v26\n"
"v_exp_f32 v27, v27\n"
"v_exp_f32 v28, v28\n"
"v_exp_f32 v29, v29\n"
"v_exp_f32 v30, v30\n"
"v_exp_f32 v31, v31\n"
"v_exp_f32 v32, v32\n"
"v_exp_f32 v33, v33\n"
"s_waitcnt vmcnt(0)\n"
"v_smfmac_f32_32x32x32_bf16 v[96:111], v[80:83], v[24:31], v0\n"
"v_exp_f32 v34, v34\n"
"v_exp_f32 v35, v35\n"
"v_exp_f32 v36, v36\n"
"v_exp_f32 v37, v37\n"
"v_exp_f32 v38, v38\n"
"v_exp_f32 v39, v39\n"
"v_exp_f32 v40, v40\n"
"v_exp_f32 v41, v41\n"
"v_smfmac_f32_32x32x32_bf16 v[112:127], v[76:79], v[32:39], v0\n"
"v_exp_f32 v42, v42\n"
"v_exp_f32 v43, v43\n"
"v_exp_f32 v44, v44\n"
"v_exp_f32 v45, v45\n"
"v_exp_f32 v46, v46\n"
"v_exp_f32 v47, v47\n"
"s_nop 1\n"
"v_smfmac_f32_32x32x32_bf16 v[112:127], v[80:83], v[40:47], v0\n"
"v_bfe_u32 v14, v1, 4, 1\n"
"v_lshlrev_b32 v14, 14, v14\n"
"v_lshrrev_b32 v15, 5, v1\n"
"v_lshl_or_b32 v14, v15, 8, v14\n"
"v_and_b32 v15, 0xf0, v2\n"
"v_or_b32 v14, v14, v15\n"
"v_add_u32 v12, s22, v13\n"
"s_lshl_b32 s22, s22, 2\n"
"v_add_u32 v3, s22, v11\n"
"v_mov_b32 v6, s72\n"
"v_mov_b32 v7, s73\n"
"s_nop 0\n"
"ds_write_b128 v11, v[112:115]\n"
"ds_write_b128 v11, v[116:119] offset:8192\n"
"ds_write_b32 v13, v120\n"
"s_cmp_ge_u32 s74, 4\n"
"s_cbranch_scc1 L83_%=\n"
"s_lshl_b32 s84, s74, 12\n"
"s_add_u32 s84, s84, 49152\n"
"s_mul_i32 s92, s76, 3200\n"
"s_mul_i32 s93, s74, 320\n"
"s_add_u32 s92, s92, s93\n"
"s_add_u32 s92, s92, 1024\n"
"v_mov_b32 v9, s92\n"
"s_mul_i32 s92, s13, 3200\n"
"s_mul_i32 s93, s74, 224\n"
"s_add_u32 s92, s92, s93\n"
"s_add_u32 s92, s92, 1984\n"
"v_mov_b32 v5, s92\n"
"v_cmp_gt_u32 vcc, 20, v1\n"
"s_mov_b32 s86, 65536\n"
"s_nop 0\n"
"v_cndmask_b32 v9, v5, v9, vcc\n"
"v_min_u32 v5, 528, v2\n"
"v_add_u32 v9, v9, v5\n"
"buffer_load_dwordx4 v[16:19], v2, s[32:35], s84 offen offset:0 sc1\n"
"buffer_load_dwordx4 v[20:23], v2, s[32:35], s84 offen offset:1024 sc1\n"
"buffer_load_dwordx4 v[24:27], v2, s[32:35], s84 offen offset:2048 sc1\n"
"buffer_load_dwordx4 v[28:31], v2, s[32:35], s84 offen offset:3072 sc1\n"
"buffer_load_dwordx4 v[68:71], v9, s[32:35], s86 offen sc1\n"
"s_waitcnt vmcnt(4)\n"
"v_cmp_ne_u64 vcc, s[72:73], v[16:17]\n"
"v_cmp_ne_u64 s[92:93], s[72:73], v[18:19]\n"
"s_nop 0\n"
"s_or_b64 vcc, vcc, s[92:93]\n"
"s_waitcnt vmcnt(3)\n"
"v_cmp_ne_u64 s[92:93], s[72:73], v[20:21]\n"
"s_nop 0\n"
"s_or_b64 vcc, vcc, s[92:93]\n"
"v_cmp_ne_u64 s[92:93], s[72:73], v[22:23]\n"
"s_nop 0\n"
"s_or_b64 vcc, vcc, s[92:93]\n"
"s_waitcnt vmcnt(2)\n"
"v_cmp_ne_u64 s[92:93], s[72:73], v[24:25]\n"
"s_nop 0\n"
"s_or_b64 vcc, vcc, s[92:93]\n"
"v_cmp_ne_u64 s[92:93], s[72:73], v[26:27]\n"
"s_nop 0\n"
"s_or_b64 vcc, vcc, s[92:93]\n"
"s_waitcnt vmcnt(1)\n"
"v_cmp_ne_u64 s[92:93], s[72:73], v[28:29]\n"
"s_nop 0\n"
"s_or_b64 vcc, vcc, s[92:93]\n"
"v_cmp_ne_u64 s[92:93], s[72:73], v[30:31]\n"
"s_nop 0\n"
"s_or_b64 vcc, vcc, s[92:93]\n"
"s_waitcnt vmcnt(0)\n"
"v_cmp_ne_u64 s[92:93], s[72:73], v[68:69]\n"
"s_nop 0\n"
"s_or_b64 vcc, vcc, s[92:93]\n"
"v_cmp_ne_u64 s[92:93], s[72:73], v[70:71]\n"
"s_nop 0\n"
"s_or_b64 vcc, vcc, s[92:93]\n"
"s_cbranch_vccnz RT84_%=\n"
"BK85_%=:\n"
"s_waitcnt lgkmcnt(0)\n"
"s_nop 3\n"
"L83_%=:\n"
"s_waitcnt lgkmcnt(0)\n"
"s_barrier\n"
"ds_read_b32 v24, v12\n"
"ds_read_b128 v[16:19], v3\n"
"ds_read_b128 v[20:23], v3 offset:8192\n"
"s_mov_b64 exec, 0xffff\n"
"buffer_store_dwordx2 v[6:7], v8, s[32:35], s21 offen sc1\n"
"s_mov_b64 exec, -1\n"
"s_waitcnt lgkmcnt(2)\n"
"v_add_f32 v104, v104, v24\n"
"v_cmp_nle_f32 vcc, 0x1f800000, v104\n"
"v_mov_b32 v15, 0x5f800000\n"
"v_cmp_nge_f32 s[92:93], v15, v104\n"
"s_waitcnt lgkmcnt(1)\n"
"v_pk_add_f32 v[96:97], v[96:97], v[16:17]\n"
"v_pk_add_f32 v[98:99], v[98:99], v[18:19]\n"
"s_waitcnt lgkmcnt(0)\n"
"v_pk_add_f32 v[100:101], v[100:101], v[20:21]\n"
"v_pk_add_f32 v[102:103], v[102:103], v[22:23]\n"
"s_or_b64 vcc, vcc, s[92:93]\n"
"s_cbranch_vccnz FB88_%=\n"
"BK89_%=:\n"
"v_rcp_f32 v104, v104\n"
"s_nop 0\n"
"v_pk_mul_f32 v[96:97], v[96:97], v[104:105] op_sel_hi:[1,0]\n"
"v_pk_mul_f32 v[98:99], v[98:99], v[104:105] op_sel_hi:[1,0]\n"
"v_pk_mul_f32 v[100:101], v[100:101], v[104:105] op_sel_hi:[1,0]\n"
"v_pk_mul_f32 v[102:103], v[102:103], v[104:105] op_sel_hi:[1,0]\n"
"v_cvt_pk_bf16_f32 v96, v96, v97\n"
"v_cvt_pk_bf16_f32 v97, v98, v99\n"
"v_cvt_pk_bf16_f32 v98, v100, v101\n"
"v_cvt_pk_bf16_f32 v99, v102, v103\n"
"buffer_store_dwordx4 v[96:99], v14, s[32:35], s20 offen sc1\n"
"s_lshr_b32 s84, s74, 2\n"
"s_and_b32 s86, s74, 3\n"
"s_mul_i32 s89, s84, 33280\n"
"v_add_u32 v4, s89, v2\n"
"s_min_u32 s69, s74, 6\n"
"s_mul_i32 s89, s76, 7\n"
"s_add_u32 s89, s89, s69\n"
"s_lshl_b32 s89, s89, 11\n"
"s_add_u32 s90, s89, 262144\n"
"s_add_u32 s89, s89, 376832\n"
"s_barrier\n"
"s_cmp_lg_u32 s74, 0\n"
"s_cbranch_scc1 L92_%=\n"
"v_mov_b32 v8, s72\n"
"v_mov_b32 v9, s73\n"
"v_lshlrev_b32 v5, 7, v1\n"
"s_lshl_b32 s68, s12, 10\n"
"s_add_u32 s68, s68, 163840\n"
"s_xor_b32 s66, s14, 1\n"
"s_lshl_b32 s66, s66, s76\n"
"s_xor_b32 s66, s66, 0xff\n"
"s_mov_b32 s67, 0\n"
"s_mov_b64 exec, s[66:67]\n"
"buffer_load_dwordx2 v[8:9], v5, s[32:35], s68 offen sc1\n"
"s_mov_b64 exec, -1\n"
"s_nop 3\n"
"L92_%=:\n"
"buffer_load_dwordx4 v[32:35], v2, s[32:35], s89 offen offset:0 sc1\n"
"buffer_load_dwordx4 v[36:39], v2, s[32:35], s89 offen offset:1024 sc1\n"
"buffer_load_dwordx4 v[40:43], v2, s[32:35], s90 offen offset:0 sc1\n"
"buffer_load_dwordx4 v[44:47], v2, s[32:35], s90 offen offset:1024 sc1\n"
"ds_read_b128 v[80:83], v4 offset:0\n"
"ds_read_b128 v[84:87], v4 offset:1024\n"
"ds_read_b128 v[88:91], v4 offset:2048\n"
"ds_read_b128 v[92:95], v4 offset:3072\n"
"s_lshl_b32 s82, s12, 2\n"
"s_add_u32 s82, s82, s86\n"
"s_lshl_b32 s82, s82, 14\n"
"s_lshr_b32 s79, s76, 2\n"
"s_lshr_b32 s87, s13, 2\n"
"s_lshl_b32 s87, s87, 1\n"
"s_add_u32 s87, s87, s84\n"
"s_lshl_b32 s87, s87, 12\n"
"s_add_u32 s87, s87, s82\n"
"s_add_u32 s87, s87, 5242880\n"
"s_xor_b32 s88, s87, 8192\n"
"s_mul_i32 s83, s76, 409600\n"
"s_add_u32 s83, s83, 17825792\n"
"ds_read_b128 v[96:99], v4 offset:4096\n"
"ds_read_b128 v[100:103], v4 offset:5120\n"
"ds_read_b128 v[104:107], v4 offset:6144\n"
"ds_read_b128 v[108:111], v4 offset:7168\n"
"ds_read_b128 v[112:115], v4 offset:8320\n"
"ds_read_b128 v[116:119], v4 offset:9344\n"
"ds_read_b128 v[120:123], v4 offset:10368\n"
"ds_read_b128 v[124:127], v4 offset:11392\n"
"s_xor_b32 s89, s74, 1\n"
"s_or_b32 s89, s98, s89\n"
"s_cbranch_scc1 L93_%=\n"
"v_mov_b32 v8, s72\n"
"v_mov_b32 v9, s73\n"
"s_mov_b64 exec, 1\n"
"buffer_store_dwordx2 v[8:9], v2, s[32:35], 0 offen sc1\n"
"s_mov_b64 exec, -1\n"
"s_waitcnt lgkmcnt(0)\n"
"s_nop 3\n"
"L93_%=:\n"
"v_lshrrev_b32 v6, 4, v1\n"
"v_lshlrev_b32 v6, 5, v6\n"
"s_lshl_b32 s89, s84, 7\n"
"s_add_u32 s89, s89, 157696\n"
"v_add_u32 v6, s89, v6\n"
"ds_read_b128 v[48:51], v6\n"
"ds_read_b128 v[52:55], v6 offset:16\n"
"s_waitcnt vmcnt(4)\n"
"s_lshl_b32 s69, s69, 6\n"
"s_add_u32 s69, s69, 160768\n"
"v_and_b32 v7, 7, v1\n"
"v_lshlrev_b32 v7, 3, v7\n"
"v_lshrrev_b32 v13, 5, v1\n"
"v_lshl_or_b32 v7, v13, 2, v7\n"
"v_add_u32 v7, s69, v7\n"
"s_waitcnt vmcnt(2)\n"
"v_pk_add_f32 v[32:33], v[32:33], v[36:37]\n"
"s_waitcnt vmcnt(0)\n"
"v_pk_add_f32 v[40:41], v[40:41], v[44:45]\n"
"v_pk_add_f32 v[34:35], v[34:35], v[38:39]\n"
"v_pk_add_f32 v[42:43], v[42:43], v[46:47]\n"
"s_nop 0\n"
"v_permlane32_swap_b32 v32, v34\n"
"v_permlane32_swap_b32 v40, v42\n"
"v_permlane32_swap_b32 v33, v35\n"
"v_permlane32_swap_b32 v41, v43\n"
"v_pk_add_f32 v[32:33], v[32:33], v[34:35]\n"
"v_pk_add_f32 v[40:41], v[40:41], v[42:43]\n"
"v_mov_b64 v[34:35], v[32:33]\n"
"v_mov_b64 v[42:43], v[40:41]\n"
"s_nop 0\n"
"v_permlane16_swap_b32 v32, v34\n"
"v_permlane16_swap_b32 v40, v42\n"
"v_permlane16_swap_b32 v33, v35\n"
"v_permlane16_swap_b32 v41, v43\n"
"v_pk_add_f32 v[32:33], v[32:33], v[34:35]\n"
"v_pk_add_f32 v[40:41], v[40:41], v[42:43]\n"
"s_nop 0\n"
"v_add_f32_dpp v32, v32, v32 row_ror:8 row_mask:0xf bank_mask:0xf\n"
"v_add_f32_dpp v40, v40, v40 row_ror:8 row_mask:0xf bank_mask:0xf\n"
"v_add_f32_dpp v33, v33, v33 row_ror:8 row_mask:0xf bank_mask:0xf\n"
"v_add_f32_dpp v41, v41, v41 row_ror:8 row_mask:0xf bank_mask:0xf\n"
"ds_write2_b32 v7, v32, v33 offset0:0 offset1:112\n"
"v_add_u32 v7, 896, v7\n"
"ds_write2_b32 v7, v40, v41 offset0:0 offset1:112\n"
"s_cmp_lg_u32 s74, 0\n"
"s_cbranch_scc1 L94_%=\n"
"s_waitcnt vmcnt(0)\n"
"v_cmp_ne_u64 vcc, s[72:73], v[8:9]\n"
"s_nop 0\n"
"s_cbranch_vccnz RT95_%=\n"
"BK96_%=:\n"
"s_waitcnt lgkmcnt(0)\n"
"s_nop 3\n"
"L94_%=:\n"
"s_barrier\n"
"buffer_load_dwordx4 v[16:19], v2, s[32:35], s87 offen offset:0 sc1\n"
"buffer_load_dwordx4 v[20:23], v2, s[32:35], s87 offen offset:1024 sc1\n"
"buffer_load_dwordx4 v[24:27], v2, s[32:35], s87 offen offset:2048 sc1\n"
"buffer_load_dwordx4 v[28:31], v2, s[32:35], s87 offen offset:3072 sc1\n"
"s_waitcnt vmcnt(2)\n"
"v_max3_u32 v0, v17, v19, v21\n"
"v_max_u32 v0, v23, v0\n"
"v_cmp_eq_u32 vcc, -1, v0\n"
"s_nop 0\n"
"s_cbranch_vccnz RT99_%=\n"
"BK100_%=:\n"
"buffer_load_dwordx4 v[32:35], v2, s[32:35], s88 offen offset:0 sc1\n"
"buffer_load_dwordx4 v[36:39], v2, s[32:35], s88 offen offset:1024 sc1\n"
"s_waitcnt lgkmcnt(13)\n"
"v_mfma_f32_16x16x32_bf16 v[72:75], v[88:91], v[16:19], 0\n"
"s_waitcnt lgkmcnt(12)\n"
"v_mfma_f32_16x16x32_bf16 v[76:79], v[92:95], v[16:19], 0\n"
"v_mfma_f32_16x16x32_bf16 v[64:67], v[80:83], v[16:19], 0\n"
"v_mfma_f32_16x16x32_bf16 v[68:71], v[84:87], v[16:19], 0\n"
"ds_read_b128 v[80:83], v4 offset:12416\n"
"ds_read_b128 v[84:87], v4 offset:13440\n"
"ds_read_b128 v[88:91], v4 offset:14464\n"
"ds_read_b128 v[92:95], v4 offset:15488\n"
"s_waitcnt lgkmcnt(13)\n"
"v_mfma_f32_16x16x32_bf16 v[72:75], v[104:107], v[20:23], v[72:75]\n"
"s_waitcnt lgkmcnt(12)\n"
"v_mfma_f32_16x16x32_bf16 v[76:79], v[108:111], v[20:23], v[76:79]\n"
"v_mfma_f32_16x16x32_bf16 v[64:67], v[96:99], v[20:23], v[64:67]\n"
"v_mfma_f32_16x16x32_bf16 v[68:71], v[100:103], v[20:23], v[68:71]\n"
"ds_read_b128 v[96:99], v4 offset:16640\n"
"ds_read_b128 v[100:103], v4 offset:17664\n"
"ds_read_b128 v[104:107], v4 offset:18688\n"
"ds_read_b128 v[108:111], v4 offset:19712\n"
"s_waitcnt vmcnt(2)\n"
"v_max3_u32 v0, v25, v27, v29\n"
"v_max_u32 v0, v31, v0\n"
"v_cmp_eq_u32 vcc, -1, v0\n"
"s_nop 0\n"
"s_cbranch_vccnz RT104_%=\n"
"BK105_%=:\n"
"buffer_load_dwordx4 v[40:43], v2, s[32:35], s88 offen offset:2048 sc1\n"
"buffer_load_dwordx4 v[44:47], v2, s[32:35], s88 offen offset:3072 sc1\n"
"s_waitcnt lgkmcnt(13)\n"
"v_mfma_f32_16x16x32_bf16 v[72:75], v[120:123], v[24:27], v[72:75]\n"
"s_waitcnt lgkmcnt(12)\n"
"v_mfma_f32_16x16x32_bf16 v[76:79], v[124:127], v[24:27], v[76:79]\n"
"v_mfma_f32_16x16x32_bf16 v[64:67], v[112:115], v[24:27], v[64:67]\n"
"v_mfma_f32_16x16x32_bf16 v[68:71], v[116:119], v[24:27], v[68:71]\n"
"ds_read_b128 v[112:115], v4 offset:20736\n"
"ds_read_b128 v[116:119], v4 offset:21760\n"
"ds_read_b128 v[120:123], v4 offset:22784\n"
"ds_read_b128 v[124:127], v4 offset:23808\n"
"s_waitcnt lgkmcnt(9)\n"
"v_mfma_f32_16x16x32_bf16 v[72:75], v[88:91], v[28:31], v[72:75]\n"
"s_waitcnt lgkmcnt(8)\n"
"v_mfma_f32_16x16x32_bf16 v[76:79], v[92:95], v[28:31], v[76:79]\n"
"v_mfma_f32_16x16x32_bf16 v[64:67], v[80:83], v[28:31], v[64:67]\n"
"v_mfma_f32_16x16x32_bf16 v[68:71], v[84:87], v[28:31], v[68:71]\n"
"ds_read_b128 v[80:83], v4 offset:24960\n"
"ds_read_b128 v[84:87], v4 offset:25984\n"
"ds_read_b128 v[88:91], v4 offset:27008\n"
"ds_read_b128 v[92:95], v4 offset:28032\n"
"s_waitcnt vmcnt(2)\n"
"v_max3_u32 v0, v33, v35, v37\n"
"v_max_u32 v0, v39, v0\n"
"v_cmp_eq_u32 vcc, -1, v0\n"
"s_nop 0\n"
"s_cbranch_vccnz RT109_%=\n"
"BK110_%=:\n"
"s_waitcnt lgkmcnt(9)\n"
"v_mfma_f32_16x16x32_bf16 v[72:75], v[104:107], v[32:35], v[72:75]\n"
"s_waitcnt lgkmcnt(8)\n"
"v_mfma_f32_16x16x32_bf16 v[76:79], v[108:111], v[32:35], v[76:79]\n"
"v_mfma_f32_16x16x32_bf16 v[64:67], v[96:99], v[32:35], v[64:67]\n"
"v_mfma_f32_16x16x32_bf16 v[68:71], v[100:103], v[32:35], v[68:71]\n"
"ds_read_b128 v[96:99], v4 offset:29056\n"
"ds_read_b128 v[100:103], v4 offset:30080\n"
"ds_read_b128 v[104:107], v4 offset:31104\n"
"ds_read_b128 v[108:111], v4 offset:32128\n"
"s_waitcnt lgkmcnt(9)\n"
"v_mfma_f32_16x16x32_bf16 v[72:75], v[120:123], v[36:39], v[72:75]\n"
"s_waitcnt lgkmcnt(8)\n"
"v_mfma_f32_16x16x32_bf16 v[76:79], v[124:127], v[36:39], v[76:79]\n"
"v_mfma_f32_16x16x32_bf16 v[64:67], v[112:115], v[36:39], v[64:67]\n"
"v_mfma_f32_16x16x32_bf16 v[68:71], v[116:119], v[36:39], v[68:71]\n"
"s_waitcnt vmcnt(0)\n"
"v_max3_u32 v0, v41, v43, v45\n"
"v_max_u32 v0, v47, v0\n"
"v_cmp_eq_u32 vcc, -1, v0\n"
"s_nop 0\n"
"s_cbranch_vccnz RT114_%=\n"
"BK115_%=:\n"
"s_waitcnt lgkmcnt(5)\n"
"v_mfma_f32_16x16x32_bf16 v[72:75], v[88:91], v[40:43], v[72:75]\n"
"s_waitcnt lgkmcnt(4)\n"
"v_mfma_f32_16x16x32_bf16 v[76:79], v[92:95], v[40:43], v[76:79]\n"
"v_mfma_f32_16x16x32_bf16 v[64:67], v[80:83], v[40:43], v[64:67]\n"
"v_mfma_f32_16x16x32_bf16 v[68:71], v[84:87], v[40:43], v[68:71]\n"
"s_waitcnt lgkmcnt(1)\n"
"v_mfma_f32_16x16x32_bf16 v[72:75], v[104:107], v[44:47], v[72:75]\n"
"s_waitcnt lgkmcnt(0)\n"
"v_mfma_f32_16x16x32_bf16 v[76:79], v[108:111], v[44:47], v[76:79]\n"
"v_mfma_f32_16x16x32_bf16 v[64:67], v[96:99], v[44:47], v[64:67]\n"
"v_mfma_f32_16x16x32_bf16 v[68:71], v[100:103], v[44:47], v[68:71]\n"
"ds_read_b128 v[80:83], v6 offset:256\n"
"ds_read_b128 v[84:87], v6 offset:272\n"
"v_lshlrev_b32 v88, 16, v60\n"
"v_and_b32 v89, 0xffff0000, v60\n"
"v_lshlrev_b32 v90, 16, v61\n"
"v_and_b32 v91, 0xffff0000, v61\n"
"v_lshlrev_b32 v92, 16, v62\n"
"v_and_b32 v93, 0xffff0000, v62\n"
"v_lshlrev_b32 v94, 16, v63\n"
"v_and_b32 v95, 0xffff0000, v63\n"
"ds_write_b128 v3, v[72:75]\n"
"ds_write_b128 v3, v[76:79] offset:8192\n"
"s_lshl_b32 s89, s13, 1\n"
"s_add_u32 s89, s89, s84\n"
"s_lshl_b32 s87, s89, 10\n"
"s_add_u32 s87, s87, s82\n"
"s_add_u32 s87, s87, 7340032\n"
"s_lshr_b32 s88, s82, 3\n"
"s_lshl_b32 s89, s89, 7\n"
"s_add_u32 s88, s88, s89\n"
"s_add_u32 s88, s88, 13107200\n"
"v_and_b32 v7, 15, v1\n"
"v_lshlrev_b32 v7, 3, v7\n"
"v_lshrrev_b32 v8, 5, v1\n"
"v_lshl_or_b32 v7, v8, 2, v7\n"
"s_waitcnt lgkmcnt(0)\n"
"s_barrier\n"
"ds_read_b128 v[112:115], v11\n"
"ds_read_b128 v[116:119], v11 offset:8192\n"
"s_waitcnt lgkmcnt(1)\n"
"v_pk_add_f32 v[64:65], v[64:65], v[112:113]\n"
"v_pk_add_f32 v[66:67], v[66:67], v[114:115]\n"
"s_waitcnt lgkmcnt(0)\n"
"v_pk_add_f32 v[68:69], v[68:69], v[116:117]\n"
"v_pk_add_f32 v[70:71], v[70:71], v[118:119]\n"
"v_pk_fma_f32 v[64:65], v[64:65], v[48:49], v[88:89]\n"
"v_pk_fma_f32 v[66:67], v[66:67], v[50:51], v[90:91]\n"
"v_pk_fma_f32 v[68:69], v[68:69], v[52:53], v[92:93]\n"
"v_pk_fma_f32 v[70:71], v[70:71], v[54:55], v[94:95]\n"
"v_pk_mul_f32 v[80:81], v[80:81], v[64:65]\n"
"v_pk_mul_f32 v[82:83], v[82:83], v[66:67]\n"
"v_pk_mul_f32 v[84:85], v[84:85], v[68:69]\n"
"v_pk_mul_f32 v[86:87], v[86:87], v[70:71]\n"
"v_cvt_pk_bf16_f32 v80, v80, v81\n"
"v_cvt_pk_bf16_f32 v81, v82, v83\n"
"v_cvt_pk_bf16_f32 v82, v84, v85\n"
"v_cvt_pk_bf16_f32 v83, v86, v87\n"
"buffer_store_dwordx4 v[80:83], v2, s[32:35], s87 offen sc1\n"
"v_pk_mul_f32 v[112:113], v[64:65], v[64:65]\n"
"v_pk_add_f32 v[114:115], v[64:65], v[66:67]\n"
"v_pk_fma_f32 v[112:113], v[66:67], v[66:67], v[112:113]\n"
"v_pk_fma_f32 v[112:113], v[68:69], v[68:69], v[112:113]\n"
"v_pk_fma_f32 v[112:113], v[70:71], v[70:71], v[112:113]\n"
"v_pk_add_f32 v[114:115], v[114:115], v[68:69]\n"
"v_pk_add_f32 v[114:115], v[114:115], v[70:71]\n"
"v_add_f32 v112, v112, v113\n"
"v_add_f32 v114, v114, v115\n"
"v_cvt_pk_bf16_f32 v12, v64, v65\n"
"v_cvt_pk_bf16_f32 v13, v66, v67\n"
"v_permlane32_swap_b32 v114, v112\n"
"v_add_f32 v112, v112, v114\n"
"v_mov_b32 v113, v112\n"
"v_cvt_pk_bf16_f32 v14, v68, v69\n"
"v_cvt_pk_bf16_f32 v15, v70, v71\n"
"v_permlane16_swap_b32 v112, v113\n"
"v_add_f32 v112, v112, v113\n"
"s_mov_b64 exec, s[80:81]\n"
"buffer_store_dword v112, v7, s[32:35], s88 offen sc1\n"
"s_mov_b64 exec, -1\n"
"s_barrier\n"
"s_mul_i32 s95, s74, 4096\n"
"s_add_u32 m0, s95, 0\n"
"s_add_u32 s95, s95, s83\n"
"s_add_u32 s95, s95, 131072\n"
"buffer_load_dwordx4 v2, s[32:35], s95 offen offset:0 sc1 lds\n"
"buffer_load_dwordx4 v2, s[32:35], s95 offen offset:1024 sc1 lds\n"
"buffer_load_dwordx4 v2, s[32:35], s95 offen offset:2048 sc1 lds\n"
"buffer_load_dwordx4 v2, s[32:35], s95 offen offset:3072 sc1 lds\n"
"v_add_u32 v3, 0x10000, v2\n"
"v_add_u32 v4, 0x20000, v2\n"
"s_lshl_b32 s89, s74, 7\n"
"s_add_u32 s89, s89, 162560\n"
"v_and_b32 v5, 7, v1\n"
"v_lshlrev_b32 v5, 3, v5\n"
"v_lshrrev_b32 v8, 5, v1\n"
"v_lshl_or_b32 v5, v8, 2, v5\n"
"v_add_u32 v5, s89, v5\n"
"v_and_b32 v6, 0x30, v1\n"
"v_add_u32 v6, s89, v6\n"
"v_and_b32 v7, 15, v1\n"
"v_lshlrev_b32 v7, 2, v7\n"
"v_add_u32 v7, s89, v7\n"
"s_lshl_b32 s89, s14, 18\n"
"s_xor_b32 s82, s82, s89\n"
"s_mul_i32 s89, s14, 0x8400\n"
"s_xor_b32 s88, s88, s89\n"
"s_lshl_b32 s87, s79, 13\n"
"s_add_u32 s87, s87, s82\n"
"s_add_u32 s87, s87, 7340032\n"
"s_xor_b32 s85, s87, 8192\n"
"s_lshr_b32 s77, s82, 3\n"
"s_add_u32 s77, s77, 13107200\n"
"s_mov_b32 s2, 0xbdd2d3e8\n"
"s_lshl_b32 s20, s86, 12\n"
"s_add_u32 s20, s20, 49152\n"
"s_mul_i32 s21, s86, 6144\n"
"s_add_u32 s21, s21, 133120\n"
"v_add_u32 v8, s20, v2\n"
"v_add_u32 v9, s21, v2\n"
"s_cmp_lg_u32 s84, 0\n"
"s_cbranch_scc1 L118_%=\n"
"ds_read_b128 v[108:111], v3 offset:1024\n"
"ds_read_b128 v[112:115], v3 offset:2048\n"
"ds_read_b128 v[116:119], v3 offset:3072\n"
"ds_read_b128 v[120:123], v3 offset:4096\n"
"ds_read_b128 v[124:127], v3 offset:8192\n"
"ds_read_b128 v[96:99], v3 offset:9344\n"
"ds_read_b128 v[100:103], v3 offset:10368\n"
"ds_read_b128 v[104:107], v3 offset:11392\n"
"s_waitcnt vmcnt(5)\n"
"buffer_load_dwordx4 v[16:19], v2, s[32:35], s87 offen offset:0 sc1\n"
"buffer_load_dwordx4 v[20:23], v2, s[32:35], s87 offen offset:1024 sc1\n"
"buffer_load_dwordx4 v[24:27], v2, s[32:35], s87 offen offset:2048 sc1\n"
"buffer_load_dwordx4 v[28:31], v2, s[32:35], s87 offen offset:3072 sc1\n"
"s_waitcnt vmcnt(2)\n"
"v_max3_u32 v0, v17, v19, v21\n"
"v_max_u32 v0, v23, v0\n"
"v_cmp_eq_u32 vcc, -1, v0\n"
"s_nop 0\n"
"s_cbranch_vccnz RT120_%=\n"
"BK121_%=:\n"
"s_waitcnt lgkmcnt(7)\n"
"v_mfma_f32_16x16x32_bf16 v[80:83], v[16:19], v[108:111], 0\n"
"ds_read_b128 v[108:111], v3 offset:15488\n"
"s_waitcnt lgkmcnt(7)\n"
"v_mfma_f32_16x16x32_bf16 v[84:87], v[16:19], v[112:115], 0\n"
"ds_read_b128 v[112:115], v3 offset:16512\n"
"s_add_u32 s94, s87, 4096\n"
"buffer_load_dwordx4 v[32:35], v2, s[32:35], s94 offen offset:0 sc1\n"
"s_waitcnt lgkmcnt(7)\n"
"v_mfma_f32_16x16x32_bf16 v[88:91], v[16:19], v[116:119], 0\n"
"ds_read_b128 v[116:119], v3 offset:17664\n"
"s_waitcnt lgkmcnt(7)\n"
"v_mfma_f32_16x16x32_bf16 v[92:95], v[16:19], v[120:123], 0\n"
"ds_read_b128 v[120:123], v3 offset:18688\n"
"s_add_u32 s94, s87, 4096\n"
"buffer_load_dwordx4 v[36:39], v2, s[32:35], s94 offen offset:1024 sc1\n"
"s_waitcnt lgkmcnt(7)\n"
"v_mfma_f32_16x16x32_bf16 v[80:83], v[20:23], v[124:127], v[80:83]\n"
"ds_read_b128 v[124:127], v3 offset:22784\n"
"s_waitcnt lgkmcnt(7)\n"
"v_mfma_f32_16x16x32_bf16 v[84:87], v[20:23], v[96:99], v[84:87]\n"
"ds_read_b128 v[96:99], v3 offset:23808\n"
"s_add_u32 m0, s20, 0\n"
"s_add_u32 s95, s87, 6144\n"
"buffer_load_dwordx4 v2, s[32:35], s95 offen sc1 lds\n"
"s_waitcnt lgkmcnt(7)\n"
"v_mfma_f32_16x16x32_bf16 v[88:91], v[20:23], v[100:103], v[88:91]\n"
"ds_read_b128 v[100:103], v3 offset:24832\n"
"s_waitcnt lgkmcnt(7)\n"
"v_mfma_f32_16x16x32_bf16 v[92:95], v[20:23], v[104:107], v[92:95]\n"
"ds_read_b128 v[104:107], v3 offset:25984\n"
"s_add_u32 m0, s20, 2048\n"
"s_add_u32 s95, s85, 0\n"
"buffer_load_dwordx4 v2, s[32:35], s95 offen sc1 lds\n"
"s_waitcnt vmcnt(4)\n"
"v_max3_u32 v0, v25, v27, v29\n"
"v_max_u32 v0, v31, v0\n"
"v_cmp_eq_u32 vcc, -1, v0\n"
"s_nop 0\n"
"s_cbranch_vccnz RT125_%=\n"
"BK126_%=:\n"
"s_waitcnt lgkmcnt(7)\n"
"v_mfma_f32_16x16x32_bf16 v[80:83], v[24:27], v[108:111], v[80:83]\n"
"ds_read_b128 v[108:111], v3 offset:30080\n"
"s_waitcnt lgkmcnt(7)\n"
"v_mfma_f32_16x16x32_bf16 v[84:87], v[24:27], v[112:115], v[84:87]\n"
"ds_read_b128 v[112:115], v3 offset:31104\n"
"buffer_load_dwordx4 v[72:75], v2, s[32:35], s77 offen offset:0 sc1\n"
"s_waitcnt lgkmcnt(7)\n"
"v_mfma_f32_16x16x32_bf16 v[88:91], v[24:27], v[116:119], v[88:91]\n"
"ds_read_b128 v[116:119], v3 offset:32128\n"
"s_waitcnt lgkmcnt(7)\n"
"v_mfma_f32_16x16x32_bf16 v[92:95], v[24:27], v[120:123], v[92:95]\n"
"ds_read_b128 v[120:123], v3 offset:33152\n"
"buffer_load_dwordx4 v[76:79], v2, s[32:35], s77 offen offset:1024 sc1\n"
"s_waitcnt lgkmcnt(7)\n"
"v_mfma_f32_16x16x32_bf16 v[80:83], v[28:31], v[124:127], v[80:83]\n"
"ds_read_b128 v[124:127], v3 offset:37376\n"
"s_waitcnt lgkmcnt(7)\n"
"v_mfma_f32_16x16x32_bf16 v[84:87], v[28:31], v[96:99], v[84:87]\n"
"ds_read_b128 v[96:99], v3 offset:38400\n"
"s_add_u32 m0, s21, 0\n"
"s_add_u32 s95, s85, 2048\n"
"buffer_load_dwordx4 v2, s[32:35], s95 offen sc1 lds\n"
"s_waitcnt lgkmcnt(7)\n"
"v_mfma_f32_16x16x32_bf16 v[88:91], v[28:31], v[100:103], v[88:91]\n"
"ds_read_b128 v[100:103], v3 offset:39424\n"
"s_waitcnt lgkmcnt(7)\n"
"v_mfma_f32_16x16x32_bf16 v[92:95], v[28:31], v[104:107], v[92:95]\n"
"ds_read_b128 v[104:107], v3 offset:40448\n"
"s_add_u32 m0, s21, 2048\n"
"s_add_u32 s95, s85, 4096\n"
"buffer_load_dwordx4 v2, s[32:35], s95 offen sc1 lds\n"
"s_waitcnt vmcnt(6)\n"
"v_max3_u32 v0, v33, v35, v37\n"
"v_max_u32 v0, v39, v0\n"
"v_cmp_eq_u32 vcc, -1, v0\n"
"s_nop 0\n"
"s_cbranch_vccnz RT130_%=\n"
"BK131_%=:\n"
"s_waitcnt lgkmcnt(7)\n"
"v_mfma_f32_16x16x32_bf16 v[80:83], v[32:35], v[108:111], v[80:83]\n"
"ds_read_b128 v[108:111], v3 offset:44672\n"
"s_waitcnt lgkmcnt(7)\n"
"v_mfma_f32_16x16x32_bf16 v[84:87], v[32:35], v[112:115], v[84:87]\n"
"ds_read_b128 v[112:115], v3 offset:45696\n"
"s_add_u32 m0, s21, 4096\n"
"s_add_u32 s95, s85, 6144\n"
"buffer_load_dwordx4 v2, s[32:35], s95 offen sc1 lds\n"
"s_waitcnt lgkmcnt(7)\n"
"v_mfma_f32_16x16x32_bf16 v[88:91], v[32:35], v[116:119], v[88:91]\n"
"ds_read_b128 v[116:119], v3 offset:46720\n"
"s_waitcnt lgkmcnt(7)\n"
"v_mfma_f32_16x16x32_bf16 v[92:95], v[32:35], v[120:123], v[92:95]\n"
"ds_read_b128 v[120:123], v3 offset:47744\n"
"s_mul_i32 s95, s74, 2048\n"
"s_add_u32 m0, s95, 32768\n"
"s_add_u32 s95, s95, s83\n"
"s_add_u32 s95, s95, 163840\n"
"buffer_load_dwordx4 v2, s[32:35], s95 offen offset:0 sc1 lds\n"
"s_waitcnt vmcnt(6)\n"
"s_barrier\n"
"ds_read_b128 v[40:43], v8 offset:0\n"
"ds_read_b128 v[44:47], v8 offset:1024\n"
"ds_read_b128 v[48:51], v8 offset:2048\n"
"ds_read_b128 v[52:55], v8 offset:3072\n"
"s_waitcnt lgkmcnt(11)\n"
"v_mfma_f32_16x16x32_bf16 v[80:83], v[36:39], v[124:127], v[80:83]\n"
"ds_read_b128 v[124:127], v3 offset:51968\n"
"s_waitcnt lgkmcnt(11)\n"
"v_mfma_f32_16x16x32_bf16 v[84:87], v[36:39], v[96:99], v[84:87]\n"
"ds_read_b128 v[96:99], v3 offset:52992\n"
"s_waitcnt lgkmcnt(11)\n"
"v_mfma_f32_16x16x32_bf16 v[88:91], v[36:39], v[100:103], v[88:91]\n"
"ds_read_b128 v[100:103], v3 offset:54016\n"
"s_waitcnt lgkmcnt(11)\n"
"v_mfma_f32_16x16x32_bf16 v[92:95], v[36:39], v[104:107], v[92:95]\n"
"ds_read_b128 v[104:107], v3 offset:55040\n"
"buffer_load_dwordx4 v2, s[32:35], s95 offen offset:1024 sc1 lds\n"
"s_waitcnt lgkmcnt(6)\n"
"v_max3_u32 v0, v41, v43, v45\n"
"v_max_u32 v0, v47, v0\n"
"v_cmp_eq_u32 vcc, -1, v0\n"
"s_nop 0\n"
"s_cbranch_vccnz RT134_%=\n"
"BK135_%=:\n"
"v_mfma_f32_16x16x32_bf16 v[80:83], v[40:43], v[108:111], v[80:83]\n"
"ds_read_b128 v[108:111], v3 offset:59264\n"
"v_mfma_f32_16x16x32_bf16 v[84:87], v[40:43], v[112:115], v[84:87]\n"
"ds_read_b128 v[112:115], v3 offset:60288\n"
"v_mfma_f32_16x16x32_bf16 v[88:91], v[40:43], v[116:119], v[88:91]\n"
"ds_read_b128 v[116:119], v3 offset:61312\n"
"v_mfma_f32_16x16x32_bf16 v[92:95], v[40:43], v[120:123], v[92:95]\n"
"ds_read_b128 v[120:123], v3 offset:62336\n"
"s_waitcnt lgkmcnt(7)\n"
"v_mfma_f32_16x16x32_bf16 v[80:83], v[44:47], v[124:127], v[80:83]\n"
"ds_read_b128 v[124:127], v4 offset:896\n"
"s_waitcnt vmcnt(6)\n"
"v_max3_u32 v0, v72, v73, v74\n"
"s_waitcnt vmcnt(5)\n"
"v_max3_u32 v0, v75, v76, v0\n"
"v_max3_u32 v0, v77, v78, v0\n"
"v_max_u32 v0, v79, v0\n"
"v_cmp_eq_u32 vcc, -1, v0\n"
"s_nop 0\n"
"s_cbranch_vccnz RT139_%=\n"
"BK140_%=:\n"
"v_pk_add_f32 v[72:73], v[72:73], v[76:77]\n"
"s_waitcnt lgkmcnt(7)\n"
"v_mfma_f32_16x16x32_bf16 v[84:87], v[44:47], v[96:99], v[84:87]\n"
"ds_read_b128 v[96:99], v2 offset:0\n"
"v_pk_add_f32 v[74:75], v[74:75], v[78:79]\n"
"s_nop 1\n"
"v_permlane32_swap_b32 v72, v74\n"
"v_permlane32_swap_b32 v73, v75\n"
"s_waitcnt lgkmcnt(7)\n"
"v_mfma_f32_16x16x32_bf16 v[88:91], v[44:47], v[100:103], v[88:91]\n"
"ds_read_b128 v[100:103], v2 offset:1024\n"
"v_pk_add_f32 v[72:73], v[72:73], v[74:75]\n"
"v_mov_b64 v[74:75], v[72:73]\n"
"s_waitcnt lgkmcnt(7)\n"
"v_mfma_f32_16x16x32_bf16 v[92:95], v[44:47], v[104:107], v[92:95]\n"
"ds_read_b128 v[104:107], v2 offset:2048\n"
"v_permlane16_swap_b32 v72, v74\n"
"v_permlane16_swap_b32 v73, v75\n"
"v_pk_add_f32 v[72:73], v[72:73], v[74:75]\n"
"v_max3_u32 v0, v49, v51, v53\n"
"v_max_u32 v0, v55, v0\n"
"v_cmp_eq_u32 vcc, -1, v0\n"
"s_nop 0\n"
"s_cbranch_vccnz RT143_%=\n"
"BK144_%=:\n"
"s_waitcnt lgkmcnt(7)\n"
"v_mfma_f32_16x16x32_bf16 v[80:83], v[48:51], v[108:111], v[80:83]\n"
"ds_read_b128 v[108:111], v2 offset:6144\n"
"v_add_f32_dpp v72, v72, v72 row_ror:8 row_mask:0xf bank_mask:0xf\n"
"v_add_f32_dpp v73, v73, v73 row_ror:8 row_mask:0xf bank_mask:0xf\n"
"s_waitcnt lgkmcnt(7)\n"
"v_mfma_f32_16x16x32_bf16 v[84:87], v[48:51], v[112:115], v[84:87]\n"
"ds_read_b128 v[112:115], v2 offset:7168\n"
"v_mul_f32 v72, 0x3b000000, v72\n"
"v_mul_f32 v73, 0x3b000000, v73\n"
"v_fma_f32 v73, -v72, v72, v73\n"
"s_waitcnt lgkmcnt(7)\n"
"v_mfma_f32_16x16x32_bf16 v[88:91], v[48:51], v[116:119], v[88:91]\n"
"ds_read_b128 v[116:119], v2 offset:8192\n"
"v_max_f32 v73, 0, v73\n"
"v_add_f32 v73, 0x3727c5ac, v73\n"
"s_waitcnt lgkmcnt(7)\n"
"v_mfma_f32_16x16x32_bf16 v[92:95], v[48:51], v[120:123], v[92:95]\n"
"ds_read_b128 v[120:123], v2 offset:9216\n"
"v_rsq_f32 v73, v73\n"
"s_nop 0\n"
"v_mul_f32 v72, v72, v73\n"
"ds_write2_b32 v5, v73, v72 offset0:0 offset1:16\n"
"s_waitcnt vmcnt(0)\n"
"s_barrier\n"
"ds_read_b128 v[56:59], v9 offset:0\n"
"ds_read_b128 v[60:63], v9 offset:1024\n"
"ds_read_b128 v[64:67], v9 offset:2048\n"
"ds_read_b128 v[68:71], v9 offset:3072\n"
"ds_read_b128 v[72:75], v9 offset:4096\n"
"ds_read_b128 v[76:79], v9 offset:5120\n"
"s_waitcnt lgkmcnt(14)\n"
"v_mfma_f32_16x16x32_bf16 v[80:83], v[52:55], v[124:127], v[80:83]\n"
"ds_read_b128 v[124:127], v2 offset:13312\n"
"s_waitcnt lgkmcnt(14)\n"
"v_mfma_f32_16x16x32_bf16 v[84:87], v[52:55], v[96:99], v[84:87]\n"
"ds_read_b128 v[96:99], v2 offset:14336\n"
"s_waitcnt lgkmcnt(14)\n"
"v_mfma_f32_16x16x32_bf16 v[88:91], v[52:55], v[100:103], v[88:91]\n"
"ds_read_b128 v[100:103], v2 offset:15360\n"
"s_waitcnt lgkmcnt(14)\n"
"v_mfma_f32_16x16x32_bf16 v[92:95], v[52:55], v[104:107], v[92:95]\n"
"ds_read_b128 v[104:107], v2 offset:16384\n"
"s_waitcnt lgkmcnt(8)\n"
"v_max3_u32 v0, v57, v59, v61\n"
"v_max_u32 v0, v63, v0\n"
"v_cmp_eq_u32 vcc, -1, v0\n"
"s_nop 0\n"
"s_cbranch_vccnz RT147_%=\n"
"BK148_%=:\n"
"v_mfma_f32_16x16x32_bf16 v[80:83], v[56:59], v[108:111], v[80:83]\n"
"ds_read_b128 v[108:111], v2 offset:20480\n"
"v_mfma_f32_16x16x32_bf16 v[84:87], v[56:59], v[112:115], v[84:87]\n"
"ds_read_b128 v[112:115], v2 offset:21504\n"
"v_mfma_f32_16x16x32_bf16 v[88:91], v[56:59], v[116:119], v[88:91]\n"
"ds_read_b128 v[116:119], v2 offset:22528\n"
"v_mfma_f32_16x16x32_bf16 v[92:95], v[56:59], v[120:123], v[92:95]\n"
"ds_read_b128 v[120:123], v2 offset:23552\n"
"s_waitcnt lgkmcnt(7)\n"
"v_mfma_f32_16x16x32_bf16 v[80:83], v[60:63], v[124:127], v[80:83]\n"
"ds_read_b128 v[124:127], v2 offset:27648\n"
"s_waitcnt lgkmcnt(7)\n"
"v_mfma_f32_16x16x32_bf16 v[84:87], v[60:63], v[96:99], v[84:87]\n"
"ds_read_b128 v[96:99], v2 offset:28672\n"
"s_waitcnt lgkmcnt(7)\n"
"v_mfma_f32_16x16x32_bf16 v[88:91], v[60:63], v[100:103], v[88:91]\n"
"ds_read_b128 v[100:103], v2 offset:29696\n"
"s_waitcnt lgkmcnt(7)\n"
"v_mfma_f32_16x16x32_bf16 v[92:95], v[60:63], v[104:107], v[92:95]\n"
"ds_read_b128 v[104:107], v2 offset:30720\n"
"s_mul_i32 s95, s74, 2048\n"
"s_add_u32 m0, s95, 49152\n"
"s_add_u32 s95, s95, s83\n"
"s_add_u32 s95, s95, 180224\n"
"buffer_load_dwordx4 v2, s[32:35], s95 offen offset:0 sc1 lds\n"
"v_max3_u32 v0, v65, v67, v69\n"
"v_max_u32 v0, v71, v0\n"
"v_cmp_eq_u32 vcc, -1, v0\n"
"s_nop 0\n"
"s_cbranch_vccnz RT153_%=\n"
"BK154_%=:\n"
"s_waitcnt lgkmcnt(7)\n"
"v_mfma_f32_16x16x32_bf16 v[80:83], v[64:67], v[108:111], v[80:83]\n"
"ds_read_b128 v[108:111], v2 offset:34816\n"
"s_waitcnt lgkmcnt(7)\n"
"v_mfma_f32_16x16x32_bf16 v[84:87], v[64:67], v[112:115], v[84:87]\n"
"ds_read_b128 v[112:115], v2 offset:35840\n"
"s_waitcnt lgkmcnt(7)\n"
"v_mfma_f32_16x16x32_bf16 v[88:91], v[64:67], v[116:119], v[88:91]\n"
"ds_read_b128 v[116:119], v2 offset:36864\n"
"s_waitcnt lgkmcnt(7)\n"
"v_mfma_f32_16x16x32_bf16 v[92:95], v[64:67], v[120:123], v[92:95]\n"
"ds_read_b128 v[120:123], v2 offset:37888\n"
"buffer_load_dwordx4 v2, s[32:35], s95 offen offset:1024 sc1 lds\n"
"s_waitcnt lgkmcnt(7)\n"
"v_mfma_f32_16x16x32_bf16 v[80:83], v[68:71], v[124:127], v[80:83]\n"
"ds_read_b128 v[124:127], v2 offset:41984\n"
"s_waitcnt lgkmcnt(7)\n"
"v_mfma_f32_16x16x32_bf16 v[84:87], v[68:71], v[96:99], v[84:87]\n"
"ds_read_b128 v[96:99], v2 offset:43008\n"
"s_waitcnt lgkmcnt(7)\n"
"v_mfma_f32_16x16x32_bf16 v[88:91], v[68:71], v[100:103], v[88:91]\n"
"ds_read_b128 v[100:103], v2 offset:44032\n"
"s_waitcnt lgkmcnt(7)\n"
"v_mfma_f32_16x16x32_bf16 v[92:95], v[68:71], v[104:107], v[92:95]\n"
"ds_read_b128 v[104:107], v2 offset:45056\n"
"s_mul_i32 s95, s74, 7168\n"
"s_add_u32 m0, s95, 66560\n"
"s_add_u32 s95, s95, s83\n"
"s_add_u32 s95, s95, 196608\n"
"buffer_load_dwordx4 v2, s[32:35], s95 offen offset:0 sc1 lds\n"
"v_max3_u32 v0, v73, v75, v77\n"
"v_max_u32 v0, v79, v0\n"
"v_cmp_eq_u32 vcc, -1, v0\n"
"s_nop 0\n"
"s_cbranch_vccnz RT158_%=\n"
"BK159_%=:\n"
"s_waitcnt lgkmcnt(7)\n"
"v_mfma_f32_16x16x32_bf16 v[80:83], v[72:75], v[108:111], v[80:83]\n"
"s_waitcnt lgkmcnt(6)\n"
"v_mfma_f32_16x16x32_bf16 v[84:87], v[72:75], v[112:115], v[84:87]\n"
"ds_read2_b32 v[8:9], v7 offset0:0 offset1:16\n"
"s_waitcnt lgkmcnt(6)\n"
"v_mfma_f32_16x16x32_bf16 v[88:91], v[72:75], v[116:119], v[88:91]\n"
"s_waitcnt lgkmcnt(5)\n"
"v_mfma_f32_16x16x32_bf16 v[92:95], v[72:75], v[120:123], v[92:95]\n"
"buffer_load_dwordx4 v2, s[32:35], s95 offen offset:1024 sc1 lds\n"
"s_waitcnt lgkmcnt(4)\n"
"v_mfma_f32_16x16x32_bf16 v[80:83], v[76:79], v[124:127], v[80:83]\n"
"s_waitcnt lgkmcnt(3)\n"
"v_mfma_f32_16x16x32_bf16 v[84:87], v[76:79], v[96:99], v[84:87]\n"
"v_and_b32 v11, 15, v1\n"
"v_lshlrev_b32 v11, 2, v11\n"
"v_add_u32 v11, 160768, v11\n"
"s_waitcnt lgkmcnt(2)\n"
"v_mfma_f32_16x16x32_bf16 v[88:91], v[76:79], v[100:103], v[88:91]\n"
"s_waitcnt lgkmcnt(1)\n"
"v_mfma_f32_16x16x32_bf16 v[92:95], v[76:79], v[104:107], v[92:95]\n"
"buffer_load_dwordx4 v2, s[32:35], s95 offen offset:2048 sc1 lds\n"
"ds_read_b128 v[96:99], v6\n"
"ds_read_b128 v[100:103], v6 offset:64\n"
"ds_read2_b32 v[108:109], v11 offset0:0 offset1:112\n"
"ds_read2_b32 v[110:111], v11 offset0:16 offset1:128\n"
"ds_read2_b32 v[112:113], v11 offset0:32 offset1:144\n"
"ds_read2_b32 v[114:115], v11 offset0:48 offset1:160\n"
"v_mov_b32 v124, 0xc0135761\n"
"s_waitcnt lgkmcnt(3)\n"
"v_pk_fma_f32 v[80:81], v[80:81], v[96:97], v[108:109] op_sel_hi:[1,1,0]\n"
"v_pk_fma_f32 v[80:81], v[108:109], v[100:101], v[80:81] op_sel:[1,0,0] op_sel_hi:[1,1,1] neg_lo:[1,0,0] neg_hi:[1,0,0]\n"
"v_pk_fma_f32 v[82:83], v[82:83], v[98:99], v[108:109] op_sel_hi:[1,1,0]\n"
"v_pk_fma_f32 v[82:83], v[108:109], v[102:103], v[82:83] op_sel:[1,0,0] op_sel_hi:[1,1,1] neg_lo:[1,0,0] neg_hi:[1,0,0]\n"
"s_waitcnt lgkmcnt(2)\n"
"v_pk_fma_f32 v[84:85], v[84:85], v[96:97], v[110:111] op_sel_hi:[1,1,0]\n"
"v_pk_fma_f32 v[84:85], v[110:111], v[100:101], v[84:85] op_sel:[1,0,0] op_sel_hi:[1,1,1] neg_lo:[1,0,0] neg_hi:[1,0,0]\n"
"v_pk_fma_f32 v[86:87], v[86:87], v[98:99], v[110:111] op_sel_hi:[1,1,0]\n"
"v_pk_fma_f32 v[86:87], v[110:111], v[102:103], v[86:87] op_sel:[1,0,0] op_sel_hi:[1,1,1] neg_lo:[1,0,0] neg_hi:[1,0,0]\n"
"s_waitcnt lgkmcnt(1)\n"
"v_pk_fma_f32 v[88:89], v[88:89], v[96:97], v[112:113] op_sel_hi:[1,1,0]\n"
"v_pk_fma_f32 v[88:89], v[112:113], v[100:101], v[88:89] op_sel:[1,0,0] op_sel_hi:[1,1,1] neg_lo:[1,0,0] neg_hi:[1,0,0]\n"
"v_pk_fma_f32 v[90:91], v[90:91], v[98:99], v[112:113] op_sel_hi:[1,1,0]\n"
"v_pk_fma_f32 v[90:91], v[112:113], v[102:103], v[90:91] op_sel:[1,0,0] op_sel_hi:[1,1,1] neg_lo:[1,0,0] neg_hi:[1,0,0]\n"
"s_waitcnt lgkmcnt(0)\n"
"v_pk_fma_f32 v[92:93], v[92:93], v[96:97], v[114:115] op_sel_hi:[1,1,0]\n"
"buffer_load_dwordx4 v2, s[32:35], s95 offen offset:3072 sc1 lds\n"
"v_pk_fma_f32 v[92:93], v[114:115], v[100:101], v[92:93] op_sel:[1,0,0] op_sel_hi:[1,1,1] neg_lo:[1,0,0] neg_hi:[1,0,0]\n"
"v_pk_fma_f32 v[94:95], v[94:95], v[98:99], v[114:115] op_sel_hi:[1,1,0]\n"
"v_pk_fma_f32 v[94:95], v[114:115], v[102:103], v[94:95] op_sel:[1,0,0] op_sel_hi:[1,1,1] neg_lo:[1,0,0] neg_hi:[1,0,0]\n"
"v_pk_mul_f32 v[116:117], v[80:81], v[80:81]\n"
"v_pk_mul_f32 v[118:119], v[82:83], v[82:83]\n"
"v_pk_fma_f32 v[116:117], v[116:117], s[2:3], v[124:125] op_sel_hi:[1,0,0]\n"
"v_pk_fma_f32 v[118:119], v[118:119], s[2:3], v[124:125] op_sel_hi:[1,0,0]\n"
"v_pk_mul_f32 v[116:117], v[116:117], v[80:81]\n"
"v_pk_mul_f32 v[118:119], v[118:119], v[82:83]\n"
"v_exp_f32 v116, v116\n"
"v_exp_f32 v117, v117\n"
"v_exp_f32 v118, v118\n"
"v_exp_f32 v119, v119\n"
"s_mul_i32 s95, s74, 7168\n"
"s_add_u32 m0, s95, 70656\n"
"s_add_u32 s95, s95, s83\n"
"s_add_u32 s95, s95, 200704\n"
"buffer_load_dwordx4 v2, s[32:35], s95 offen offset:0 sc1 lds\n"
"v_pk_add_f32 v[116:117], v[116:117], 1.0 op_sel_hi:[1,0]\n"
"v_pk_add_f32 v[118:119], v[118:119], 1.0 op_sel_hi:[1,0]\n"
"v_rcp_f32 v116, v116\n"
"v_rcp_f32 v117, v117\n"
"v_rcp_f32 v118, v118\n"
"v_rcp_f32 v119, v119\n"
"v_pk_mul_f32 v[80:81], v[80:81], v[116:117]\n"
"v_pk_mul_f32 v[82:83], v[82:83], v[118:119]\n"
"v_pk_mul_f32 v[120:121], v[84:85], v[84:85]\n"
"v_pk_mul_f32 v[122:123], v[86:87], v[86:87]\n"
"v_pk_fma_f32 v[120:121], v[120:121], s[2:3], v[124:125] op_sel_hi:[1,0,0]\n"
"v_pk_fma_f32 v[122:123], v[122:123], s[2:3], v[124:125] op_sel_hi:[1,0,0]\n"
"v_pk_mul_f32 v[120:121], v[120:121], v[84:85]\n"
"buffer_load_dwordx4 v2, s[32:35], s95 offen offset:1024 sc1 lds\n"
"v_pk_mul_f32 v[122:123], v[122:123], v[86:87]\n"
"v_exp_f32 v120, v120\n"
"v_exp_f32 v121, v121\n"
"v_exp_f32 v122, v122\n"
"v_exp_f32 v123, v123\n"
"v_pk_add_f32 v[120:121], v[120:121], 1.0 op_sel_hi:[1,0]\n"
"v_pk_add_f32 v[122:123], v[122:123], 1.0 op_sel_hi:[1,0]\n"
"v_rcp_f32 v120, v120\n"
"v_rcp_f32 v121, v121\n"
"v_rcp_f32 v122, v122\n"
"v_rcp_f32 v123, v123\n"
"v_pk_mul_f32 v[84:85], v[84:85], v[120:121]\n"
"buffer_load_dwordx4 v2, s[32:35], s95 offen offset:2048 sc1 lds\n"
"v_pk_mul_f32 v[86:87], v[86:87], v[122:123]\n"
"v_pk_mul_f32 v[116:117], v[88:89], v[88:89]\n"
"v_pk_mul_f32 v[118:119], v[90:91], v[90:91]\n"
"v_pk_fma_f32 v[116:117], v[116:117], s[2:3], v[124:125] op_sel_hi:[1,0,0]\n"
"v_pk_fma_f32 v[118:119], v[118:119], s[2:3], v[124:125] op_sel_hi:[1,0,0]\n"
"v_pk_mul_f32 v[116:117], v[116:117], v[88:89]\n"
"v_pk_mul_f32 v[118:119], v[118:119], v[90:91]\n"
"v_exp_f32 v116, v116\n"
"v_exp_f32 v117, v117\n"
"v_exp_f32 v118, v118\n"
"v_exp_f32 v119, v119\n"
"v_pk_add_f32 v[116:117], v[116:117], 1.0 op_sel_hi:[1,0]\n"
"v_pk_add_f32 v[118:119], v[118:119], 1.0 op_sel_hi:[1,0]\n"
"v_rcp_f32 v116, v116\n"
"v_rcp_f32 v117, v117\n"
"v_rcp_f32 v118, v118\n"
"v_rcp_f32 v119, v119\n"
"v_pk_mul_f32 v[88:89], v[88:89], v[116:117]\n"
"v_pk_mul_f32 v[90:91], v[90:91], v[118:119]\n"
"v_pk_mul_f32 v[120:121], v[92:93], v[92:93]\n"
"v_pk_mul_f32 v[122:123], v[94:95], v[94:95]\n"
"v_pk_fma_f32 v[120:121], v[120:121], s[2:3], v[124:125] op_sel_hi:[1,0,0]\n"
"v_pk_fma_f32 v[122:123], v[122:123], s[2:3], v[124:125] op_sel_hi:[1,0,0]\n"
"v_pk_mul_f32 v[120:121], v[120:121], v[92:93]\n"
"v_pk_mul_f32 v[122:123], v[122:123], v[94:95]\n"
"v_exp_f32 v120, v120\n"
"v_exp_f32 v121, v121\n"
"v_exp_f32 v122, v122\n"
"v_exp_f32 v123, v123\n"
"v_pk_add_f32 v[120:121], v[120:121], 1.0 op_sel_hi:[1,0]\n"
"v_pk_add_f32 v[122:123], v[122:123], 1.0 op_sel_hi:[1,0]\n"
"v_rcp_f32 v120, v120\n"
"v_rcp_f32 v121, v121\n"
"v_rcp_f32 v122, v122\n"
"v_rcp_f32 v123, v123\n"
"v_pk_mul_f32 v[92:93], v[92:93], v[120:121]\n"
"v_pk_mul_f32 v[94:95], v[94:95], v[122:123]\n"
"v_pk_add_f32 v[96:97], v[80:81], v[84:85]\n"
"v_pk_mul_f32 v[100:101], v[80:81], v[80:81]\n"
"v_pk_add_f32 v[98:99], v[82:83], v[86:87]\n"
"v_pk_mul_f32 v[102:103], v[82:83], v[82:83]\n"
"v_pk_fma_f32 v[100:101], v[84:85], v[84:85], v[100:101]\n"
"v_pk_fma_f32 v[102:103], v[86:87], v[86:87], v[102:103]\n"
"v_pk_add_f32 v[96:97], v[96:97], v[88:89]\n"
"v_pk_fma_f32 v[100:101], v[88:89], v[88:89], v[100:101]\n"
"v_pk_add_f32 v[98:99], v[98:99], v[90:91]\n"
"v_pk_fma_f32 v[102:103], v[90:91], v[90:91], v[102:103]\n"
"v_pk_add_f32 v[96:97], v[96:97], v[92:93]\n"
"v_pk_fma_f32 v[100:101], v[92:93], v[92:93], v[100:101]\n"
"v_pk_add_f32 v[98:99], v[98:99], v[94:95]\n"
"v_pk_fma_f32 v[102:103], v[94:95], v[94:95], v[102:103]\n"
"v_and_b32 v116, 0x30, v1\n"
"v_lshlrev_b32 v116, 1, v116\n"
"v_and_b32 v117, 1, v1\n"
"v_cmp_eq_u32 vcc, 0, v117\n"
"v_lshl_or_b32 v116, v117, 4, v116\n"
"v_add_f32_dpp v96, v96, v96 row_ror:8 row_mask:0xf bank_mask:0x3\n"
"v_add_f32_dpp v97, v97, v97 row_ror:8 row_mask:0xf bank_mask:0x3\n"
"v_add_f32_dpp v98, v98, v98 row_ror:8 row_mask:0xf bank_mask:0x3\n"
"v_add_f32_dpp v99, v99, v99 row_ror:8 row_mask:0xf bank_mask:0x3\n"
"v_add_f32_dpp v96, v100, v100 row_ror:8 row_mask:0xf bank_mask:0xc\n"
"v_add_f32_dpp v97, v101, v101 row_ror:8 row_mask:0xf bank_mask:0xc\n"
"v_add_f32_dpp v98, v102, v102 row_ror:8 row_mask:0xf bank_mask:0xc\n"
"v_add_f32_dpp v99, v103, v103 row_ror:8 row_mask:0xf bank_mask:0xc\n"
"v_bfe_u32 v117, v1, 2, 1\n"
"v_lshl_or_b32 v116, v117, 3, v116\n"
"v_add_f32_dpp v96, v96, v96 row_half_mirror row_mask:0xf bank_mask:0x5\n"
"v_add_f32_dpp v98, v98, v98 row_half_mirror row_mask:0xf bank_mask:0x5\n"
"s_nop 0\n"
"v_add_f32_dpp v96, v97, v97 row_half_mirror row_mask:0xf bank_mask:0xa\n"
"v_add_f32_dpp v98, v99, v99 row_half_mirror row_mask:0xf bank_mask:0xa\n"
"v_bfe_u32 v117, v1, 3, 1\n"
"v_lshl_or_b32 v116, v117, 2, v116\n"
"v_add_f32_dpp v96, v96, v96 quad_perm:[1,0,3,2] row_mask:0xf bank_mask:0xf\n"
"v_add_f32_dpp v98, v98, v98 quad_perm:[1,0,3,2] row_mask:0xf bank_mask:0xf\n"
"s_nop 0\n"
"v_add_f32_dpp v96, v96, v96 quad_perm:[2,3,0,1] row_mask:0xf bank_mask:0xf\n"
"v_add_f32_dpp v98, v98, v98 quad_perm:[2,3,0,1] row_mask:0xf bank_mask:0xf\n"
"s_add_u32 s88, s88, 262144\n"
"s_add_u32 s77, s77, 262144\n"
"v_cndmask_b32 v96, v98, v96, vcc\n"
"buffer_store_dword v96, v116, s[32:35], s88 offen sc1\n"
"s_waitcnt vmcnt(1)\n"
"s_barrier\n"
"ds_read_b128 v[108:111], v2 offset:53248\n"
"ds_read_b128 v[112:115], v2 offset:54272\n"
"ds_read_b128 v[116:119], v2 offset:55296\n"
"ds_read_b128 v[120:123], v2 offset:60416\n"
"ds_read_b128 v[124:127], v2 offset:61440\n"
"s_waitcnt lgkmcnt(4)\n"
"v_mfma_f32_16x16x32_bf16 v[96:99], v[108:111], v[16:19], 0\n"
"ds_read_b128 v[108:111], v2 offset:62464\n"
"s_waitcnt lgkmcnt(4)\n"
"v_mfma_f32_16x16x32_bf16 v[100:103], v[112:115], v[16:19], 0\n"
"ds_read_b128 v[112:115], v3 offset:3072\n"
"s_mul_i32 s95, s74, 5120\n"
"s_add_u32 m0, s95, 0\n"
"s_add_u32 s95, s95, s83\n"
"s_add_u32 s95, s95, 253952\n"
"buffer_load_dwordx4 v2, s[32:35], s95 offen offset:0 sc1 lds\n"
"s_waitcnt lgkmcnt(4)\n"
"v_mfma_f32_16x16x32_bf16 v[104:107], v[116:119], v[16:19], 0\n"
"ds_read_b128 v[116:119], v3 offset:4096\n"
"buffer_load_dwordx4 v2, s[32:35], s95 offen offset:1024 sc1 lds\n"
"s_waitcnt lgkmcnt(4)\n"
"v_mfma_f32_16x16x32_bf16 v[96:99], v[120:123], v[20:23], v[96:99]\n"
"ds_read_b128 v[120:123], v3 offset:5120\n"
"s_waitcnt lgkmcnt(4)\n"
"v_mfma_f32_16x16x32_bf16 v[100:103], v[124:127], v[20:23], v[100:103]\n"
"ds_read_b128 v[124:127], v3 offset:10240\n"
"s_waitcnt lgkmcnt(4)\n"
"v_mfma_f32_16x16x32_bf16 v[104:107], v[108:111], v[20:23], v[104:107]\n"
"ds_read_b128 v[108:111], v3 offset:11264\n"
"s_waitcnt lgkmcnt(4)\n"
"v_mfma_f32_16x16x32_bf16 v[96:99], v[112:115], v[24:27], v[96:99]\n"
"ds_read_b128 v[112:115], v3 offset:12288\n"
"s_waitcnt lgkmcnt(4)\n"
"v_mfma_f32_16x16x32_bf16 v[100:103], v[116:119], v[24:27], v[100:103]\n"
"ds_read_b128 v[116:119], v3 offset:17408\n"
"s_waitcnt lgkmcnt(4)\n"
"v_mfma_f32_16x16x32_bf16 v[104:107], v[120:123], v[24:27], v[104:107]\n"
"ds_read_b128 v[120:123], v3 offset:18432\n"
"s_lshr_b32 s90, s82, 15\n"
"s_lshl_b32 s89, s76, 6\n"
"s_add_u32 s89, s89, s90\n"
"s_mul_i32 s89, s89, 7168\n"
"s_and_b32 s90, s86, 1\n"
"s_lshl_b32 s90, s90, 9\n"
"s_add_u32 s89, s89, s90\n"
"s_add_u32 s88, s89, 13631488\n"
"s_waitcnt lgkmcnt(4)\n"
"v_mfma_f32_16x16x32_bf16 v[96:99], v[124:127], v[28:31], v[96:99]\n"
"ds_read_b128 v[124:127], v3 offset:19456\n"
"s_waitcnt lgkmcnt(4)\n"
"v_mfma_f32_16x16x32_bf16 v[100:103], v[108:111], v[28:31], v[100:103]\n"
"ds_read_b128 v[108:111], v3 offset:24576\n"
"v_bfe_u32 v11, v1, 4, 1\n"
"v_lshlrev_b32 v11, 10, v11\n"
"v_lshrrev_b32 v24, 5, v1\n"
"s_waitcnt lgkmcnt(4)\n"
"v_mfma_f32_16x16x32_bf16 v[104:107], v[112:115], v[28:31], v[104:107]\n"
"ds_read_b128 v[112:115], v3 offset:25600\n"
"v_lshl_or_b32 v11, v24, 8, v11\n"
"v_and_b32 v24, 0xf0, v2\n"
"v_or_b32 v11, v11, v24\n"
"s_waitcnt lgkmcnt(4)\n"
"v_mfma_f32_16x16x32_bf16 v[96:99], v[116:119], v[32:35], v[96:99]\n"
"ds_read_b128 v[116:119], v3 offset:26624\n"
"s_waitcnt vmcnt(2)\n"
"s_waitcnt lgkmcnt(4)\n"
"v_mfma_f32_16x16x32_bf16 v[100:103], v[120:123], v[32:35], v[100:103]\n"
"ds_read_b128 v[120:123], v3 offset:31744\n"
"buffer_load_dwordx4 v[16:19], v2, s[32:35], s77 offen offset:0 sc1\n"
"s_waitcnt lgkmcnt(4)\n"
"v_mfma_f32_16x16x32_bf16 v[104:107], v[124:127], v[32:35], v[104:107]\n"
"ds_read_b128 v[124:127], v3 offset:32768\n"
"buffer_load_dwordx4 v[20:23], v2, s[32:35], s77 offen offset:1024 sc1\n"
"s_waitcnt lgkmcnt(4)\n"
"v_mfma_f32_16x16x32_bf16 v[96:99], v[108:111], v[36:39], v[96:99]\n"
"ds_read_b128 v[108:111], v3 offset:33792\n"
"buffer_load_dwordx4 v2, s[32:35], s95 offen offset:2048 sc1 lds\n"
"s_waitcnt lgkmcnt(4)\n"
"v_mfma_f32_16x16x32_bf16 v[100:103], v[112:115], v[36:39], v[100:103]\n"
"ds_read_b128 v[112:115], v3 offset:38912\n"
"buffer_load_dwordx4 v2, s[32:35], s95 offen offset:3072 sc1 lds\n"
"s_waitcnt lgkmcnt(4)\n"
"v_mfma_f32_16x16x32_bf16 v[104:107], v[116:119], v[36:39], v[104:107]\n"
"ds_read_b128 v[116:119], v3 offset:39936\n"
"s_mul_i32 s95, s74, 5120\n"
"s_add_u32 m0, s95, 4096\n"
"s_add_u32 s95, s95, s83\n"
"s_add_u32 s95, s95, 258048\n"
"buffer_load_dwordx4 v2, s[32:35], s95 offen offset:0 sc1 lds\n"
"s_waitcnt lgkmcnt(4)\n"
"v_mfma_f32_16x16x32_bf16 v[96:99], v[120:123], v[40:43], v[96:99]\n"
"ds_read_b128 v[120:123], v3 offset:40960\n"
"s_waitcnt lgkmcnt(4)\n"
"v_mfma_f32_16x16x32_bf16 v[100:103], v[124:127], v[40:43], v[100:103]\n"
"ds_read_b128 v[124:127], v3 offset:46080\n"
"s_waitcnt lgkmcnt(4)\n"
"v_mfma_f32_16x16x32_bf16 v[104:107], v[108:111], v[40:43], v[104:107]\n"
"ds_read_b128 v[108:111], v3 offset:47104\n"
"s_waitcnt lgkmcnt(4)\n"
"v_mfma_f32_16x16x32_bf16 v[96:99], v[112:115], v[44:47], v[96:99]\n"
"ds_read_b128 v[112:115], v3 offset:48128\n"
"s_waitcnt vmcnt(4)\n"
"v_max3_u32 v0, v16, v17, v18\n"
"s_waitcnt vmcnt(3)\n"
"v_max3_u32 v0, v19, v20, v0\n"
"v_max3_u32 v0, v21, v22, v0\n"
"v_max_u32 v0, v23, v0\n"
"v_cmp_eq_u32 vcc, -1, v0\n"
"s_nop 0\n"
"s_cbranch_vccnz RT162_%=\n"
"BK163_%=:\n"
"v_pk_add_f32 v[16:17], v[16:17], v[20:21]\n"
"v_pk_add_f32 v[18:19], v[18:19], v[22:23]\n"
"s_nop 1\n"
"v_permlane32_swap_b32 v16, v18\n"
"v_permlane32_swap_b32 v17, v19\n"
"v_pk_add_f32 v[16:17], v[16:17], v[18:19]\n"
"v_mov_b64 v[18:19], v[16:17]\n"
"s_nop 1\n"
"v_permlane16_swap_b32 v16, v18\n"
"v_permlane16_swap_b32 v17, v19\n"
"v_pk_add_f32 v[16:17], v[16:17], v[18:19]\n"
"s_nop 1\n"
"v_add_f32_dpp v16, v16, v16 row_ror:8 row_mask:0xf bank_mask:0xf\n"
"v_add_f32_dpp v17, v17, v17 row_ror:8 row_mask:0xf bank_mask:0xf\n"
"v_mul_f32 v16, 0x3a924925, v16\n"
"v_mul_f32 v17, 0x3a924925, v17\n"
"v_fma_f32 v17, -v16, v16, v17\n"
"s_waitcnt lgkmcnt(4)\n"
"v_mfma_f32_16x16x32_bf16 v[100:103], v[116:119], v[44:47], v[100:103]\n"
"ds_read_b128 v[116:119], v3 offset:53248\n"
"v_max_f32 v17, 0, v17\n"
"v_add_f32 v17, 0x3727c5ac, v17\n"
"v_rsq_f32 v17, v17\n"
"s_nop 0\n"
"v_mul_f32 v16, v16, v17\n"
"ds_write2_b32 v5, v17, v16 offset0:0 offset1:16\n"
"ds_read_b128 v[16:19], v6\n"
"ds_read_b128 v[20:23], v6 offset:64\n"
"s_waitcnt lgkmcnt(0)\n"
"v_pk_fma_f32 v[80:81], v[80:81], v[16:17], v[20:21] neg_lo:[0,0,1] neg_hi:[0,0,1]\n"
"v_pk_fma_f32 v[82:83], v[82:83], v[18:19], v[22:23] neg_lo:[0,0,1] neg_hi:[0,0,1]\n"
"v_cvt_pk_bf16_f32 v24, v80, v81\n"
"v_cvt_pk_bf16_f32 v25, v82, v83\n"
"v_pk_fma_f32 v[84:85], v[84:85], v[16:17], v[20:21] neg_lo:[0,0,1] neg_hi:[0,0,1]\n"
"v_pk_fma_f32 v[86:87], v[86:87], v[18:19], v[22:23] neg_lo:[0,0,1] neg_hi:[0,0,1]\n"
"v_cvt_pk_bf16_f32 v26, v84, v85\n"
"v_cvt_pk_bf16_f32 v27, v86, v87\n"
"v_mfma_f32_16x16x32_bf16 v[104:107], v[120:123], v[44:47], v[104:107]\n"
"ds_read_b128 v[120:123], v3 offset:54272\n"
"v_permlane16_swap_b32 v24, v26\n"
"v_permlane16_swap_b32 v25, v27\n"
"buffer_store_dwordx4 v[24:27], v11, s[32:35], s88 offen offset:0 sc1\n"
"v_pk_fma_f32 v[88:89], v[88:89], v[16:17], v[20:21] neg_lo:[0,0,1] neg_hi:[0,0,1]\n"
"v_pk_fma_f32 v[90:91], v[90:91], v[18:19], v[22:23] neg_lo:[0,0,1] neg_hi:[0,0,1]\n"
"v_cvt_pk_bf16_f32 v28, v88, v89\n"
"v_cvt_pk_bf16_f32 v29, v90, v91\n"
"v_pk_fma_f32 v[92:93], v[92:93], v[16:17], v[20:21] neg_lo:[0,0,1] neg_hi:[0,0,1]\n"
"v_pk_fma_f32 v[94:95], v[94:95], v[18:19], v[22:23] neg_lo:[0,0,1] neg_hi:[0,0,1]\n"
"v_cvt_pk_bf16_f32 v30, v92, v93\n"
"v_cvt_pk_bf16_f32 v31, v94, v95\n"
"s_nop 0\n"
"v_permlane16_swap_b32 v28, v30\n"
"v_permlane16_swap_b32 v29, v31\n"
"buffer_store_dwordx4 v[28:31], v11, s[32:35], s88 offen offset:2048 sc1\n"
"v_mfma_f32_16x16x32_bf16 v[96:99], v[124:127], v[48:51], v[96:99]\n"
"ds_read_b128 v[32:35], v3 offset:55296\n"
"s_waitcnt vmcnt(2)\n"
"s_barrier\n"
"ds_read_b128 v[36:39], v2 offset:2048\n"
"v_mfma_f32_16x16x32_bf16 v[100:103], v[108:111], v[48:51], v[100:103]\n"
"ds_read_b128 v[40:43], v2 offset:3072\n"
"ds_read_b128 v[44:47], v2 offset:4096\n"
"v_mfma_f32_16x16x32_bf16 v[104:107], v[112:115], v[48:51], v[104:107]\n"
"ds_read_b128 v[124:127], v2 offset:9216\n"
"ds_read_b128 v[108:111], v2 offset:10240\n"
"v_mfma_f32_16x16x32_bf16 v[96:99], v[116:119], v[52:55], v[96:99]\n"
"ds_read_b128 v[112:115], v2 offset:11264\n"
"ds_read_b128 v[116:119], v2 offset:16384\n"
"s_waitcnt lgkmcnt(8)\n"
"v_mfma_f32_16x16x32_bf16 v[100:103], v[120:123], v[52:55], v[100:103]\n"
"ds_read_b128 v[120:123], v2 offset:17408\n"
"s_waitcnt lgkmcnt(8)\n"
"v_mfma_f32_16x16x32_bf16 v[104:107], v[32:35], v[52:55], v[104:107]\n"
"ds_read_b128 v[32:35], v2 offset:18432\n"
"s_waitcnt lgkmcnt(8)\n"
"v_mfma_f32_16x16x32_bf16 v[96:99], v[36:39], v[56:59], v[96:99]\n"
"ds_read_b128 v[36:39], v2 offset:23552\n"
"s_waitcnt lgkmcnt(8)\n"
"v_mfma_f32_16x16x32_bf16 v[100:103], v[40:43], v[56:59], v[100:103]\n"
"ds_read_b128 v[40:43], v2 offset:24576\n"
"s_waitcnt lgkmcnt(8)\n"
"v_mfma_f32_16x16x32_bf16 v[104:107], v[44:47], v[56:59], v[104:107]\n"
"ds_read_b128 v[44:47], v2 offset:25600\n"
"s_waitcnt lgkmcnt(8)\n"
"v_mfma_f32_16x16x32_bf16 v[96:99], v[124:127], v[60:63], v[96:99]\n"
"ds_read_b128 v[124:127], v2 offset:30720\n"
"s_waitcnt lgkmcnt(8)\n"
"v_mfma_f32_16x16x32_bf16 v[100:103], v[108:111], v[60:63], v[100:103]\n"
"ds_read_b128 v[108:111], v2 offset:31744\n"
"s_waitcnt lgkmcnt(8)\n"
"v_mfma_f32_16x16x32_bf16 v[104:107], v[112:115], v[60:63], v[104:107]\n"
"ds_read_b128 v[112:115], v2 offset:32768\n"
"s_waitcnt lgkmcnt(8)\n"
"v_mfma_f32_16x16x32_bf16 v[96:99], v[116:119], v[64:67], v[96:99]\n"
"ds_read_b128 v[116:119], v2 offset:37888\n"
"s_waitcnt lgkmcnt(8)\n"
"v_mfma_f32_16x16x32_bf16 v[100:103], v[120:123], v[64:67], v[100:103]\n"
"ds_read_b128 v[120:123], v2 offset:38912\n"
"s_waitcnt lgkmcnt(8)\n"
"v_mfma_f32_16x16x32_bf16 v[104:107], v[32:35], v[64:67], v[104:107]\n"
"ds_read_b128 v[32:35], v2 offset:39936\n"
"s_waitcnt lgkmcnt(8)\n"
"v_mfma_f32_16x16x32_bf16 v[96:99], v[36:39], v[68:71], v[96:99]\n"
"s_waitcnt lgkmcnt(7)\n"
"v_mfma_f32_16x16x32_bf16 v[100:103], v[40:43], v[68:71], v[100:103]\n"
"s_waitcnt lgkmcnt(6)\n"
"v_mfma_f32_16x16x32_bf16 v[104:107], v[44:47], v[68:71], v[104:107]\n"
"s_and_b32 s89, s74, 1\n"
"s_lshl_b32 s90, s84, 1\n"
"s_add_u32 s89, s89, s90\n"
"s_mul_i32 s68, s89, 7168\n"
"s_lshl_b32 s90, s76, 4\n"
"s_add_u32 s89, s89, s90\n"
"s_lshl_b32 s69, s89, 8\n"
"s_add_u32 s69, s69, 131072\n"
"s_mul_i32 s85, s89, 28672\n"
"s_add_u32 s85, s85, 13631488\n"
"s_lshr_b32 s89, s75, 3\n"
"s_mul_i32 s89, s89, 114688\n"
"s_add_u32 s87, s85, s89\n"
"s_bfe_u32 s89, s74, 0x10001\n"
"s_mul_i32 s89, s89, 3072\n"
"s_add_u32 s66, s87, s89\n"
"s_add_u32 s85, s85, s89\n"
"s_add_u32 s67, s68, s89\n"
"s_waitcnt lgkmcnt(5)\n"
"v_mfma_f32_16x16x32_bf16 v[96:99], v[124:127], v[72:75], v[96:99]\n"
"s_waitcnt lgkmcnt(4)\n"
"v_mfma_f32_16x16x32_bf16 v[100:103], v[108:111], v[72:75], v[100:103]\n"
"s_waitcnt lgkmcnt(3)\n"
"v_mfma_f32_16x16x32_bf16 v[104:107], v[112:115], v[72:75], v[104:107]\n"
"s_waitcnt lgkmcnt(2)\n"
"v_mfma_f32_16x16x32_bf16 v[96:99], v[116:119], v[76:79], v[96:99]\n"
"s_waitcnt lgkmcnt(1)\n"
"v_mfma_f32_16x16x32_bf16 v[100:103], v[120:123], v[76:79], v[100:103]\n"
"s_waitcnt lgkmcnt(0)\n"
"v_mfma_f32_16x16x32_bf16 v[104:107], v[32:35], v[76:79], v[104:107]\n"
"v_and_b32 v11, 0x30, v1\n"
"v_add_u32 v11, 161664, v11\n"
"ds_read_b128 v[44:47], v11 offset:256\n"
"ds_read_b128 v[48:51], v11 offset:704\n"
"ds_read_b128 v[52:55], v11 offset:320\n"
"ds_read_b128 v[56:59], v11 offset:768\n"
"ds_read_b128 v[60:63], v11 offset:384\n"
"ds_read_b128 v[64:67], v11 offset:832\n"
"s_waitcnt vmcnt(0)\n"
"s_waitcnt lgkmcnt(0)\n"
"s_barrier\n"
"v_mov_b32 v116, 0xc0135761\n"
"s_add_u32 m0, s67, 114688\n"
"s_add_u32 s95, s66, 0\n"
"buffer_load_dwordx4 v2, s[32:35], s95 offen offset:3072 sc1 lds\n"
"buffer_load_dwordx4 v2, s[32:35], s95 offen offset:1024 sc1 lds\n"
"buffer_load_dwordx4 v2, s[32:35], s95 offen offset:2048 sc1 lds\n"
"s_bitcmp1_b32 s74, 1\n"
"s_cbranch_scc1 L166_%=\n"
"buffer_load_dwordx4 v2, s[32:35], s95 offen offset:0 sc1 lds\n"
"L166_%=:\n"
"v_pk_fma_f32 v[96:97], v[96:97], v[8:9], v[44:45] op_sel_hi:[1,0,1]\n"
"v_pk_fma_f32 v[96:97], v[8:9], v[48:49], v[96:97] op_sel:[1,0,0] op_sel_hi:[1,1,1] neg_lo:[1,0,0] neg_hi:[1,0,0]\n"
"v_pk_fma_f32 v[98:99], v[98:99], v[8:9], v[46:47] op_sel_hi:[1,0,1]\n"
"v_pk_fma_f32 v[98:99], v[8:9], v[50:51], v[98:99] op_sel:[1,0,0] op_sel_hi:[1,1,1] neg_lo:[1,0,0] neg_hi:[1,0,0]\n"
"v_pk_fma_f32 v[100:101], v[100:101], v[8:9], v[52:53] op_sel_hi:[1,0,1]\n"
"v_pk_fma_f32 v[100:101], v[8:9], v[56:57], v[100:101] op_sel:[1,0,0] op_sel_hi:[1,1,1] neg_lo:[1,0,0] neg_hi:[1,0,0]\n"
"v_pk_fma_f32 v[102:103], v[102:103], v[8:9], v[54:55] op_sel_hi:[1,0,1]\n"
"v_pk_fma_f32 v[102:103], v[8:9], v[58:59], v[102:103] op_sel:[1,0,0] op_sel_hi:[1,1,1] neg_lo:[1,0,0] neg_hi:[1,0,0]\n"
"v_pk_fma_f32 v[104:105], v[104:105], v[8:9], v[60:61] op_sel_hi:[1,0,1]\n"
"v_pk_fma_f32 v[104:105], v[8:9], v[64:65], v[104:105] op_sel:[1,0,0] op_sel_hi:[1,1,1] neg_lo:[1,0,0] neg_hi:[1,0,0]\n"
"v_pk_fma_f32 v[106:107], v[106:107], v[8:9], v[62:63] op_sel_hi:[1,0,1]\n"
"v_pk_fma_f32 v[106:107], v[8:9], v[66:67], v[106:107] op_sel:[1,0,0] op_sel_hi:[1,1,1] neg_lo:[1,0,0] neg_hi:[1,0,0]\n"
"v_pk_mul_f32 v[108:109], v[96:97], v[96:97]\n"
"v_pk_mul_f32 v[110:111], v[98:99], v[98:99]\n"
"s_add_u32 m0, s67, 0\n"
"s_add_u32 s95, s66, 7168\n"
"buffer_load_dwordx4 v2, s[32:35], s95 offen offset:0 sc1 lds\n"
"v_pk_fma_f32 v[108:109], v[108:109], s[2:3], v[116:117] op_sel_hi:[1,0,0]\n"
"v_pk_fma_f32 v[110:111], v[110:111], s[2:3], v[116:117] op_sel_hi:[1,0,0]\n"
"v_pk_mul_f32 v[108:109], v[108:109], v[96:97]\n"
"v_pk_mul_f32 v[110:111], v[110:111], v[98:99]\n"
"v_exp_f32 v108, v108\n"
"v_exp_f32 v109, v109\n"
"v_exp_f32 v110, v110\n"
"buffer_load_dwordx4 v2, s[32:35], s95 offen offset:1024 sc1 lds\n"
"v_exp_f32 v111, v111\n"
"v_pk_add_f32 v[108:109], v[108:109], 1.0 op_sel_hi:[1,0]\n"
"v_pk_add_f32 v[110:111], v[110:111], 1.0 op_sel_hi:[1,0]\n"
"v_rcp_f32 v108, v108\n"
"v_rcp_f32 v109, v109\n"
"v_rcp_f32 v110, v110\n"
"v_rcp_f32 v111, v111\n"
"buffer_load_dwordx4 v2, s[32:35], s95 offen offset:2048 sc1 lds\n"
"v_pk_mul_f32 v[96:97], v[96:97], v[108:109]\n"
"v_pk_mul_f32 v[98:99], v[98:99], v[110:111]\n"
"v_pk_mul_f32 v[112:113], v[100:101], v[100:101]\n"
"v_pk_mul_f32 v[114:115], v[102:103], v[102:103]\n"
"v_pk_fma_f32 v[112:113], v[112:113], s[2:3], v[116:117] op_sel_hi:[1,0,0]\n"
"v_pk_fma_f32 v[114:115], v[114:115], s[2:3], v[116:117] op_sel_hi:[1,0,0]\n"
"s_bitcmp0_b32 s74, 1\n"
"s_cbranch_scc1 L167_%=\n"
"buffer_load_dwordx4 v2, s[32:35], s95 offen offset:3072 sc1 lds\n"
"L167_%=:\n"
"v_pk_mul_f32 v[112:113], v[112:113], v[100:101]\n"
"v_pk_mul_f32 v[114:115], v[114:115], v[102:103]\n"
"v_exp_f32 v112, v112\n"
"v_exp_f32 v113, v113\n"
"v_exp_f32 v114, v114\n"
"v_exp_f32 v115, v115\n"
"v_pk_add_f32 v[112:113], v[112:113], 1.0 op_sel_hi:[1,0]\n"
"s_add_u32 m0, s67, 28672\n"
"s_add_u32 s95, s66, 14336\n"
"buffer_load_dwordx4 v2, s[32:35], s95 offen offset:3072 sc1 lds\n"
"v_pk_add_f32 v[114:115], v[114:115], 1.0 op_sel_hi:[1,0]\n"
"v_rcp_f32 v112, v112\n"
"v_rcp_f32 v113, v113\n"
"v_rcp_f32 v114, v114\n"
"v_rcp_f32 v115, v115\n"
"v_pk_mul_f32 v[100:101], v[100:101], v[112:113]\n"
"v_pk_mul_f32 v[102:103], v[102:103], v[114:115]\n"
"buffer_load_dwordx4 v2, s[32:35], s95 offen offset:1024 sc1 lds\n"
"v_pk_mul_f32 v[108:109], v[104:105], v[104:105]\n"
"v_pk_mul_f32 v[110:111], v[106:107], v[106:107]\n"
"v_pk_fma_f32 v[108:109], v[108:109], s[2:3], v[116:117] op_sel_hi:[1,0,0]\n"
"v_pk_fma_f32 v[110:111], v[110:111], s[2:3], v[116:117] op_sel_hi:[1,0,0]\n"
"v_pk_mul_f32 v[108:109], v[108:109], v[104:105]\n"
"v_pk_mul_f32 v[110:111], v[110:111], v[106:107]\n"
"v_exp_f32 v108, v108\n"
"buffer_load_dwordx4 v2, s[32:35], s95 offen offset:2048 sc1 lds\n"
"v_exp_f32 v109, v109\n"
"v_exp_f32 v110, v110\n"
"v_exp_f32 v111, v111\n"
"v_pk_add_f32 v[108:109], v[108:109], 1.0 op_sel_hi:[1,0]\n"
"v_pk_add_f32 v[110:111], v[110:111], 1.0 op_sel_hi:[1,0]\n"
"v_rcp_f32 v108, v108\n"
"s_bitcmp1_b32 s74, 1\n"
"s_cbranch_scc1 L168_%=\n"
"buffer_load_dwordx4 v2, s[32:35], s95 offen offset:0 sc1 lds\n"
"L168_%=:\n"
"v_add_u32 v3, s68, v2\n"
"v_add_u32 v4, 0x10000, v3\n"
"v_add_u32 v5, 0x20000, v3\n"
"s_waitcnt vmcnt(7)\n"
"s_barrier\n"
"v_rcp_f32 v109, v109\n"
"v_rcp_f32 v110, v110\n"
"ds_read_b128 v[16:19], v4 offset:49152\n"
"v_rcp_f32 v111, v111\n"
"ds_read_b128 v[20:23], v4 offset:50176\n"
"s_add_u32 m0, s67, 57344\n"
"s_add_u32 s95, s66, 21504\n"
"buffer_load_dwordx4 v2, s[32:35], s95 offen offset:0 sc1 lds\n"
"v_pk_mul_f32 v[104:105], v[104:105], v[108:109]\n"
"v_pk_mul_f32 v[106:107], v[106:107], v[110:111]\n"
"ds_read_b128 v[24:27], v4 offset:51200\n"
"v_cvt_pk_bf16_f32 v120, v96, v97\n"
"buffer_load_dwordx4 v2, s[32:35], s95 offen offset:1024 sc1 lds\n"
"ds_read_b128 v[28:31], v4 offset:52224\n"
"v_cvt_pk_bf16_f32 v121, v98, v99\n"
"v_cvt_pk_bf16_f32 v122, v100, v101\n"
"ds_read_b128 v[32:35], v4 offset:53248\n"
"v_cvt_pk_bf16_f32 v123, v102, v103\n"
"buffer_load_dwordx4 v2, s[32:35], s95 offen offset:2048 sc1 lds\n"
"ds_read_b128 v[36:39], v4 offset:54272\n"
"v_cvt_pk_bf16_f32 v124, v104, v105\n"
"ds_read_b128 v[40:43], v4 offset:55296\n"
"v_cvt_pk_bf16_f32 v125, v106, v107\n"
"s_bitcmp0_b32 s74, 1\n"
"s_cbranch_scc1 L169_%=\n"
"buffer_load_dwordx4 v2, s[32:35], s95 offen offset:3072 sc1 lds\n"
"L169_%=:\n"
"s_waitcnt lgkmcnt(0)\n"
"s_nop 15\n"
"s_branch L119_%=\n"
"L118_%=:\n"
"ds_read_b128 v[108:111], v3 offset:5120\n"
"ds_read_b128 v[112:115], v3 offset:6144\n"
"ds_read_b128 v[116:119], v3 offset:7168\n"
"ds_read_b128 v[120:123], v3 offset:12416\n"
"ds_read_b128 v[124:127], v3 offset:13440\n"
"ds_read_b128 v[92:95], v3 offset:14464\n"
"ds_read_b128 v[96:99], v3 offset:19712\n"
"ds_read_b128 v[100:103], v3 offset:20736\n"
"s_waitcnt vmcnt(5)\n"
"buffer_load_dwordx4 v[16:19], v2, s[32:35], s87 offen offset:0 sc1\n"
"buffer_load_dwordx4 v[20:23], v2, s[32:35], s87 offen offset:1024 sc1\n"
"buffer_load_dwordx4 v[24:27], v2, s[32:35], s87 offen offset:2048 sc1\n"
"buffer_load_dwordx4 v[28:31], v2, s[32:35], s87 offen offset:3072 sc1\n"
"s_waitcnt vmcnt(2)\n"
"v_max3_u32 v0, v17, v19, v21\n"
"v_max_u32 v0, v23, v0\n"
"v_cmp_eq_u32 vcc, -1, v0\n"
"s_nop 0\n"
"s_cbranch_vccnz RT170_%=\n"
"BK171_%=:\n"
"s_waitcnt lgkmcnt(7)\n"
"v_mfma_f32_16x16x32_bf16 v[80:83], v[16:19], v[108:111], 0\n"
"ds_read_b128 v[108:111], v3 offset:21760\n"
"s_waitcnt lgkmcnt(7)\n"
"v_mfma_f32_16x16x32_bf16 v[84:87], v[16:19], v[112:115], 0\n"
"ds_read_b128 v[112:115], v3 offset:27008\n"
"s_add_u32 s94, s87, 4096\n"
"buffer_load_dwordx4 v[32:35], v2, s[32:35], s94 offen offset:0 sc1\n"
"s_waitcnt lgkmcnt(7)\n"
"v_mfma_f32_16x16x32_bf16 v[88:91], v[16:19], v[116:119], 0\n"
"ds_read_b128 v[116:119], v3 offset:28032\n"
"s_add_u32 s94, s87, 4096\n"
"buffer_load_dwordx4 v[36:39], v2, s[32:35], s94 offen offset:1024 sc1\n"
"s_waitcnt lgkmcnt(7)\n"
"v_mfma_f32_16x16x32_bf16 v[80:83], v[20:23], v[120:123], v[80:83]\n"
"ds_read_b128 v[120:123], v3 offset:29056\n"
"s_waitcnt lgkmcnt(7)\n"
"v_mfma_f32_16x16x32_bf16 v[84:87], v[20:23], v[124:127], v[84:87]\n"
"ds_read_b128 v[124:127], v3 offset:34304\n"
"s_add_u32 m0, s20, 1024\n"
"s_add_u32 s95, s87, 7168\n"
"buffer_load_dwordx4 v2, s[32:35], s95 offen sc1 lds\n"
"s_waitcnt lgkmcnt(7)\n"
"v_mfma_f32_16x16x32_bf16 v[88:91], v[20:23], v[92:95], v[88:91]\n"
"ds_read_b128 v[92:95], v3 offset:35328\n"
"s_add_u32 m0, s20, 3072\n"
"s_add_u32 s95, s85, 1024\n"
"buffer_load_dwordx4 v2, s[32:35], s95 offen sc1 lds\n"
"s_waitcnt vmcnt(4)\n"
"v_max3_u32 v0, v25, v27, v29\n"
"v_max_u32 v0, v31, v0\n"
"v_cmp_eq_u32 vcc, -1, v0\n"
"s_nop 0\n"
"s_cbranch_vccnz RT175_%=\n"
"BK176_%=:\n"
"s_waitcnt lgkmcnt(7)\n"
"v_mfma_f32_16x16x32_bf16 v[80:83], v[24:27], v[96:99], v[80:83]\n"
"ds_read_b128 v[96:99], v3 offset:36352\n"
"s_waitcnt lgkmcnt(7)\n"
"v_mfma_f32_16x16x32_bf16 v[84:87], v[24:27], v[100:103], v[84:87]\n"
"ds_read_b128 v[100:103], v3 offset:41472\n"
"buffer_load_dwordx4 v[72:75], v2, s[32:35], s77 offen offset:0 sc1\n"
"s_waitcnt lgkmcnt(7)\n"
"v_mfma_f32_16x16x32_bf16 v[88:91], v[24:27], v[108:111], v[88:91]\n"
"ds_read_b128 v[108:111], v3 offset:42624\n"
"buffer_load_dwordx4 v[76:79], v2, s[32:35], s77 offen offset:1024 sc1\n"
"s_waitcnt lgkmcnt(7)\n"
"v_mfma_f32_16x16x32_bf16 v[80:83], v[28:31], v[112:115], v[80:83]\n"
"ds_read_b128 v[112:115], v3 offset:43648\n"
"s_waitcnt lgkmcnt(7)\n"
"v_mfma_f32_16x16x32_bf16 v[84:87], v[28:31], v[116:119], v[84:87]\n"
"ds_read_b128 v[116:119], v3 offset:48768\n"
"s_add_u32 m0, s21, 1024\n"
"s_add_u32 s95, s85, 3072\n"
"buffer_load_dwordx4 v2, s[32:35], s95 offen sc1 lds\n"
"s_waitcnt lgkmcnt(7)\n"
"v_mfma_f32_16x16x32_bf16 v[88:91], v[28:31], v[120:123], v[88:91]\n"
"ds_read_b128 v[120:123], v3 offset:49792\n"
"s_add_u32 m0, s21, 3072\n"
"s_add_u32 s95, s85, 5120\n"
"buffer_load_dwordx4 v2, s[32:35], s95 offen sc1 lds\n"
"s_waitcnt vmcnt(6)\n"
"v_max3_u32 v0, v33, v35, v37\n"
"v_max_u32 v0, v39, v0\n"
"v_cmp_eq_u32 vcc, -1, v0\n"
"s_nop 0\n"
"s_cbranch_vccnz RT180_%=\n"
"BK181_%=:\n"
"s_waitcnt lgkmcnt(7)\n"
"v_mfma_f32_16x16x32_bf16 v[80:83], v[32:35], v[124:127], v[80:83]\n"
"ds_read_b128 v[124:127], v3 offset:50944\n"
"s_waitcnt lgkmcnt(7)\n"
"v_mfma_f32_16x16x32_bf16 v[84:87], v[32:35], v[92:95], v[84:87]\n"
"ds_read_b128 v[92:95], v3 offset:56064\n"
"s_add_u32 m0, s21, 5120\n"
"s_add_u32 s95, s85, 7168\n"
"buffer_load_dwordx4 v2, s[32:35], s95 offen sc1 lds\n"
"s_waitcnt lgkmcnt(7)\n"
"v_mfma_f32_16x16x32_bf16 v[88:91], v[32:35], v[96:99], v[88:91]\n"
"ds_read_b128 v[96:99], v3 offset:57088\n"
"s_mul_i32 s95, s74, 2048\n"
"s_add_u32 m0, s95, 32768\n"
"s_add_u32 s95, s95, s83\n"
"s_add_u32 s95, s95, 163840\n"
"buffer_load_dwordx4 v2, s[32:35], s95 offen offset:0 sc1 lds\n"
"s_waitcnt vmcnt(6)\n"
"s_barrier\n"
"ds_read_b128 v[40:43], v8 offset:0\n"
"ds_read_b128 v[44:47], v8 offset:1024\n"
"ds_read_b128 v[48:51], v8 offset:2048\n"
"ds_read_b128 v[52:55], v8 offset:3072\n"
"s_waitcnt lgkmcnt(11)\n"
"v_mfma_f32_16x16x32_bf16 v[80:83], v[36:39], v[100:103], v[80:83]\n"
"ds_read_b128 v[100:103], v3 offset:58112\n"
"s_waitcnt lgkmcnt(11)\n"
"v_mfma_f32_16x16x32_bf16 v[84:87], v[36:39], v[108:111], v[84:87]\n"
"ds_read_b128 v[108:111], v3 offset:63360\n"
"s_waitcnt lgkmcnt(11)\n"
"v_mfma_f32_16x16x32_bf16 v[88:91], v[36:39], v[112:115], v[88:91]\n"
"ds_read_b128 v[112:115], v3 offset:64384\n"
"buffer_load_dwordx4 v2, s[32:35], s95 offen offset:1024 sc1 lds\n"
"s_waitcnt lgkmcnt(5)\n"
"v_max3_u32 v0, v41, v43, v45\n"
"v_max_u32 v0, v47, v0\n"
"v_cmp_eq_u32 vcc, -1, v0\n"
"s_nop 0\n"
"s_cbranch_vccnz RT184_%=\n"
"BK185_%=:\n"
"v_mfma_f32_16x16x32_bf16 v[80:83], v[40:43], v[116:119], v[80:83]\n"
"ds_read_b128 v[116:119], v3 offset:65408\n"
"v_mfma_f32_16x16x32_bf16 v[84:87], v[40:43], v[120:123], v[84:87]\n"
"ds_read_b128 v[120:123], v2 offset:3072\n"
"v_mfma_f32_16x16x32_bf16 v[88:91], v[40:43], v[124:127], v[88:91]\n"
"ds_read_b128 v[124:127], v2 offset:4096\n"
"v_mfma_f32_16x16x32_bf16 v[80:83], v[44:47], v[92:95], v[80:83]\n"
"ds_read_b128 v[92:95], v2 offset:5120\n"
"s_waitcnt vmcnt(6)\n"
"v_max3_u32 v0, v72, v73, v74\n"
"s_waitcnt vmcnt(5)\n"
"v_max3_u32 v0, v75, v76, v0\n"
"v_max3_u32 v0, v77, v78, v0\n"
"v_max_u32 v0, v79, v0\n"
"v_cmp_eq_u32 vcc, -1, v0\n"
"s_nop 0\n"
"s_cbranch_vccnz RT189_%=\n"
"BK190_%=:\n"
"v_pk_add_f32 v[72:73], v[72:73], v[76:77]\n"
"v_pk_add_f32 v[74:75], v[74:75], v[78:79]\n"
"v_mfma_f32_16x16x32_bf16 v[84:87], v[44:47], v[96:99], v[84:87]\n"
"ds_read_b128 v[96:99], v2 offset:10240\n"
"v_permlane32_swap_b32 v72, v74\n"
"v_permlane32_swap_b32 v73, v75\n"
"v_pk_add_f32 v[72:73], v[72:73], v[74:75]\n"
"s_waitcnt lgkmcnt(7)\n"
"v_mfma_f32_16x16x32_bf16 v[88:91], v[44:47], v[100:103], v[88:91]\n"
"ds_read_b128 v[100:103], v2 offset:11264\n"
"v_mov_b64 v[74:75], v[72:73]\n"
"s_nop 1\n"
"v_permlane16_swap_b32 v72, v74\n"
"v_permlane16_swap_b32 v73, v75\n"
"v_pk_add_f32 v[72:73], v[72:73], v[74:75]\n"
"v_max3_u32 v0, v49, v51, v53\n"
"v_max_u32 v0, v55, v0\n"
"v_cmp_eq_u32 vcc, -1, v0\n"
"s_nop 0\n"
"s_cbranch_vccnz RT193_%=\n"
"BK194_%=:\n"
"s_waitcnt lgkmcnt(7)\n"
"v_mfma_f32_16x16x32_bf16 v[80:83], v[48:51], v[108:111], v[80:83]\n"
"ds_read_b128 v[108:111], v2 offset:12288\n"
"v_add_f32_dpp v72, v72, v72 row_ror:8 row_mask:0xf bank_mask:0xf\n"
"v_add_f32_dpp v73, v73, v73 row_ror:8 row_mask:0xf bank_mask:0xf\n"
"v_mul_f32 v72, 0x3b000000, v72\n"
"s_waitcnt lgkmcnt(7)\n"
"v_mfma_f32_16x16x32_bf16 v[84:87], v[48:51], v[112:115], v[84:87]\n"
"ds_read_b128 v[112:115], v2 offset:17408\n"
"v_mul_f32 v73, 0x3b000000, v73\n"
"v_fma_f32 v73, -v72, v72, v73\n"
"v_max_f32 v73, 0, v73\n"
"s_waitcnt lgkmcnt(7)\n"
"v_mfma_f32_16x16x32_bf16 v[88:91], v[48:51], v[116:119], v[88:91]\n"
"ds_read_b128 v[116:119], v2 offset:18432\n"
"v_add_f32 v73, 0x3727c5ac, v73\n"
"v_rsq_f32 v73, v73\n"
"s_nop 0\n"
"v_mul_f32 v72, v72, v73\n"
"ds_write2_b32 v5, v73, v72 offset0:0 offset1:16\n"
"s_waitcnt vmcnt(0)\n"
"s_barrier\n"
"ds_read_b128 v[56:59], v9 offset:0\n"
"ds_read_b128 v[60:63], v9 offset:1024\n"
"ds_read_b128 v[64:67], v9 offset:2048\n"
"ds_read_b128 v[68:71], v9 offset:3072\n"
"ds_read_b128 v[72:75], v9 offset:4096\n"
"ds_read_b128 v[76:79], v9 offset:5120\n"
"s_waitcnt lgkmcnt(14)\n"
"v_mfma_f32_16x16x32_bf16 v[80:83], v[52:55], v[120:123], v[80:83]\n"
"ds_read_b128 v[120:123], v2 offset:19456\n"
"s_waitcnt lgkmcnt(14)\n"
"v_mfma_f32_16x16x32_bf16 v[84:87], v[52:55], v[124:127], v[84:87]\n"
"ds_read_b128 v[124:127], v2 offset:24576\n"
"s_waitcnt lgkmcnt(14)\n"
"v_mfma_f32_16x16x32_bf16 v[88:91], v[52:55], v[92:95], v[88:91]\n"
"ds_read_b128 v[92:95], v2 offset:25600\n"
"s_waitcnt lgkmcnt(7)\n"
"v_max3_u32 v0, v57, v59, v61\n"
"v_max_u32 v0, v63, v0\n"
"v_cmp_eq_u32 vcc, -1, v0\n"
"s_nop 0\n"
"s_cbranch_vccnz RT197_%=\n"
"BK198_%=:\n"
"v_mfma_f32_16x16x32_bf16 v[80:83], v[56:59], v[96:99], v[80:83]\n"
"ds_read_b128 v[96:99], v2 offset:26624\n"
"v_mfma_f32_16x16x32_bf16 v[84:87], v[56:59], v[100:103], v[84:87]\n"
"ds_read_b128 v[100:103], v2 offset:31744\n"
"v_mfma_f32_16x16x32_bf16 v[88:91], v[56:59], v[108:111], v[88:91]\n"
"ds_read_b128 v[108:111], v2 offset:32768\n"
"v_mfma_f32_16x16x32_bf16 v[80:83], v[60:63], v[112:115], v[80:83]\n"
"ds_read_b128 v[112:115], v2 offset:33792\n"
"v_mfma_f32_16x16x32_bf16 v[84:87], v[60:63], v[116:119], v[84:87]\n"
"ds_read_b128 v[116:119], v2 offset:38912\n"
"s_waitcnt lgkmcnt(7)\n"
"v_mfma_f32_16x16x32_bf16 v[88:91], v[60:63], v[120:123], v[88:91]\n"
"ds_read_b128 v[120:123], v2 offset:39936\n"
"s_mul_i32 s95, s74, 2048\n"
"s_add_u32 m0, s95, 49152\n"
"s_add_u32 s95, s95, s83\n"
"s_add_u32 s95, s95, 180224\n"
"buffer_load_dwordx4 v2, s[32:35], s95 offen offset:0 sc1 lds\n"
"v_max3_u32 v0, v65, v67, v69\n"
"v_max_u32 v0, v71, v0\n"
"v_cmp_eq_u32 vcc, -1, v0\n"
"s_nop 0\n"
"s_cbranch_vccnz RT203_%=\n"
"BK204_%=:\n"
"s_waitcnt lgkmcnt(7)\n"
"v_mfma_f32_16x16x32_bf16 v[80:83], v[64:67], v[124:127], v[80:83]\n"
"ds_read_b128 v[124:127], v2 offset:40960\n"
"s_waitcnt lgkmcnt(7)\n"
"v_mfma_f32_16x16x32_bf16 v[84:87], v[64:67], v[92:95], v[84:87]\n"
"ds_read_b128 v[92:95], v2 offset:46080\n"
"s_waitcnt lgkmcnt(7)\n"
"v_mfma_f32_16x16x32_bf16 v[88:91], v[64:67], v[96:99], v[88:91]\n"
"ds_read_b128 v[96:99], v2 offset:47104\n"
"buffer_load_dwordx4 v2, s[32:35], s95 offen offset:1024 sc1 lds\n"
"s_waitcnt lgkmcnt(7)\n"
"v_mfma_f32_16x16x32_bf16 v[80:83], v[68:71], v[100:103], v[80:83]\n"
"ds_read_b128 v[100:103], v2 offset:48128\n"
"s_waitcnt lgkmcnt(7)\n"
"v_mfma_f32_16x16x32_bf16 v[84:87], v[68:71], v[108:111], v[84:87]\n"
"s_waitcnt lgkmcnt(6)\n"
"v_mfma_f32_16x16x32_bf16 v[88:91], v[68:71], v[112:115], v[88:91]\n"
"s_mul_i32 s95, s74, 7168\n"
"s_add_u32 m0, s95, 66560\n"
"s_add_u32 s95, s95, s83\n"
"s_add_u32 s95, s95, 196608\n"
"buffer_load_dwordx4 v2, s[32:35], s95 offen offset:0 sc1 lds\n"
"v_max3_u32 v0, v73, v75, v77\n"
"v_max_u32 v0, v79, v0\n"
"v_cmp_eq_u32 vcc, -1, v0\n"
"s_nop 0\n"
"s_cbranch_vccnz RT208_%=\n"
"BK209_%=:\n"
"s_waitcnt lgkmcnt(5)\n"
"v_mfma_f32_16x16x32_bf16 v[80:83], v[72:75], v[116:119], v[80:83]\n"
"s_waitcnt lgkmcnt(4)\n"
"v_mfma_f32_16x16x32_bf16 v[84:87], v[72:75], v[120:123], v[84:87]\n"
"ds_read2_b32 v[8:9], v7 offset0:0 offset1:16\n"
"s_waitcnt lgkmcnt(4)\n"
"v_mfma_f32_16x16x32_bf16 v[88:91], v[72:75], v[124:127], v[88:91]\n"
"buffer_load_dwordx4 v2, s[32:35], s95 offen offset:1024 sc1 lds\n"
"s_waitcnt lgkmcnt(3)\n"
"v_mfma_f32_16x16x32_bf16 v[80:83], v[76:79], v[92:95], v[80:83]\n"
"s_waitcnt lgkmcnt(2)\n"
"v_mfma_f32_16x16x32_bf16 v[84:87], v[76:79], v[96:99], v[84:87]\n"
"v_and_b32 v11, 15, v1\n"
"v_lshlrev_b32 v11, 2, v11\n"
"v_add_u32 v11, 160768, v11\n"
"s_waitcnt lgkmcnt(1)\n"
"v_mfma_f32_16x16x32_bf16 v[88:91], v[76:79], v[100:103], v[88:91]\n"
"buffer_load_dwordx4 v2, s[32:35], s95 offen offset:2048 sc1 lds\n"
"ds_read_b128 v[92:95], v6\n"
"ds_read_b128 v[96:99], v6 offset:64\n"
"ds_read2_b32 v[108:109], v11 offset0:64 offset1:176\n"
"ds_read2_b32 v[110:111], v11 offset0:80 offset1:192\n"
"ds_read2_b32 v[112:113], v11 offset0:96 offset1:208\n"
"v_mov_b32 v124, 0xc0135761\n"
"s_waitcnt lgkmcnt(2)\n"
"v_pk_fma_f32 v[80:81], v[80:81], v[92:93], v[108:109] op_sel_hi:[1,1,0]\n"
"v_pk_fma_f32 v[80:81], v[108:109], v[96:97], v[80:81] op_sel:[1,0,0] op_sel_hi:[1,1,1] neg_lo:[1,0,0] neg_hi:[1,0,0]\n"
"v_pk_fma_f32 v[82:83], v[82:83], v[94:95], v[108:109] op_sel_hi:[1,1,0]\n"
"v_pk_fma_f32 v[82:83], v[108:109], v[98:99], v[82:83] op_sel:[1,0,0] op_sel_hi:[1,1,1] neg_lo:[1,0,0] neg_hi:[1,0,0]\n"
"s_waitcnt lgkmcnt(1)\n"
"v_pk_fma_f32 v[84:85], v[84:85], v[92:93], v[110:111] op_sel_hi:[1,1,0]\n"
"v_pk_fma_f32 v[84:85], v[110:111], v[96:97], v[84:85] op_sel:[1,0,0] op_sel_hi:[1,1,1] neg_lo:[1,0,0] neg_hi:[1,0,0]\n"
"v_pk_fma_f32 v[86:87], v[86:87], v[94:95], v[110:111] op_sel_hi:[1,1,0]\n"
"v_pk_fma_f32 v[86:87], v[110:111], v[98:99], v[86:87] op_sel:[1,0,0] op_sel_hi:[1,1,1] neg_lo:[1,0,0] neg_hi:[1,0,0]\n"
"s_waitcnt lgkmcnt(0)\n"
"v_pk_fma_f32 v[88:89], v[88:89], v[92:93], v[112:113] op_sel_hi:[1,1,0]\n"
"v_pk_fma_f32 v[88:89], v[112:113], v[96:97], v[88:89] op_sel:[1,0,0] op_sel_hi:[1,1,1] neg_lo:[1,0,0] neg_hi:[1,0,0]\n"
"buffer_load_dwordx4 v2, s[32:35], s95 offen offset:3072 sc1 lds\n"
"v_pk_fma_f32 v[90:91], v[90:91], v[94:95], v[112:113] op_sel_hi:[1,1,0]\n"
"v_pk_fma_f32 v[90:91], v[112:113], v[98:99], v[90:91] op_sel:[1,0,0] op_sel_hi:[1,1,1] neg_lo:[1,0,0] neg_hi:[1,0,0]\n"
"v_pk_mul_f32 v[116:117], v[80:81], v[80:81]\n"
"v_pk_mul_f32 v[118:119], v[82:83], v[82:83]\n"
"v_pk_fma_f32 v[116:117], v[116:117], s[2:3], v[124:125] op_sel_hi:[1,0,0]\n"
"v_pk_fma_f32 v[118:119], v[118:119], s[2:3], v[124:125] op_sel_hi:[1,0,0]\n"
"v_pk_mul_f32 v[116:117], v[116:117], v[80:81]\n"
"v_pk_mul_f32 v[118:119], v[118:119], v[82:83]\n"
"v_exp_f32 v116, v116\n"
"s_mul_i32 s95, s74, 7168\n"
"s_add_u32 m0, s95, 70656\n"
"s_add_u32 s95, s95, s83\n"
"s_add_u32 s95, s95, 200704\n"
"buffer_load_dwordx4 v2, s[32:35], s95 offen offset:0 sc1 lds\n"
"v_exp_f32 v117, v117\n"
"v_exp_f32 v118, v118\n"
"v_exp_f32 v119, v119\n"
"v_pk_add_f32 v[116:117], v[116:117], 1.0 op_sel_hi:[1,0]\n"
"v_pk_add_f32 v[118:119], v[118:119], 1.0 op_sel_hi:[1,0]\n"
"v_rcp_f32 v116, v116\n"
"v_rcp_f32 v117, v117\n"
"v_rcp_f32 v118, v118\n"
"v_rcp_f32 v119, v119\n"
"v_pk_mul_f32 v[80:81], v[80:81], v[116:117]\n"
"buffer_load_dwordx4 v2, s[32:35], s95 offen offset:1024 sc1 lds\n"
"v_pk_mul_f32 v[82:83], v[82:83], v[118:119]\n"
"v_pk_mul_f32 v[120:121], v[84:85], v[84:85]\n"
"v_pk_mul_f32 v[122:123], v[86:87], v[86:87]\n"
"v_pk_fma_f32 v[120:121], v[120:121], s[2:3], v[124:125] op_sel_hi:[1,0,0]\n"
"v_pk_fma_f32 v[122:123], v[122:123], s[2:3], v[124:125] op_sel_hi:[1,0,0]\n"
"v_pk_mul_f32 v[120:121], v[120:121], v[84:85]\n"
"v_pk_mul_f32 v[122:123], v[122:123], v[86:87]\n"
"v_exp_f32 v120, v120\n"
"v_exp_f32 v121, v121\n"
"buffer_load_dwordx4 v2, s[32:35], s95 offen offset:2048 sc1 lds\n"
"v_exp_f32 v122, v122\n"
"v_exp_f32 v123, v123\n"
"v_pk_add_f32 v[120:121], v[120:121], 1.0 op_sel_hi:[1,0]\n"
"v_pk_add_f32 v[122:123], v[122:123], 1.0 op_sel_hi:[1,0]\n"
"v_rcp_f32 v120, v120\n"
"v_rcp_f32 v121, v121\n"
"v_rcp_f32 v122, v122\n"
"v_rcp_f32 v123, v123\n"
"v_pk_mul_f32 v[84:85], v[84:85], v[120:121]\n"
"v_pk_mul_f32 v[86:87], v[86:87], v[122:123]\n"
"v_pk_mul_f32 v[116:117], v[88:89], v[88:89]\n"
"v_pk_mul_f32 v[118:119], v[90:91], v[90:91]\n"
"v_pk_fma_f32 v[116:117], v[116:117], s[2:3], v[124:125] op_sel_hi:[1,0,0]\n"
"v_pk_fma_f32 v[118:119], v[118:119], s[2:3], v[124:125] op_sel_hi:[1,0,0]\n"
"v_pk_mul_f32 v[116:117], v[116:117], v[88:89]\n"
"v_pk_mul_f32 v[118:119], v[118:119], v[90:91]\n"
"v_exp_f32 v116, v116\n"
"v_exp_f32 v117, v117\n"
"v_exp_f32 v118, v118\n"
"v_exp_f32 v119, v119\n"
"v_pk_add_f32 v[116:117], v[116:117], 1.0 op_sel_hi:[1,0]\n"
"v_pk_add_f32 v[118:119], v[118:119], 1.0 op_sel_hi:[1,0]\n"
"v_rcp_f32 v116, v116\n"
"v_rcp_f32 v117, v117\n"
"v_rcp_f32 v118, v118\n"
"v_rcp_f32 v119, v119\n"
"v_pk_mul_f32 v[88:89], v[88:89], v[116:117]\n"
"v_pk_mul_f32 v[90:91], v[90:91], v[118:119]\n"
"v_pk_add_f32 v[92:93], v[80:81], v[84:85]\n"
"v_pk_mul_f32 v[96:97], v[80:81], v[80:81]\n"
"v_pk_add_f32 v[94:95], v[82:83], v[86:87]\n"
"v_pk_mul_f32 v[98:99], v[82:83], v[82:83]\n"
"v_pk_fma_f32 v[96:97], v[84:85], v[84:85], v[96:97]\n"
"v_pk_fma_f32 v[98:99], v[86:87], v[86:87], v[98:99]\n"
"v_pk_add_f32 v[92:93], v[92:93], v[88:89]\n"
"v_pk_fma_f32 v[96:97], v[88:89], v[88:89], v[96:97]\n"
"v_pk_add_f32 v[94:95], v[94:95], v[90:91]\n"
"v_pk_fma_f32 v[98:99], v[90:91], v[90:91], v[98:99]\n"
"v_and_b32 v116, 0x30, v1\n"
"v_lshlrev_b32 v116, 1, v116\n"
"v_and_b32 v117, 1, v1\n"
"v_cmp_eq_u32 vcc, 0, v117\n"
"v_lshl_or_b32 v116, v117, 4, v116\n"
"v_add_f32_dpp v92, v92, v92 row_ror:8 row_mask:0xf bank_mask:0x3\n"
"v_add_f32_dpp v93, v93, v93 row_ror:8 row_mask:0xf bank_mask:0x3\n"
"v_add_f32_dpp v94, v94, v94 row_ror:8 row_mask:0xf bank_mask:0x3\n"
"v_add_f32_dpp v95, v95, v95 row_ror:8 row_mask:0xf bank_mask:0x3\n"
"v_add_f32_dpp v92, v96, v96 row_ror:8 row_mask:0xf bank_mask:0xc\n"
"v_add_f32_dpp v93, v97, v97 row_ror:8 row_mask:0xf bank_mask:0xc\n"
"v_add_f32_dpp v94, v98, v98 row_ror:8 row_mask:0xf bank_mask:0xc\n"
"v_add_f32_dpp v95, v99, v99 row_ror:8 row_mask:0xf bank_mask:0xc\n"
"v_bfe_u32 v117, v1, 2, 1\n"
"v_lshl_or_b32 v116, v117, 3, v116\n"
"v_add_f32_dpp v92, v92, v92 row_half_mirror row_mask:0xf bank_mask:0x5\n"
"v_add_f32_dpp v94, v94, v94 row_half_mirror row_mask:0xf bank_mask:0x5\n"
"s_nop 0\n"
"v_add_f32_dpp v92, v93, v93 row_half_mirror row_mask:0xf bank_mask:0xa\n"
"v_add_f32_dpp v94, v95, v95 row_half_mirror row_mask:0xf bank_mask:0xa\n"
"v_bfe_u32 v117, v1, 3, 1\n"
"v_lshl_or_b32 v116, v117, 2, v116\n"
"v_add_f32_dpp v92, v92, v92 quad_perm:[1,0,3,2] row_mask:0xf bank_mask:0xf\n"
"v_add_f32_dpp v94, v94, v94 quad_perm:[1,0,3,2] row_mask:0xf bank_mask:0xf\n"
"s_nop 0\n"
"v_add_f32_dpp v92, v92, v92 quad_perm:[2,3,0,1] row_mask:0xf bank_mask:0xf\n"
"v_add_f32_dpp v94, v94, v94 quad_perm:[2,3,0,1] row_mask:0xf bank_mask:0xf\n"
"s_add_u32 s88, s88, 262144\n"
"s_add_u32 s77, s77, 262144\n"
"v_cndmask_b32 v92, v94, v92, vcc\n"
"buffer_store_dword v92, v116, s[32:35], s88 offen sc1\n"
"s_waitcnt vmcnt(1)\n"
"s_barrier\n"
"ds_read_b128 v[108:111], v2 offset:49152\n"
"ds_read_b128 v[112:115], v2 offset:50176\n"
"ds_read_b128 v[116:119], v2 offset:51200\n"
"ds_read_b128 v[120:123], v2 offset:52224\n"
"ds_read_b128 v[124:127], v2 offset:56320\n"
"s_waitcnt lgkmcnt(4)\n"
"v_mfma_f32_16x16x32_bf16 v[92:95], v[108:111], v[16:19], 0\n"
"ds_read_b128 v[108:111], v2 offset:57344\n"
"s_waitcnt lgkmcnt(4)\n"
"v_mfma_f32_16x16x32_bf16 v[96:99], v[112:115], v[16:19], 0\n"
"ds_read_b128 v[112:115], v2 offset:58368\n"
"s_mul_i32 s95, s74, 5120\n"
"s_add_u32 m0, s95, 0\n"
"s_add_u32 s95, s95, s83\n"
"s_add_u32 s95, s95, 253952\n"
"buffer_load_dwordx4 v2, s[32:35], s95 offen offset:0 sc1 lds\n"
"s_waitcnt lgkmcnt(4)\n"
"v_mfma_f32_16x16x32_bf16 v[100:103], v[116:119], v[16:19], 0\n"
"ds_read_b128 v[116:119], v2 offset:59392\n"
"s_waitcnt lgkmcnt(4)\n"
"v_mfma_f32_16x16x32_bf16 v[104:107], v[120:123], v[16:19], 0\n"
"ds_read_b128 v[120:123], v2 offset:63488\n"
"buffer_load_dwordx4 v2, s[32:35], s95 offen offset:1024 sc1 lds\n"
"s_waitcnt lgkmcnt(4)\n"
"v_mfma_f32_16x16x32_bf16 v[92:95], v[124:127], v[20:23], v[92:95]\n"
"ds_read_b128 v[124:127], v2 offset:64512\n"
"s_waitcnt lgkmcnt(4)\n"
"v_mfma_f32_16x16x32_bf16 v[96:99], v[108:111], v[20:23], v[96:99]\n"
"ds_read_b128 v[108:111], v3 offset:1024\n"
"s_waitcnt lgkmcnt(4)\n"
"v_mfma_f32_16x16x32_bf16 v[100:103], v[112:115], v[20:23], v[100:103]\n"
"ds_read_b128 v[112:115], v3 offset:2048\n"
"s_waitcnt lgkmcnt(4)\n"
"v_mfma_f32_16x16x32_bf16 v[104:107], v[116:119], v[20:23], v[104:107]\n"
"ds_read_b128 v[116:119], v3 offset:6144\n"
"s_waitcnt lgkmcnt(4)\n"
"v_mfma_f32_16x16x32_bf16 v[92:95], v[120:123], v[24:27], v[92:95]\n"
"ds_read_b128 v[120:123], v3 offset:7168\n"
"s_waitcnt lgkmcnt(4)\n"
"v_mfma_f32_16x16x32_bf16 v[96:99], v[124:127], v[24:27], v[96:99]\n"
"ds_read_b128 v[124:127], v3 offset:8192\n"
"s_waitcnt lgkmcnt(4)\n"
"v_mfma_f32_16x16x32_bf16 v[100:103], v[108:111], v[24:27], v[100:103]\n"
"ds_read_b128 v[108:111], v3 offset:9216\n"
"s_waitcnt lgkmcnt(4)\n"
"v_mfma_f32_16x16x32_bf16 v[104:107], v[112:115], v[24:27], v[104:107]\n"
"ds_read_b128 v[112:115], v3 offset:13312\n"
"s_lshr_b32 s90, s82, 15\n"
"s_lshl_b32 s89, s76, 6\n"
"s_add_u32 s89, s89, s90\n"
"s_mul_i32 s89, s89, 7168\n"
"s_and_b32 s90, s86, 1\n"
"s_lshl_b32 s90, s90, 9\n"
"s_add_u32 s89, s89, s90\n"
"s_add_u32 s88, s89, 13635584\n"
"s_waitcnt lgkmcnt(4)\n"
"v_mfma_f32_16x16x32_bf16 v[92:95], v[116:119], v[28:31], v[92:95]\n"
"ds_read_b128 v[116:119], v3 offset:14336\n"
"s_waitcnt lgkmcnt(4)\n"
"v_mfma_f32_16x16x32_bf16 v[96:99], v[120:123], v[28:31], v[96:99]\n"
"ds_read_b128 v[120:123], v3 offset:15360\n"
"v_bfe_u32 v11, v1, 4, 1\n"
"v_lshlrev_b32 v11, 10, v11\n"
"v_lshrrev_b32 v24, 5, v1\n"
"s_waitcnt lgkmcnt(4)\n"
"v_mfma_f32_16x16x32_bf16 v[100:103], v[124:127], v[28:31], v[100:103]\n"
"ds_read_b128 v[124:127], v3 offset:16384\n"
"s_waitcnt lgkmcnt(4)\n"
"v_mfma_f32_16x16x32_bf16 v[104:107], v[108:111], v[28:31], v[104:107]\n"
"ds_read_b128 v[108:111], v3 offset:20480\n"
"v_lshl_or_b32 v11, v24, 8, v11\n"
"v_and_b32 v24, 0xf0, v2\n"
"v_or_b32 v11, v11, v24\n"
"s_waitcnt lgkmcnt(4)\n"
"v_mfma_f32_16x16x32_bf16 v[92:95], v[112:115], v[32:35], v[92:95]\n"
"ds_read_b128 v[112:115], v3 offset:21504\n"
"s_waitcnt lgkmcnt(4)\n"
"v_mfma_f32_16x16x32_bf16 v[96:99], v[116:119], v[32:35], v[96:99]\n"
"ds_read_b128 v[116:119], v3 offset:22528\n"
"s_waitcnt vmcnt(2)\n"
"s_waitcnt lgkmcnt(4)\n"
"v_mfma_f32_16x16x32_bf16 v[100:103], v[120:123], v[32:35], v[100:103]\n"
"ds_read_b128 v[120:123], v3 offset:23552\n"
"buffer_load_dwordx4 v[16:19], v2, s[32:35], s77 offen offset:0 sc1\n"
"s_waitcnt lgkmcnt(4)\n"
"v_mfma_f32_16x16x32_bf16 v[104:107], v[124:127], v[32:35], v[104:107]\n"
"ds_read_b128 v[124:127], v3 offset:27648\n"
"buffer_load_dwordx4 v[20:23], v2, s[32:35], s77 offen offset:1024 sc1\n"
"s_waitcnt lgkmcnt(4)\n"
"v_mfma_f32_16x16x32_bf16 v[92:95], v[108:111], v[36:39], v[92:95]\n"
"ds_read_b128 v[108:111], v3 offset:28672\n"
"s_waitcnt lgkmcnt(4)\n"
"v_mfma_f32_16x16x32_bf16 v[96:99], v[112:115], v[36:39], v[96:99]\n"
"ds_read_b128 v[112:115], v3 offset:29696\n"
"buffer_load_dwordx4 v2, s[32:35], s95 offen offset:2048 sc1 lds\n"
"s_waitcnt lgkmcnt(4)\n"
"v_mfma_f32_16x16x32_bf16 v[100:103], v[116:119], v[36:39], v[100:103]\n"
"ds_read_b128 v[116:119], v3 offset:30720\n"
"buffer_load_dwordx4 v2, s[32:35], s95 offen offset:3072 sc1 lds\n"
"s_waitcnt lgkmcnt(4)\n"
"v_mfma_f32_16x16x32_bf16 v[104:107], v[120:123], v[36:39], v[104:107]\n"
"ds_read_b128 v[120:123], v3 offset:34816\n"
"s_mul_i32 s95, s74, 5120\n"
"s_add_u32 m0, s95, 4096\n"
"s_add_u32 s95, s95, s83\n"
"s_add_u32 s95, s95, 258048\n"
"buffer_load_dwordx4 v2, s[32:35], s95 offen offset:0 sc1 lds\n"
"s_waitcnt lgkmcnt(4)\n"
"v_mfma_f32_16x16x32_bf16 v[92:95], v[124:127], v[40:43], v[92:95]\n"
"ds_read_b128 v[124:127], v3 offset:35840\n"
"s_waitcnt lgkmcnt(4)\n"
"v_mfma_f32_16x16x32_bf16 v[96:99], v[108:111], v[40:43], v[96:99]\n"
"ds_read_b128 v[108:111], v3 offset:36864\n"
"s_waitcnt lgkmcnt(4)\n"
"v_mfma_f32_16x16x32_bf16 v[100:103], v[112:115], v[40:43], v[100:103]\n"
"ds_read_b128 v[112:115], v3 offset:37888\n"
"s_waitcnt lgkmcnt(4)\n"
"v_mfma_f32_16x16x32_bf16 v[104:107], v[116:119], v[40:43], v[104:107]\n"
"ds_read_b128 v[116:119], v3 offset:41984\n"
"s_waitcnt lgkmcnt(4)\n"
"v_mfma_f32_16x16x32_bf16 v[92:95], v[120:123], v[44:47], v[92:95]\n"
"ds_read_b128 v[120:123], v3 offset:43008\n"
"s_waitcnt vmcnt(4)\n"
"v_max3_u32 v0, v16, v17, v18\n"
"s_waitcnt vmcnt(3)\n"
"v_max3_u32 v0, v19, v20, v0\n"
"v_max3_u32 v0, v21, v22, v0\n"
"v_max_u32 v0, v23, v0\n"
"v_cmp_eq_u32 vcc, -1, v0\n"
"s_nop 0\n"
"s_cbranch_vccnz RT212_%=\n"
"BK213_%=:\n"
"v_pk_add_f32 v[16:17], v[16:17], v[20:21]\n"
"v_pk_add_f32 v[18:19], v[18:19], v[22:23]\n"
"s_nop 1\n"
"v_permlane32_swap_b32 v16, v18\n"
"v_permlane32_swap_b32 v17, v19\n"
"v_pk_add_f32 v[16:17], v[16:17], v[18:19]\n"
"v_mov_b64 v[18:19], v[16:17]\n"
"s_nop 1\n"
"v_permlane16_swap_b32 v16, v18\n"
"v_permlane16_swap_b32 v17, v19\n"
"v_pk_add_f32 v[16:17], v[16:17], v[18:19]\n"
"s_waitcnt lgkmcnt(4)\n"
"v_mfma_f32_16x16x32_bf16 v[96:99], v[124:127], v[44:47], v[96:99]\n"
"ds_read_b128 v[124:127], v3 offset:44032\n"
"v_add_f32_dpp v16, v16, v16 row_ror:8 row_mask:0xf bank_mask:0xf\n"
"v_add_f32_dpp v17, v17, v17 row_ror:8 row_mask:0xf bank_mask:0xf\n"
"v_mul_f32 v16, 0x3a924925, v16\n"
"v_mul_f32 v17, 0x3a924925, v17\n"
"v_fma_f32 v17, -v16, v16, v17\n"
"v_max_f32 v17, 0, v17\n"
"v_add_f32 v17, 0x3727c5ac, v17\n"
"v_rsq_f32 v17, v17\n"
"s_nop 0\n"
"v_mul_f32 v16, v16, v17\n"
"ds_write2_b32 v5, v17, v16 offset0:0 offset1:16\n"
"ds_read_b128 v[16:19], v6\n"
"s_waitcnt lgkmcnt(6)\n"
"v_mfma_f32_16x16x32_bf16 v[100:103], v[108:111], v[44:47], v[100:103]\n"
"ds_read_b128 v[108:111], v3 offset:45056\n"
"ds_read_b128 v[20:23], v6 offset:64\n"
"s_waitcnt lgkmcnt(0)\n"
"v_pk_fma_f32 v[80:81], v[80:81], v[16:17], v[20:21] neg_lo:[0,0,1] neg_hi:[0,0,1]\n"
"v_pk_fma_f32 v[82:83], v[82:83], v[18:19], v[22:23] neg_lo:[0,0,1] neg_hi:[0,0,1]\n"
"v_cvt_pk_bf16_f32 v24, v80, v81\n"
"v_cvt_pk_bf16_f32 v25, v82, v83\n"
"v_pk_fma_f32 v[84:85], v[84:85], v[16:17], v[20:21] neg_lo:[0,0,1] neg_hi:[0,0,1]\n"
"v_pk_fma_f32 v[86:87], v[86:87], v[18:19], v[22:23] neg_lo:[0,0,1] neg_hi:[0,0,1]\n"
"v_cvt_pk_bf16_f32 v26, v84, v85\n"
"v_cvt_pk_bf16_f32 v27, v86, v87\n"
"s_nop 0\n"
"v_permlane16_swap_b32 v24, v26\n"
"v_permlane16_swap_b32 v25, v27\n"
"v_mfma_f32_16x16x32_bf16 v[104:107], v[112:115], v[44:47], v[104:107]\n"
"ds_read_b128 v[112:115], v3 offset:49152\n"
"buffer_store_dwordx4 v[24:27], v11, s[32:35], s88 offen offset:0 sc1\n"
"v_pk_fma_f32 v[88:89], v[88:89], v[16:17], v[20:21] neg_lo:[0,0,1] neg_hi:[0,0,1]\n"
"v_pk_fma_f32 v[90:91], v[90:91], v[18:19], v[22:23] neg_lo:[0,0,1] neg_hi:[0,0,1]\n"
"v_cvt_pk_bf16_f32 v28, v88, v89\n"
"v_cvt_pk_bf16_f32 v29, v90, v91\n"
"v_cvt_pk_bf16_f32 v30, v88, v89\n"
"v_cvt_pk_bf16_f32 v31, v90, v91\n"
"s_nop 0\n"
"v_permlane16_swap_b32 v28, v30\n"
"v_permlane16_swap_b32 v29, v31\n"
"s_mov_b64 exec, s[80:81]\n"
"buffer_store_dwordx4 v[28:31], v11, s[32:35], s88 offen offset:2048 sc1\n"
"s_mov_b64 exec, -1\n"
"v_mfma_f32_16x16x32_bf16 v[92:95], v[116:119], v[48:51], v[92:95]\n"
"ds_read_b128 v[32:35], v3 offset:50176\n"
"ds_read_b128 v[36:39], v3 offset:51200\n"
"v_mfma_f32_16x16x32_bf16 v[96:99], v[120:123], v[48:51], v[96:99]\n"
"ds_read_b128 v[40:43], v3 offset:52224\n"
"s_waitcnt vmcnt(2)\n"
"s_barrier\n"
"ds_read_b128 v[44:47], v3 offset:56320\n"
"v_mfma_f32_16x16x32_bf16 v[100:103], v[124:127], v[48:51], v[100:103]\n"
"ds_read_b128 v[116:119], v3 offset:57344\n"
"ds_read_b128 v[120:123], v2 offset:0\n"
"v_mfma_f32_16x16x32_bf16 v[104:107], v[108:111], v[48:51], v[104:107]\n"
"ds_read_b128 v[124:127], v2 offset:1024\n"
"ds_read_b128 v[108:111], v2 offset:5120\n"
"s_waitcnt lgkmcnt(8)\n"
"v_mfma_f32_16x16x32_bf16 v[92:95], v[112:115], v[52:55], v[92:95]\n"
"ds_read_b128 v[112:115], v2 offset:6144\n"
"s_waitcnt lgkmcnt(8)\n"
"v_mfma_f32_16x16x32_bf16 v[96:99], v[32:35], v[52:55], v[96:99]\n"
"ds_read_b128 v[32:35], v2 offset:7168\n"
"s_waitcnt lgkmcnt(8)\n"
"v_mfma_f32_16x16x32_bf16 v[100:103], v[36:39], v[52:55], v[100:103]\n"
"ds_read_b128 v[36:39], v2 offset:8192\n"
"s_waitcnt lgkmcnt(8)\n"
"v_mfma_f32_16x16x32_bf16 v[104:107], v[40:43], v[52:55], v[104:107]\n"
"ds_read_b128 v[40:43], v2 offset:12288\n"
"s_waitcnt lgkmcnt(8)\n"
"v_mfma_f32_16x16x32_bf16 v[92:95], v[44:47], v[56:59], v[92:95]\n"
"ds_read_b128 v[44:47], v2 offset:13312\n"
"s_waitcnt lgkmcnt(8)\n"
"v_mfma_f32_16x16x32_bf16 v[96:99], v[116:119], v[56:59], v[96:99]\n"
"ds_read_b128 v[116:119], v2 offset:14336\n"
"s_waitcnt lgkmcnt(8)\n"
"v_mfma_f32_16x16x32_bf16 v[100:103], v[120:123], v[56:59], v[100:103]\n"
"ds_read_b128 v[120:123], v2 offset:15360\n"
"s_waitcnt lgkmcnt(8)\n"
"v_mfma_f32_16x16x32_bf16 v[104:107], v[124:127], v[56:59], v[104:107]\n"
"ds_read_b128 v[124:127], v2 offset:19456\n"
"s_waitcnt lgkmcnt(8)\n"
"v_mfma_f32_16x16x32_bf16 v[92:95], v[108:111], v[60:63], v[92:95]\n"
"ds_read_b128 v[108:111], v2 offset:20480\n"
"s_waitcnt lgkmcnt(8)\n"
"v_mfma_f32_16x16x32_bf16 v[96:99], v[112:115], v[60:63], v[96:99]\n"
"ds_read_b128 v[112:115], v2 offset:21504\n"
"s_waitcnt lgkmcnt(8)\n"
"v_mfma_f32_16x16x32_bf16 v[100:103], v[32:35], v[60:63], v[100:103]\n"
"ds_read_b128 v[32:35], v2 offset:22528\n"
"s_waitcnt lgkmcnt(8)\n"
"v_mfma_f32_16x16x32_bf16 v[104:107], v[36:39], v[60:63], v[104:107]\n"
"ds_read_b128 v[36:39], v2 offset:26624\n"
"s_waitcnt lgkmcnt(8)\n"
"v_mfma_f32_16x16x32_bf16 v[92:95], v[40:43], v[64:67], v[92:95]\n"
"ds_read_b128 v[40:43], v2 offset:27648\n"
"s_waitcnt lgkmcnt(8)\n"
"v_mfma_f32_16x16x32_bf16 v[96:99], v[44:47], v[64:67], v[96:99]\n"
"ds_read_b128 v[44:47], v2 offset:28672\n"
"s_waitcnt lgkmcnt(8)\n"
"v_mfma_f32_16x16x32_bf16 v[100:103], v[116:119], v[64:67], v[100:103]\n"
"ds_read_b128 v[116:119], v2 offset:29696\n"
"s_waitcnt lgkmcnt(8)\n"
"v_mfma_f32_16x16x32_bf16 v[104:107], v[120:123], v[64:67], v[104:107]\n"
"ds_read_b128 v[120:123], v2 offset:33792\n"
"s_waitcnt lgkmcnt(8)\n"
"v_mfma_f32_16x16x32_bf16 v[92:95], v[124:127], v[68:71], v[92:95]\n"
"ds_read_b128 v[124:127], v2 offset:34816\n"
"s_waitcnt lgkmcnt(8)\n"
"v_mfma_f32_16x16x32_bf16 v[96:99], v[108:111], v[68:71], v[96:99]\n"
"ds_read_b128 v[108:111], v2 offset:35840\n"
"s_waitcnt lgkmcnt(8)\n"
"v_mfma_f32_16x16x32_bf16 v[100:103], v[112:115], v[68:71], v[100:103]\n"
"ds_read_b128 v[112:115], v2 offset:36864\n"
"s_waitcnt lgkmcnt(8)\n"
"v_mfma_f32_16x16x32_bf16 v[104:107], v[32:35], v[68:71], v[104:107]\n"
"s_and_b32 s89, s74, 1\n"
"s_lshl_b32 s90, s84, 1\n"
"s_add_u32 s89, s89, s90\n"
"s_mul_i32 s68, s89, 7168\n"
"s_lshl_b32 s90, s76, 4\n"
"s_add_u32 s89, s89, s90\n"
"s_lshl_b32 s69, s89, 8\n"
"s_add_u32 s69, s69, 131072\n"
"s_mul_i32 s85, s89, 28672\n"
"s_add_u32 s85, s85, 13631488\n"
"s_lshr_b32 s89, s75, 3\n"
"s_mul_i32 s89, s89, 114688\n"
"s_add_u32 s87, s85, s89\n"
"s_bfe_u32 s89, s74, 0x10001\n"
"s_mul_i32 s89, s89, 3072\n"
"s_add_u32 s66, s87, s89\n"
"s_add_u32 s85, s85, s89\n"
"s_add_u32 s67, s68, s89\n"
"s_waitcnt lgkmcnt(7)\n"
"v_mfma_f32_16x16x32_bf16 v[92:95], v[36:39], v[72:75], v[92:95]\n"
"s_waitcnt lgkmcnt(6)\n"
"v_mfma_f32_16x16x32_bf16 v[96:99], v[40:43], v[72:75], v[96:99]\n"
"s_waitcnt lgkmcnt(5)\n"
"v_mfma_f32_16x16x32_bf16 v[100:103], v[44:47], v[72:75], v[100:103]\n"
"s_waitcnt lgkmcnt(4)\n"
"v_mfma_f32_16x16x32_bf16 v[104:107], v[116:119], v[72:75], v[104:107]\n"
"s_waitcnt lgkmcnt(3)\n"
"v_mfma_f32_16x16x32_bf16 v[92:95], v[120:123], v[76:79], v[92:95]\n"
"s_waitcnt lgkmcnt(2)\n"
"v_mfma_f32_16x16x32_bf16 v[96:99], v[124:127], v[76:79], v[96:99]\n"
"s_waitcnt lgkmcnt(1)\n"
"v_mfma_f32_16x16x32_bf16 v[100:103], v[108:111], v[76:79], v[100:103]\n"
"s_waitcnt lgkmcnt(0)\n"
"v_mfma_f32_16x16x32_bf16 v[104:107], v[112:115], v[76:79], v[104:107]\n"
"v_and_b32 v11, 0x30, v1\n"
"v_add_u32 v11, 161664, v11\n"
"ds_read_b128 v[44:47], v11 offset:0\n"
"ds_read_b128 v[48:51], v11 offset:448\n"
"ds_read_b128 v[52:55], v11 offset:64\n"
"ds_read_b128 v[56:59], v11 offset:512\n"
"ds_read_b128 v[60:63], v11 offset:128\n"
"ds_read_b128 v[64:67], v11 offset:576\n"
"ds_read_b128 v[68:71], v11 offset:192\n"
"ds_read_b128 v[72:75], v11 offset:640\n"
"s_waitcnt vmcnt(0)\n"
"s_waitcnt lgkmcnt(0)\n"
"s_barrier\n"
"v_mov_b32 v116, 0xc0135761\n"
"s_add_u32 m0, s67, 114688\n"
"s_add_u32 s95, s66, 0\n"
"buffer_load_dwordx4 v2, s[32:35], s95 offen offset:3072 sc1 lds\n"
"buffer_load_dwordx4 v2, s[32:35], s95 offen offset:1024 sc1 lds\n"
"buffer_load_dwordx4 v2, s[32:35], s95 offen offset:2048 sc1 lds\n"
"s_bitcmp1_b32 s74, 1\n"
"s_cbranch_scc1 L216_%=\n"
"buffer_load_dwordx4 v2, s[32:35], s95 offen offset:0 sc1 lds\n"
"L216_%=:\n"
"v_pk_fma_f32 v[92:93], v[92:93], v[8:9], v[44:45] op_sel_hi:[1,0,1]\n"
"v_pk_fma_f32 v[92:93], v[8:9], v[48:49], v[92:93] op_sel:[1,0,0] op_sel_hi:[1,1,1] neg_lo:[1,0,0] neg_hi:[1,0,0]\n"
"v_pk_fma_f32 v[94:95], v[94:95], v[8:9], v[46:47] op_sel_hi:[1,0,1]\n"
"v_pk_fma_f32 v[94:95], v[8:9], v[50:51], v[94:95] op_sel:[1,0,0] op_sel_hi:[1,1,1] neg_lo:[1,0,0] neg_hi:[1,0,0]\n"
"v_pk_fma_f32 v[96:97], v[96:97], v[8:9], v[52:53] op_sel_hi:[1,0,1]\n"
"v_pk_fma_f32 v[96:97], v[8:9], v[56:57], v[96:97] op_sel:[1,0,0] op_sel_hi:[1,1,1] neg_lo:[1,0,0] neg_hi:[1,0,0]\n"
"v_pk_fma_f32 v[98:99], v[98:99], v[8:9], v[54:55] op_sel_hi:[1,0,1]\n"
"v_pk_fma_f32 v[98:99], v[8:9], v[58:59], v[98:99] op_sel:[1,0,0] op_sel_hi:[1,1,1] neg_lo:[1,0,0] neg_hi:[1,0,0]\n"
"v_pk_fma_f32 v[100:101], v[100:101], v[8:9], v[60:61] op_sel_hi:[1,0,1]\n"
"s_cmp_lg_u32 s74, 7\n"
"s_cbranch_scc1 L217_%=\n"
"v_mov_b32 v32, s72\n"
"v_mov_b32 v33, s73\n"
"s_lshl_b32 s89, s76, 5\n"
"s_add_u32 s89, s89, s75\n"
"s_lshl_b32 s89, s89, 7\n"
"s_add_u32 s89, s89, 131072\n"
"v_lshlrev_b32 v34, 3, v1\n"
"s_mov_b64 exec, 0xffff\n"
"buffer_store_dwordx2 v[32:33], v34, s[32:35], s89 offen sc1\n"
"s_mov_b64 exec, -1\n"
"s_nop 3\n"
"L217_%=:\n"
"v_pk_fma_f32 v[100:101], v[8:9], v[64:65], v[100:101] op_sel:[1,0,0] op_sel_hi:[1,1,1] neg_lo:[1,0,0] neg_hi:[1,0,0]\n"
"v_pk_fma_f32 v[102:103], v[102:103], v[8:9], v[62:63] op_sel_hi:[1,0,1]\n"
"v_pk_fma_f32 v[102:103], v[8:9], v[66:67], v[102:103] op_sel:[1,0,0] op_sel_hi:[1,1,1] neg_lo:[1,0,0] neg_hi:[1,0,0]\n"
"v_pk_fma_f32 v[104:105], v[104:105], v[8:9], v[68:69] op_sel_hi:[1,0,1]\n"
"v_pk_fma_f32 v[104:105], v[8:9], v[72:73], v[104:105] op_sel:[1,0,0] op_sel_hi:[1,1,1] neg_lo:[1,0,0] neg_hi:[1,0,0]\n"
"v_pk_fma_f32 v[106:107], v[106:107], v[8:9], v[70:71] op_sel_hi:[1,0,1]\n"
"v_pk_fma_f32 v[106:107], v[8:9], v[74:75], v[106:107] op_sel:[1,0,0] op_sel_hi:[1,1,1] neg_lo:[1,0,0] neg_hi:[1,0,0]\n"
"v_pk_mul_f32 v[108:109], v[92:93], v[92:93]\n"
"v_pk_mul_f32 v[110:111], v[94:95], v[94:95]\n"
"s_add_u32 m0, s67, 0\n"
"s_add_u32 s95, s66, 7168\n"
"buffer_load_dwordx4 v2, s[32:35], s95 offen offset:0 sc1 lds\n"
"v_pk_fma_f32 v[108:109], v[108:109], s[2:3], v[116:117] op_sel_hi:[1,0,0]\n"
"v_pk_fma_f32 v[110:111], v[110:111], s[2:3], v[116:117] op_sel_hi:[1,0,0]\n"
"v_pk_mul_f32 v[108:109], v[108:109], v[92:93]\n"
"v_pk_mul_f32 v[110:111], v[110:111], v[94:95]\n"
"v_exp_f32 v108, v108\n"
"v_exp_f32 v109, v109\n"
"v_exp_f32 v110, v110\n"
"v_exp_f32 v111, v111\n"
"v_pk_add_f32 v[108:109], v[108:109], 1.0 op_sel_hi:[1,0]\n"
"buffer_load_dwordx4 v2, s[32:35], s95 offen offset:1024 sc1 lds\n"
"v_pk_add_f32 v[110:111], v[110:111], 1.0 op_sel_hi:[1,0]\n"
"v_rcp_f32 v108, v108\n"
"v_rcp_f32 v109, v109\n"
"v_rcp_f32 v110, v110\n"
"v_rcp_f32 v111, v111\n"
"v_pk_mul_f32 v[92:93], v[92:93], v[108:109]\n"
"v_pk_mul_f32 v[94:95], v[94:95], v[110:111]\n"
"v_pk_mul_f32 v[112:113], v[96:97], v[96:97]\n"
"v_pk_mul_f32 v[114:115], v[98:99], v[98:99]\n"
"buffer_load_dwordx4 v2, s[32:35], s95 offen offset:2048 sc1 lds\n"
"v_pk_fma_f32 v[112:113], v[112:113], s[2:3], v[116:117] op_sel_hi:[1,0,0]\n"
"v_pk_fma_f32 v[114:115], v[114:115], s[2:3], v[116:117] op_sel_hi:[1,0,0]\n"
"v_pk_mul_f32 v[112:113], v[112:113], v[96:97]\n"
"v_pk_mul_f32 v[114:115], v[114:115], v[98:99]\n"
"v_exp_f32 v112, v112\n"
"v_exp_f32 v113, v113\n"
"v_exp_f32 v114, v114\n"
"v_exp_f32 v115, v115\n"
"v_pk_add_f32 v[112:113], v[112:113], 1.0 op_sel_hi:[1,0]\n"
"s_bitcmp0_b32 s74, 1\n"
"s_cbranch_scc1 L218_%=\n"
"buffer_load_dwordx4 v2, s[32:35], s95 offen offset:3072 sc1 lds\n"
"L218_%=:\n"
"v_pk_add_f32 v[114:115], v[114:115], 1.0 op_sel_hi:[1,0]\n"
"v_rcp_f32 v112, v112\n"
"v_rcp_f32 v113, v113\n"
"v_rcp_f32 v114, v114\n"
"v_rcp_f32 v115, v115\n"
"v_pk_mul_f32 v[96:97], v[96:97], v[112:113]\n"
"v_pk_mul_f32 v[98:99], v[98:99], v[114:115]\n"
"v_pk_mul_f32 v[108:109], v[100:101], v[100:101]\n"
"v_pk_mul_f32 v[110:111], v[102:103], v[102:103]\n"
"s_add_u32 m0, s67, 28672\n"
"s_add_u32 s95, s66, 14336\n"
"buffer_load_dwordx4 v2, s[32:35], s95 offen offset:3072 sc1 lds\n"
"v_pk_fma_f32 v[108:109], v[108:109], s[2:3], v[116:117] op_sel_hi:[1,0,0]\n"
"v_pk_fma_f32 v[110:111], v[110:111], s[2:3], v[116:117] op_sel_hi:[1,0,0]\n"
"v_pk_mul_f32 v[108:109], v[108:109], v[100:101]\n"
"v_pk_mul_f32 v[110:111], v[110:111], v[102:103]\n"
"v_exp_f32 v108, v108\n"
"v_exp_f32 v109, v109\n"
"v_exp_f32 v110, v110\n"
"v_exp_f32 v111, v111\n"
"v_pk_add_f32 v[108:109], v[108:109], 1.0 op_sel_hi:[1,0]\n"
"buffer_load_dwordx4 v2, s[32:35], s95 offen offset:1024 sc1 lds\n"
"v_pk_add_f32 v[110:111], v[110:111], 1.0 op_sel_hi:[1,0]\n"
"v_rcp_f32 v108, v108\n"
"v_rcp_f32 v109, v109\n"
"v_rcp_f32 v110, v110\n"
"v_rcp_f32 v111, v111\n"
"v_pk_mul_f32 v[100:101], v[100:101], v[108:109]\n"
"v_pk_mul_f32 v[102:103], v[102:103], v[110:111]\n"
"v_pk_mul_f32 v[112:113], v[104:105], v[104:105]\n"
"v_pk_mul_f32 v[114:115], v[106:107], v[106:107]\n"
"buffer_load_dwordx4 v2, s[32:35], s95 offen offset:2048 sc1 lds\n"
"v_pk_fma_f32 v[112:113], v[112:113], s[2:3], v[116:117] op_sel_hi:[1,0,0]\n"
"v_pk_fma_f32 v[114:115], v[114:115], s[2:3], v[116:117] op_sel_hi:[1,0,0]\n"
"v_pk_mul_f32 v[112:113], v[112:113], v[104:105]\n"
"v_pk_mul_f32 v[114:115], v[114:115], v[106:107]\n"
"v_exp_f32 v112, v112\n"
"v_exp_f32 v113, v113\n"
"v_exp_f32 v114, v114\n"
"v_exp_f32 v115, v115\n"
"v_pk_add_f32 v[112:113], v[112:113], 1.0 op_sel_hi:[1,0]\n"
"s_bitcmp1_b32 s74, 1\n"
"s_cbranch_scc1 L219_%=\n"
"buffer_load_dwordx4 v2, s[32:35], s95 offen offset:0 sc1 lds\n"
"L219_%=:\n"
"v_add_u32 v3, s68, v2\n"
"v_add_u32 v4, 0x10000, v3\n"
"v_add_u32 v5, 0x20000, v3\n"
"s_waitcnt vmcnt(7)\n"
"s_barrier\n"
"v_pk_add_f32 v[114:115], v[114:115], 1.0 op_sel_hi:[1,0]\n"
"v_rcp_f32 v112, v112\n"
"ds_read_b128 v[16:19], v4 offset:49152\n"
"v_rcp_f32 v113, v113\n"
"ds_read_b128 v[20:23], v4 offset:50176\n"
"v_rcp_f32 v114, v114\n"
"s_add_u32 m0, s67, 57344\n"
"s_add_u32 s95, s66, 21504\n"
"buffer_load_dwordx4 v2, s[32:35], s95 offen offset:0 sc1 lds\n"
"v_rcp_f32 v115, v115\n"
"ds_read_b128 v[24:27], v4 offset:51200\n"
"v_pk_mul_f32 v[104:105], v[104:105], v[112:113]\n"
"ds_read_b128 v[28:31], v4 offset:52224\n"
"v_pk_mul_f32 v[106:107], v[106:107], v[114:115]\n"
"buffer_load_dwordx4 v2, s[32:35], s95 offen offset:1024 sc1 lds\n"
"v_cvt_pk_bf16_f32 v120, v92, v93\n"
"ds_read_b128 v[32:35], v4 offset:53248\n"
"v_cvt_pk_bf16_f32 v121, v94, v95\n"
"ds_read_b128 v[36:39], v4 offset:54272\n"
"v_cvt_pk_bf16_f32 v122, v96, v97\n"
"ds_read_b128 v[40:43], v4 offset:55296\n"
"buffer_load_dwordx4 v2, s[32:35], s95 offen offset:2048 sc1 lds\n"
"v_cvt_pk_bf16_f32 v123, v98, v99\n"
"v_cvt_pk_bf16_f32 v124, v100, v101\n"
"v_cvt_pk_bf16_f32 v125, v102, v103\n"
"v_cvt_pk_bf16_f32 v126, v104, v105\n"
"v_cvt_pk_bf16_f32 v127, v106, v107\n"
"s_bitcmp0_b32 s74, 1\n"
"s_cbranch_scc1 L220_%=\n"
"buffer_load_dwordx4 v2, s[32:35], s95 offen offset:3072 sc1 lds\n"
"L220_%=:\n"
"s_waitcnt lgkmcnt(0)\n"
"s_nop 15\n"
"L119_%=:\n"
"v_lshlrev_b32 v7, 3, v1\n"
"v_and_b32 v11, 1, v1\n"
"v_lshlrev_b32 v11, 7, v11\n"
"v_mov_b32 v8, s72\n"
"v_mov_b32 v9, s73\n"
"v_max3_u32 v0, v17, v19, v21\n"
"v_max3_u32 v0, v23, v25, v0\n"
"v_max3_u32 v0, v27, v29, v0\n"
"v_max3_u32 v0, v31, v33, v0\n"
"v_max3_u32 v0, v35, v37, v0\n"
"v_max3_u32 v0, v39, v41, v0\n"
"v_max_u32 v0, v43, v0\n"
"v_cmp_eq_u32 vcc, -1, v0\n"
"s_nop 0\n"
"s_cbranch_vccnz RT221_%=\n"
"BK222_%=:\n"
"s_waitcnt vmcnt(7)\n"
"s_barrier\n"
"v_mfma_f32_16x16x32_bf16 v[64:67], v[16:19], v[128:131], 0\n"
"ds_read_b128 v[44:47], v3 offset:0\n"
"v_mfma_f32_16x16x32_bf16 v[68:71], v[16:19], v[192:195], 0\n"
"ds_read_b128 v[48:51], v3 offset:1024\n"
"v_mfma_f32_16x16x32_bf16 v[72:75], v[20:23], v[128:131], 0\n"
"ds_read_b128 v[52:55], v3 offset:2048\n"
"s_lshr_b32 s94, s75, 3\n"
"s_xor_b32 s94, s94, 1\n"
"s_lshl_b32 s94, s94, 10\n"
"s_add_u32 s94, s94, s69\n"
"s_mov_b64 exec, 3\n"
"buffer_load_dwordx2 v[8:9], v11, s[32:35], s94 offen sc1\n"
"s_mov_b64 exec, -1\n"
"v_mfma_f32_16x16x32_bf16 v[76:79], v[20:23], v[192:195], 0\n"
"ds_read_b128 v[56:59], v3 offset:3072\n"
"v_mfma_f32_16x16x32_bf16 v[80:83], v[24:27], v[128:131], 0\n"
"ds_read_b128 v[60:63], v3 offset:4096\n"
"v_mfma_f32_16x16x32_bf16 v[84:87], v[24:27], v[192:195], 0\n"
"s_lshr_b32 s66, s75, 3\n"
"s_xor_b32 s66, s66, 1\n"
"s_mul_i32 s66, s66, 114688\n"
"s_add_u32 s66, s66, s85\n"
"s_add_u32 m0, s67, 86016\n"
"s_add_u32 s95, s66, 0\n"
"buffer_load_dwordx4 v2, s[32:35], s95 offen offset:3072 sc1 lds\n"
"ds_read_b128 v[16:19], v3 offset:5120\n"
"v_mfma_f32_16x16x32_bf16 v[88:91], v[28:31], v[128:131], 0\n"
"ds_read_b128 v[20:23], v3 offset:6144\n"
"v_mfma_f32_16x16x32_bf16 v[92:95], v[28:31], v[192:195], 0\n"
"s_waitcnt lgkmcnt(5)\n"
"v_max3_u32 v0, v45, v47, v49\n"
"v_mfma_f32_16x16x32_bf16 v[96:99], v[32:35], v[128:131], 0\n"
"buffer_load_dwordx4 v2, s[32:35], s95 offen offset:1024 sc1 lds\n"
"s_waitcnt lgkmcnt(4)\n"
"v_max3_u32 v0, v51, v53, v0\n"
"v_mfma_f32_16x16x32_bf16 v[100:103], v[32:35], v[192:195], 0\n"
"s_waitcnt lgkmcnt(3)\n"
"v_max3_u32 v0, v55, v57, v0\n"
"v_mfma_f32_16x16x32_bf16 v[104:107], v[36:39], v[128:131], 0\n"
"s_waitcnt lgkmcnt(2)\n"
"v_max3_u32 v0, v59, v61, v0\n"
"buffer_load_dwordx4 v2, s[32:35], s95 offen offset:2048 sc1 lds\n"
"v_mfma_f32_16x16x32_bf16 v[108:111], v[36:39], v[192:195], 0\n"
"s_waitcnt lgkmcnt(1)\n"
"v_max3_u32 v0, v63, v17, v0\n"
"v_mfma_f32_16x16x32_bf16 v[112:115], v[40:43], v[128:131], 0\n"
"s_waitcnt lgkmcnt(0)\n"
"v_max3_u32 v0, v19, v21, v0\n"
"v_max_u32 v0, v23, v0\n"
"s_bitcmp1_b32 s74, 1\n"
"s_cbranch_scc1 L225_%=\n"
"buffer_load_dwordx4 v2, s[32:35], s95 offen offset:0 sc1 lds\n"
"L225_%=:\n"
"v_cmp_eq_u32 vcc, -1, v0\n"
"s_nop 0\n"
"s_cbranch_vccnz RT226_%=\n"
"BK227_%=:\n"
"s_waitcnt vmcnt(8)\n"
"v_mfma_f32_16x16x32_bf16 v[116:119], v[40:43], v[192:195], 0\n"
"s_barrier\n"
"v_mfma_f32_16x16x32_bf16 v[64:67], v[44:47], v[132:135], v[64:67]\n"
"ds_read_b128 v[24:27], v3 offset:28672\n"
"v_mfma_f32_16x16x32_bf16 v[68:71], v[44:47], v[196:199], v[68:71]\n"
"ds_read_b128 v[28:31], v3 offset:29696\n"
"v_mfma_f32_16x16x32_bf16 v[72:75], v[48:51], v[132:135], v[72:75]\n"
"ds_read_b128 v[32:35], v3 offset:30720\n"
"s_waitcnt vmcnt(3)\n"
"v_cmp_ne_u64 vcc, s[72:73], v[8:9]\n"
"s_nop 0\n"
"s_cbranch_vccnz RT230_%=\n"
"BK231_%=:\n"
"v_mfma_f32_16x16x32_bf16 v[76:79], v[48:51], v[196:199], v[76:79]\n"
"ds_read_b128 v[36:39], v3 offset:31744\n"
"v_mfma_f32_16x16x32_bf16 v[80:83], v[52:55], v[132:135], v[80:83]\n"
"ds_read_b128 v[40:43], v3 offset:32768\n"
"v_mfma_f32_16x16x32_bf16 v[84:87], v[52:55], v[196:199], v[84:87]\n"
"s_add_u32 m0, s67, 114688\n"
"s_add_u32 s95, s66, 7168\n"
"buffer_load_dwordx4 v2, s[32:35], s95 offen offset:0 sc1 lds\n"
"ds_read_b128 v[44:47], v3 offset:33792\n"
"v_mfma_f32_16x16x32_bf16 v[88:91], v[56:59], v[132:135], v[88:91]\n"
"ds_read_b128 v[48:51], v3 offset:34816\n"
"v_mfma_f32_16x16x32_bf16 v[92:95], v[56:59], v[196:199], v[92:95]\n"
"s_waitcnt lgkmcnt(5)\n"
"v_max3_u32 v0, v25, v27, v29\n"
"v_mfma_f32_16x16x32_bf16 v[96:99], v[60:63], v[132:135], v[96:99]\n"
"buffer_load_dwordx4 v2, s[32:35], s95 offen offset:1024 sc1 lds\n"
"s_waitcnt lgkmcnt(4)\n"
"v_max3_u32 v0, v31, v33, v0\n"
"v_mfma_f32_16x16x32_bf16 v[100:103], v[60:63], v[196:199], v[100:103]\n"
"s_waitcnt lgkmcnt(3)\n"
"v_max3_u32 v0, v35, v37, v0\n"
"v_mfma_f32_16x16x32_bf16 v[104:107], v[16:19], v[132:135], v[104:107]\n"
"s_waitcnt lgkmcnt(2)\n"
"v_max3_u32 v0, v39, v41, v0\n"
"buffer_load_dwordx4 v2, s[32:35], s95 offen offset:2048 sc1 lds\n"
"v_mfma_f32_16x16x32_bf16 v[108:111], v[16:19], v[196:199], v[108:111]\n"
"s_waitcnt lgkmcnt(1)\n"
"v_max3_u32 v0, v43, v45, v0\n"
"v_mfma_f32_16x16x32_bf16 v[112:115], v[20:23], v[132:135], v[112:115]\n"
"s_waitcnt lgkmcnt(0)\n"
"v_max3_u32 v0, v47, v49, v0\n"
"v_max_u32 v0, v51, v0\n"
"s_bitcmp0_b32 s74, 1\n"
"s_cbranch_scc1 L234_%=\n"
"buffer_load_dwordx4 v2, s[32:35], s95 offen offset:3072 sc1 lds\n"
"L234_%=:\n"
"v_cmp_eq_u32 vcc, -1, v0\n"
"s_nop 0\n"
"s_cbranch_vccnz RT235_%=\n"
"BK236_%=:\n"
"s_waitcnt vmcnt(7)\n"
"v_mfma_f32_16x16x32_bf16 v[116:119], v[20:23], v[196:199], v[116:119]\n"
"s_barrier\n"
"v_mfma_f32_16x16x32_bf16 v[64:67], v[24:27], v[136:139], v[64:67]\n"
"ds_read_b128 v[52:55], v3 offset:57344\n"
"v_mfma_f32_16x16x32_bf16 v[68:71], v[24:27], v[200:203], v[68:71]\n"
"ds_read_b128 v[56:59], v3 offset:58368\n"
"v_mfma_f32_16x16x32_bf16 v[72:75], v[28:31], v[136:139], v[72:75]\n"
"ds_read_b128 v[60:63], v3 offset:59392\n"
"s_add_u32 m0, s67, 0\n"
"s_add_u32 s95, s66, 14336\n"
"buffer_load_dwordx4 v2, s[32:35], s95 offen offset:3072 sc1 lds\n"
"v_mfma_f32_16x16x32_bf16 v[76:79], v[28:31], v[200:203], v[76:79]\n"
"ds_read_b128 v[16:19], v3 offset:60416\n"
"v_mfma_f32_16x16x32_bf16 v[80:83], v[32:35], v[136:139], v[80:83]\n"
"ds_read_b128 v[20:23], v3 offset:61440\n"
"v_mfma_f32_16x16x32_bf16 v[84:87], v[32:35], v[200:203], v[84:87]\n"
"buffer_load_dwordx4 v2, s[32:35], s95 offen offset:1024 sc1 lds\n"
"ds_read_b128 v[24:27], v3 offset:62464\n"
"v_mfma_f32_16x16x32_bf16 v[88:91], v[36:39], v[136:139], v[88:91]\n"
"ds_read_b128 v[28:31], v3 offset:63488\n"
"v_mfma_f32_16x16x32_bf16 v[92:95], v[36:39], v[200:203], v[92:95]\n"
"s_waitcnt lgkmcnt(5)\n"
"v_max3_u32 v0, v53, v55, v57\n"
"v_mfma_f32_16x16x32_bf16 v[96:99], v[40:43], v[136:139], v[96:99]\n"
"buffer_load_dwordx4 v2, s[32:35], s95 offen offset:2048 sc1 lds\n"
"s_waitcnt lgkmcnt(4)\n"
"v_max3_u32 v0, v59, v61, v0\n"
"v_mfma_f32_16x16x32_bf16 v[100:103], v[40:43], v[200:203], v[100:103]\n"
"s_waitcnt lgkmcnt(3)\n"
"v_max3_u32 v0, v63, v17, v0\n"
"v_mfma_f32_16x16x32_bf16 v[104:107], v[44:47], v[136:139], v[104:107]\n"
"s_waitcnt lgkmcnt(2)\n"
"v_max3_u32 v0, v19, v21, v0\n"
"s_bitcmp1_b32 s74, 1\n"
"s_cbranch_scc1 L239_%=\n"
"buffer_load_dwordx4 v2, s[32:35], s95 offen offset:0 sc1 lds\n"
"L239_%=:\n"
"v_mfma_f32_16x16x32_bf16 v[108:111], v[44:47], v[200:203], v[108:111]\n"
"s_waitcnt lgkmcnt(1)\n"
"v_max3_u32 v0, v23, v25, v0\n"
"v_mfma_f32_16x16x32_bf16 v[112:115], v[48:51], v[136:139], v[112:115]\n"
"s_waitcnt lgkmcnt(0)\n"
"v_max3_u32 v0, v27, v29, v0\n"
"v_max_u32 v0, v31, v0\n"
"s_lshr_b32 s94, s75, 3\n"
"s_xor_b32 s94, s94, 2\n"
"s_lshl_b32 s94, s94, 10\n"
"s_add_u32 s94, s94, s69\n"
"s_mov_b64 exec, 3\n"
"buffer_load_dwordx2 v[8:9], v11, s[32:35], s94 offen sc1\n"
"s_mov_b64 exec, -1\n"
"v_cmp_eq_u32 vcc, -1, v0\n"
"s_nop 0\n"
"s_cbranch_vccnz RT240_%=\n"
"BK241_%=:\n"
"s_waitcnt vmcnt(8)\n"
"v_mfma_f32_16x16x32_bf16 v[116:119], v[48:51], v[200:203], v[116:119]\n"
"s_barrier\n"
"v_mfma_f32_16x16x32_bf16 v[64:67], v[52:55], v[140:143], v[64:67]\n"
"ds_read_b128 v[32:35], v4 offset:20480\n"
"v_mfma_f32_16x16x32_bf16 v[68:71], v[52:55], v[204:207], v[68:71]\n"
"ds_read_b128 v[36:39], v4 offset:21504\n"
"v_mfma_f32_16x16x32_bf16 v[72:75], v[56:59], v[140:143], v[72:75]\n"
"ds_read_b128 v[40:43], v4 offset:22528\n"
"v_mfma_f32_16x16x32_bf16 v[76:79], v[56:59], v[204:207], v[76:79]\n"
"s_add_u32 m0, s67, 28672\n"
"s_add_u32 s95, s66, 21504\n"
"buffer_load_dwordx4 v2, s[32:35], s95 offen offset:0 sc1 lds\n"
"ds_read_b128 v[44:47], v4 offset:23552\n"
"v_mfma_f32_16x16x32_bf16 v[80:83], v[60:63], v[140:143], v[80:83]\n"
"ds_read_b128 v[48:51], v4 offset:24576\n"
"v_mfma_f32_16x16x32_bf16 v[84:87], v[60:63], v[204:207], v[84:87]\n"
"ds_read_b128 v[52:55], v4 offset:25600\n"
"v_mfma_f32_16x16x32_bf16 v[88:91], v[16:19], v[140:143], v[88:91]\n"
"ds_read_b128 v[56:59], v4 offset:26624\n"
"buffer_load_dwordx4 v2, s[32:35], s95 offen offset:1024 sc1 lds\n"
"v_mfma_f32_16x16x32_bf16 v[92:95], v[16:19], v[204:207], v[92:95]\n"
"s_waitcnt lgkmcnt(5)\n"
"v_max3_u32 v0, v33, v35, v37\n"
"v_mfma_f32_16x16x32_bf16 v[96:99], v[20:23], v[140:143], v[96:99]\n"
"s_waitcnt lgkmcnt(4)\n"
"v_max3_u32 v0, v39, v41, v0\n"
"v_mfma_f32_16x16x32_bf16 v[100:103], v[20:23], v[204:207], v[100:103]\n"
"s_waitcnt lgkmcnt(3)\n"
"v_max3_u32 v0, v43, v45, v0\n"
"v_mfma_f32_16x16x32_bf16 v[104:107], v[24:27], v[140:143], v[104:107]\n"
"buffer_load_dwordx4 v2, s[32:35], s95 offen offset:2048 sc1 lds\n"
"s_waitcnt lgkmcnt(2)\n"
"v_max3_u32 v0, v47, v49, v0\n"
"v_mfma_f32_16x16x32_bf16 v[108:111], v[24:27], v[204:207], v[108:111]\n"
"s_waitcnt lgkmcnt(1)\n"
"v_max3_u32 v0, v51, v53, v0\n"
"v_mfma_f32_16x16x32_bf16 v[112:115], v[28:31], v[140:143], v[112:115]\n"
"s_waitcnt lgkmcnt(0)\n"
"v_max3_u32 v0, v55, v57, v0\n"
"v_max_u32 v0, v59, v0\n"
"s_bitcmp0_b32 s74, 1\n"
"s_cbranch_scc1 L244_%=\n"
"buffer_load_dwordx4 v2, s[32:35], s95 offen offset:3072 sc1 lds\n"
"L244_%=:\n"
"v_cmp_eq_u32 vcc, -1, v0\n"
"s_nop 0\n"
"s_cbranch_vccnz RT245_%=\n"
"BK246_%=:\n"
"s_waitcnt vmcnt(8)\n"
"v_mfma_f32_16x16x32_bf16 v[116:119], v[28:31], v[204:207], v[116:119]\n"
"s_barrier\n"
"v_mfma_f32_16x16x32_bf16 v[64:67], v[32:35], v[144:147], v[64:67]\n"
"ds_read_b128 v[60:63], v4 offset:49152\n"
"v_mfma_f32_16x16x32_bf16 v[68:71], v[32:35], v[208:211], v[68:71]\n"
"ds_read_b128 v[16:19], v4 offset:50176\n"
"s_waitcnt vmcnt(3)\n"
"v_cmp_ne_u64 vcc, s[72:73], v[8:9]\n"
"s_nop 0\n"
"s_cbranch_vccnz RT249_%=\n"
"BK250_%=:\n"
"v_mfma_f32_16x16x32_bf16 v[72:75], v[36:39], v[144:147], v[72:75]\n"
"ds_read_b128 v[20:23], v4 offset:51200\n"
"v_mfma_f32_16x16x32_bf16 v[76:79], v[36:39], v[208:211], v[76:79]\n"
"ds_read_b128 v[24:27], v4 offset:52224\n"
"s_lshr_b32 s66, s75, 3\n"
"s_xor_b32 s66, s66, 2\n"
"s_mul_i32 s66, s66, 114688\n"
"s_add_u32 s66, s66, s85\n"
"s_add_u32 m0, s67, 57344\n"
"s_add_u32 s95, s66, 0\n"
"buffer_load_dwordx4 v2, s[32:35], s95 offen offset:3072 sc1 lds\n"
"v_mfma_f32_16x16x32_bf16 v[80:83], v[40:43], v[144:147], v[80:83]\n"
"ds_read_b128 v[28:31], v4 offset:53248\n"
"v_mfma_f32_16x16x32_bf16 v[84:87], v[40:43], v[208:211], v[84:87]\n"
"ds_read_b128 v[32:35], v4 offset:54272\n"
"buffer_load_dwordx4 v2, s[32:35], s95 offen offset:1024 sc1 lds\n"
"v_mfma_f32_16x16x32_bf16 v[88:91], v[44:47], v[144:147], v[88:91]\n"
"ds_read_b128 v[36:39], v4 offset:55296\n"
"v_mfma_f32_16x16x32_bf16 v[92:95], v[44:47], v[208:211], v[92:95]\n"
"v_mfma_f32_16x16x32_bf16 v[96:99], v[48:51], v[144:147], v[96:99]\n"
"buffer_load_dwordx4 v2, s[32:35], s95 offen offset:2048 sc1 lds\n"
"v_mfma_f32_16x16x32_bf16 v[100:103], v[48:51], v[208:211], v[100:103]\n"
"v_mfma_f32_16x16x32_bf16 v[104:107], v[52:55], v[144:147], v[104:107]\n"
"v_mfma_f32_16x16x32_bf16 v[108:111], v[52:55], v[208:211], v[108:111]\n"
"v_mfma_f32_16x16x32_bf16 v[112:115], v[56:59], v[144:147], v[112:115]\n"
"s_bitcmp1_b32 s74, 1\n"
"s_cbranch_scc1 L253_%=\n"
"buffer_load_dwordx4 v2, s[32:35], s95 offen offset:0 sc1 lds\n"
"L253_%=:\n"
"s_waitcnt vmcnt(7)\n"
"s_waitcnt lgkmcnt(0)\n"
"v_mfma_f32_16x16x32_bf16 v[116:119], v[56:59], v[208:211], v[116:119]\n"
"s_barrier\n"
"v_mfma_f32_16x16x32_bf16 v[64:67], v[60:63], v[148:151], v[64:67]\n"
"ds_read_b128 v[40:43], v3 offset:0\n"
"v_mfma_f32_16x16x32_bf16 v[68:71], v[60:63], v[212:215], v[68:71]\n"
"ds_read_b128 v[44:47], v3 offset:1024\n"
"v_mfma_f32_16x16x32_bf16 v[72:75], v[16:19], v[148:151], v[72:75]\n"
"s_add_u32 m0, s67, 86016\n"
"s_add_u32 s95, s66, 7168\n"
"buffer_load_dwordx4 v2, s[32:35], s95 offen offset:0 sc1 lds\n"
"ds_read_b128 v[48:51], v3 offset:2048\n"
"v_mfma_f32_16x16x32_bf16 v[76:79], v[16:19], v[212:215], v[76:79]\n"
"ds_read_b128 v[52:55], v3 offset:3072\n"
"v_mfma_f32_16x16x32_bf16 v[80:83], v[20:23], v[148:151], v[80:83]\n"
"ds_read_b128 v[56:59], v3 offset:4096\n"
"buffer_load_dwordx4 v2, s[32:35], s95 offen offset:1024 sc1 lds\n"
"v_mfma_f32_16x16x32_bf16 v[84:87], v[20:23], v[212:215], v[84:87]\n"
"ds_read_b128 v[60:63], v3 offset:5120\n"
"v_mfma_f32_16x16x32_bf16 v[88:91], v[24:27], v[148:151], v[88:91]\n"
"ds_read_b128 v[16:19], v3 offset:6144\n"
"v_mfma_f32_16x16x32_bf16 v[92:95], v[24:27], v[212:215], v[92:95]\n"
"buffer_load_dwordx4 v2, s[32:35], s95 offen offset:2048 sc1 lds\n"
"v_mfma_f32_16x16x32_bf16 v[96:99], v[28:31], v[148:151], v[96:99]\n"
"v_mfma_f32_16x16x32_bf16 v[100:103], v[28:31], v[212:215], v[100:103]\n"
"v_mfma_f32_16x16x32_bf16 v[104:107], v[32:35], v[148:151], v[104:107]\n"
"v_mfma_f32_16x16x32_bf16 v[108:111], v[32:35], v[212:215], v[108:111]\n"
"v_mfma_f32_16x16x32_bf16 v[112:115], v[36:39], v[148:151], v[112:115]\n"
"s_bitcmp0_b32 s74, 1\n"
"s_cbranch_scc1 L254_%=\n"
"buffer_load_dwordx4 v2, s[32:35], s95 offen offset:3072 sc1 lds\n"
"L254_%=:\n"
"s_waitcnt vmcnt(7)\n"
"s_waitcnt lgkmcnt(0)\n"
"v_mfma_f32_16x16x32_bf16 v[116:119], v[36:39], v[212:215], v[116:119]\n"
"s_barrier\n"
"v_mfma_f32_16x16x32_bf16 v[64:67], v[40:43], v[152:155], v[64:67]\n"
"ds_read_b128 v[20:23], v3 offset:28672\n"
"v_mfma_f32_16x16x32_bf16 v[68:71], v[40:43], v[216:219], v[68:71]\n"
"ds_read_b128 v[24:27], v3 offset:29696\n"
"s_add_u32 m0, s67, 114688\n"
"s_add_u32 s95, s66, 14336\n"
"buffer_load_dwordx4 v2, s[32:35], s95 offen offset:3072 sc1 lds\n"
"v_mfma_f32_16x16x32_bf16 v[72:75], v[44:47], v[152:155], v[72:75]\n"
"ds_read_b128 v[28:31], v3 offset:30720\n"
"v_mfma_f32_16x16x32_bf16 v[76:79], v[44:47], v[216:219], v[76:79]\n"
"ds_read_b128 v[32:35], v3 offset:31744\n"
"buffer_load_dwordx4 v2, s[32:35], s95 offen offset:1024 sc1 lds\n"
"v_mfma_f32_16x16x32_bf16 v[80:83], v[48:51], v[152:155], v[80:83]\n"
"ds_read_b128 v[36:39], v3 offset:32768\n"
"v_mfma_f32_16x16x32_bf16 v[84:87], v[48:51], v[216:219], v[84:87]\n"
"ds_read_b128 v[40:43], v3 offset:33792\n"
"buffer_load_dwordx4 v2, s[32:35], s95 offen offset:2048 sc1 lds\n"
"v_mfma_f32_16x16x32_bf16 v[88:91], v[52:55], v[152:155], v[88:91]\n"
"ds_read_b128 v[44:47], v3 offset:34816\n"
"v_mfma_f32_16x16x32_bf16 v[92:95], v[52:55], v[216:219], v[92:95]\n"
"v_mfma_f32_16x16x32_bf16 v[96:99], v[56:59], v[152:155], v[96:99]\n"
"s_bitcmp1_b32 s74, 1\n"
"s_cbranch_scc1 L255_%=\n"
"buffer_load_dwordx4 v2, s[32:35], s95 offen offset:0 sc1 lds\n"
"L255_%=:\n"
"v_mfma_f32_16x16x32_bf16 v[100:103], v[56:59], v[216:219], v[100:103]\n"
"v_mfma_f32_16x16x32_bf16 v[104:107], v[60:63], v[152:155], v[104:107]\n"
"v_mfma_f32_16x16x32_bf16 v[108:111], v[60:63], v[216:219], v[108:111]\n"
"v_mfma_f32_16x16x32_bf16 v[112:115], v[16:19], v[152:155], v[112:115]\n"
"s_lshr_b32 s94, s75, 3\n"
"s_xor_b32 s94, s94, 3\n"
"s_lshl_b32 s94, s94, 10\n"
"s_add_u32 s94, s94, s69\n"
"s_mov_b64 exec, 3\n"
"buffer_load_dwordx2 v[8:9], v11, s[32:35], s94 offen sc1\n"
"s_mov_b64 exec, -1\n"
"s_waitcnt vmcnt(8)\n"
"s_waitcnt lgkmcnt(0)\n"
"v_mfma_f32_16x16x32_bf16 v[116:119], v[16:19], v[216:219], v[116:119]\n"
"s_barrier\n"
"v_mfma_f32_16x16x32_bf16 v[64:67], v[20:23], v[156:159], v[64:67]\n"
"ds_read_b128 v[48:51], v3 offset:57344\n"
"v_mfma_f32_16x16x32_bf16 v[68:71], v[20:23], v[220:223], v[68:71]\n"
"ds_read_b128 v[52:55], v3 offset:58368\n"
"s_add_u32 m0, s67, 0\n"
"s_add_u32 s95, s66, 21504\n"
"buffer_load_dwordx4 v2, s[32:35], s95 offen offset:0 sc1 lds\n"
"v_mfma_f32_16x16x32_bf16 v[72:75], v[24:27], v[156:159], v[72:75]\n"
"ds_read_b128 v[56:59], v3 offset:59392\n"
"v_mfma_f32_16x16x32_bf16 v[76:79], v[24:27], v[220:223], v[76:79]\n"
"ds_read_b128 v[60:63], v3 offset:60416\n"
"buffer_load_dwordx4 v2, s[32:35], s95 offen offset:1024 sc1 lds\n"
"v_mfma_f32_16x16x32_bf16 v[80:83], v[28:31], v[156:159], v[80:83]\n"
"ds_read_b128 v[16:19], v3 offset:61440\n"
"v_mfma_f32_16x16x32_bf16 v[84:87], v[28:31], v[220:223], v[84:87]\n"
"ds_read_b128 v[20:23], v3 offset:62464\n"
"buffer_load_dwordx4 v2, s[32:35], s95 offen offset:2048 sc1 lds\n"
"v_mfma_f32_16x16x32_bf16 v[88:91], v[32:35], v[156:159], v[88:91]\n"
"ds_read_b128 v[24:27], v3 offset:63488\n"
"v_mfma_f32_16x16x32_bf16 v[92:95], v[32:35], v[220:223], v[92:95]\n"
"v_mfma_f32_16x16x32_bf16 v[96:99], v[36:39], v[156:159], v[96:99]\n"
"s_bitcmp0_b32 s74, 1\n"
"s_cbranch_scc1 L256_%=\n"
"buffer_load_dwordx4 v2, s[32:35], s95 offen offset:3072 sc1 lds\n"
"L256_%=:\n"
"v_mfma_f32_16x16x32_bf16 v[100:103], v[36:39], v[220:223], v[100:103]\n"
"v_mfma_f32_16x16x32_bf16 v[104:107], v[40:43], v[156:159], v[104:107]\n"
"v_mfma_f32_16x16x32_bf16 v[108:111], v[40:43], v[220:223], v[108:111]\n"
"v_mfma_f32_16x16x32_bf16 v[112:115], v[44:47], v[156:159], v[112:115]\n"
"v_and_b32 v6, 15, v1\n"
"v_lshlrev_b32 v6, 1, v6\n"
"s_lshr_b32 s89, s82, 9\n"
"buffer_load_ushort v6, v6, s[28:31], s89 offen\n"
"s_waitcnt vmcnt(9)\n"
"s_waitcnt lgkmcnt(0)\n"
"v_mfma_f32_16x16x32_bf16 v[116:119], v[44:47], v[220:223], v[116:119]\n"
"s_barrier\n"
"v_mfma_f32_16x16x32_bf16 v[64:67], v[48:51], v[160:163], v[64:67]\n"
"ds_read_b128 v[28:31], v4 offset:20480\n"
"v_mfma_f32_16x16x32_bf16 v[68:71], v[48:51], v[224:227], v[68:71]\n"
"ds_read_b128 v[32:35], v4 offset:21504\n"
"s_waitcnt vmcnt(4)\n"
"v_cmp_ne_u64 vcc, s[72:73], v[8:9]\n"
"s_nop 0\n"
"s_cbranch_vccnz RT257_%=\n"
"BK258_%=:\n"
"v_mfma_f32_16x16x32_bf16 v[72:75], v[52:55], v[160:163], v[72:75]\n"
"ds_read_b128 v[36:39], v4 offset:22528\n"
"v_mfma_f32_16x16x32_bf16 v[76:79], v[52:55], v[224:227], v[76:79]\n"
"ds_read_b128 v[40:43], v4 offset:23552\n"
"s_lshr_b32 s66, s75, 3\n"
"s_xor_b32 s66, s66, 3\n"
"s_mul_i32 s66, s66, 114688\n"
"s_add_u32 s66, s66, s85\n"
"s_add_u32 m0, s67, 28672\n"
"s_add_u32 s95, s66, 0\n"
"buffer_load_dwordx4 v2, s[32:35], s95 offen offset:3072 sc1 lds\n"
"v_mfma_f32_16x16x32_bf16 v[80:83], v[56:59], v[160:163], v[80:83]\n"
"ds_read_b128 v[44:47], v4 offset:24576\n"
"v_mfma_f32_16x16x32_bf16 v[84:87], v[56:59], v[224:227], v[84:87]\n"
"ds_read_b128 v[48:51], v4 offset:25600\n"
"buffer_load_dwordx4 v2, s[32:35], s95 offen offset:1024 sc1 lds\n"
"v_mfma_f32_16x16x32_bf16 v[88:91], v[60:63], v[160:163], v[88:91]\n"
"ds_read_b128 v[52:55], v4 offset:26624\n"
"v_mfma_f32_16x16x32_bf16 v[92:95], v[60:63], v[224:227], v[92:95]\n"
"v_mfma_f32_16x16x32_bf16 v[96:99], v[16:19], v[160:163], v[96:99]\n"
"buffer_load_dwordx4 v2, s[32:35], s95 offen offset:2048 sc1 lds\n"
"v_mfma_f32_16x16x32_bf16 v[100:103], v[16:19], v[224:227], v[100:103]\n"
"v_mfma_f32_16x16x32_bf16 v[104:107], v[20:23], v[160:163], v[104:107]\n"
"v_mfma_f32_16x16x32_bf16 v[108:111], v[20:23], v[224:227], v[108:111]\n"
"v_mfma_f32_16x16x32_bf16 v[112:115], v[24:27], v[160:163], v[112:115]\n"
"s_bitcmp1_b32 s74, 1\n"
"s_cbranch_scc1 L261_%=\n"
"buffer_load_dwordx4 v2, s[32:35], s95 offen offset:0 sc1 lds\n"
"L261_%=:\n"
"s_waitcnt vmcnt(8)\n"
"s_waitcnt lgkmcnt(0)\n"
"v_mfma_f32_16x16x32_bf16 v[116:119], v[24:27], v[224:227], v[116:119]\n"
"s_barrier\n"
"v_mfma_f32_16x16x32_bf16 v[64:67], v[28:31], v[164:167], v[64:67]\n"
"ds_read_b128 v[56:59], v4 offset:49152\n"
"v_mfma_f32_16x16x32_bf16 v[68:71], v[28:31], v[228:231], v[68:71]\n"
"ds_read_b128 v[60:63], v4 offset:50176\n"
"v_mfma_f32_16x16x32_bf16 v[72:75], v[32:35], v[164:167], v[72:75]\n"
"s_add_u32 m0, s67, 57344\n"
"s_add_u32 s95, s66, 7168\n"
"buffer_load_dwordx4 v2, s[32:35], s95 offen offset:0 sc1 lds\n"
"ds_read_b128 v[16:19], v4 offset:51200\n"
"v_mfma_f32_16x16x32_bf16 v[76:79], v[32:35], v[228:231], v[76:79]\n"
"ds_read_b128 v[20:23], v4 offset:52224\n"
"v_mfma_f32_16x16x32_bf16 v[80:83], v[36:39], v[164:167], v[80:83]\n"
"ds_read_b128 v[24:27], v4 offset:53248\n"
"buffer_load_dwordx4 v2, s[32:35], s95 offen offset:1024 sc1 lds\n"
"v_mfma_f32_16x16x32_bf16 v[84:87], v[36:39], v[228:231], v[84:87]\n"
"ds_read_b128 v[28:31], v4 offset:54272\n"
"v_mfma_f32_16x16x32_bf16 v[88:91], v[40:43], v[164:167], v[88:91]\n"
"ds_read_b128 v[32:35], v4 offset:55296\n"
"v_mfma_f32_16x16x32_bf16 v[92:95], v[40:43], v[228:231], v[92:95]\n"
"buffer_load_dwordx4 v2, s[32:35], s95 offen offset:2048 sc1 lds\n"
"v_mfma_f32_16x16x32_bf16 v[96:99], v[44:47], v[164:167], v[96:99]\n"
"v_mfma_f32_16x16x32_bf16 v[100:103], v[44:47], v[228:231], v[100:103]\n"
"v_mfma_f32_16x16x32_bf16 v[104:107], v[48:51], v[164:167], v[104:107]\n"
"v_mfma_f32_16x16x32_bf16 v[108:111], v[48:51], v[228:231], v[108:111]\n"
"v_mfma_f32_16x16x32_bf16 v[112:115], v[52:55], v[164:167], v[112:115]\n"
"s_bitcmp0_b32 s74, 1\n"
"s_cbranch_scc1 L262_%=\n"
"buffer_load_dwordx4 v2, s[32:35], s95 offen offset:3072 sc1 lds\n"
"L262_%=:\n"
"s_waitcnt vmcnt(8)\n"
"s_waitcnt lgkmcnt(0)\n"
"v_mfma_f32_16x16x32_bf16 v[116:119], v[52:55], v[228:231], v[116:119]\n"
"s_barrier\n"
"v_mfma_f32_16x16x32_bf16 v[64:67], v[56:59], v[168:171], v[64:67]\n"
"s_add_u32 m0, s67, 86016\n"
"s_add_u32 s95, s66, 14336\n"
"buffer_load_dwordx4 v2, s[32:35], s95 offen offset:3072 sc1 lds\n"
"ds_read_b128 v[36:39], v3 offset:0\n"
"buffer_load_dwordx4 v2, s[32:35], s95 offen offset:1024 sc1 lds\n"
"v_mfma_f32_16x16x32_bf16 v[68:71], v[56:59], v[232:235], v[68:71]\n"
"buffer_load_dwordx4 v2, s[32:35], s95 offen offset:2048 sc1 lds\n"
"ds_read_b128 v[40:43], v3 offset:1024\n"
"s_bitcmp1_b32 s74, 1\n"
"s_cbranch_scc1 L263_%=\n"
"buffer_load_dwordx4 v2, s[32:35], s95 offen offset:0 sc1 lds\n"
"L263_%=:\n"
"v_mfma_f32_16x16x32_bf16 v[72:75], v[60:63], v[168:171], v[72:75]\n"
"v_lshlrev_b32 v128, 16, v120\n"
"ds_read_b128 v[44:47], v3 offset:2048\n"
"v_and_b32 v129, 0xffff0000, v120\n"
"v_mfma_f32_16x16x32_bf16 v[76:79], v[60:63], v[232:235], v[76:79]\n"
"v_lshlrev_b32 v130, 16, v121\n"
"ds_read_b128 v[48:51], v3 offset:3072\n"
"v_and_b32 v131, 0xffff0000, v121\n"
"v_mfma_f32_16x16x32_bf16 v[80:83], v[16:19], v[168:171], v[80:83]\n"
"v_lshlrev_b32 v132, 16, v122\n"
"ds_read_b128 v[52:55], v3 offset:4096\n"
"v_and_b32 v133, 0xffff0000, v122\n"
"v_mfma_f32_16x16x32_bf16 v[84:87], v[16:19], v[232:235], v[84:87]\n"
"v_lshlrev_b32 v134, 16, v123\n"
"ds_read_b128 v[56:59], v3 offset:5120\n"
"v_and_b32 v135, 0xffff0000, v123\n"
"v_mfma_f32_16x16x32_bf16 v[88:91], v[20:23], v[168:171], v[88:91]\n"
"v_lshlrev_b32 v136, 16, v124\n"
"ds_read_b128 v[60:63], v3 offset:6144\n"
"v_and_b32 v137, 0xffff0000, v124\n"
"v_mfma_f32_16x16x32_bf16 v[92:95], v[20:23], v[232:235], v[92:95]\n"
"v_lshlrev_b32 v138, 16, v125\n"
"v_mfma_f32_16x16x32_bf16 v[96:99], v[24:27], v[168:171], v[96:99]\n"
"v_and_b32 v139, 0xffff0000, v125\n"
"v_mfma_f32_16x16x32_bf16 v[100:103], v[24:27], v[232:235], v[100:103]\n"
"v_lshlrev_b32 v140, 16, v126\n"
"v_mfma_f32_16x16x32_bf16 v[104:107], v[28:31], v[168:171], v[104:107]\n"
"v_and_b32 v141, 0xffff0000, v126\n"
"v_mfma_f32_16x16x32_bf16 v[108:111], v[28:31], v[232:235], v[108:111]\n"
"v_lshlrev_b32 v142, 16, v127\n"
"v_mfma_f32_16x16x32_bf16 v[112:115], v[32:35], v[168:171], v[112:115]\n"
"v_and_b32 v143, 0xffff0000, v127\n"
"s_waitcnt vmcnt(9)\n"
"v_lshlrev_b32 v6, 16, v6\n"
"s_waitcnt vmcnt(7)\n"
"s_waitcnt lgkmcnt(0)\n"
"v_mfma_f32_16x16x32_bf16 v[116:119], v[32:35], v[232:235], v[116:119]\n"
"s_barrier\n"
"v_mfma_f32_16x16x32_bf16 v[64:67], v[36:39], v[172:175], v[64:67]\n"
"ds_read_b128 v[16:19], v3 offset:28672\n"
"s_add_u32 m0, s67, 114688\n"
"s_add_u32 s95, s66, 21504\n"
"buffer_load_dwordx4 v2, s[32:35], s95 offen offset:0 sc1 lds\n"
"v_mfma_f32_16x16x32_bf16 v[68:71], v[36:39], v[236:239], v[68:71]\n"
"buffer_load_dwordx4 v2, s[32:35], s95 offen offset:1024 sc1 lds\n"
"ds_read_b128 v[20:23], v3 offset:29696\n"
"v_mfma_f32_16x16x32_bf16 v[72:75], v[40:43], v[172:175], v[72:75]\n"
"buffer_load_dwordx4 v2, s[32:35], s95 offen offset:2048 sc1 lds\n"
"ds_read_b128 v[24:27], v3 offset:30720\n"
"s_bitcmp0_b32 s74, 1\n"
"s_cbranch_scc1 L264_%=\n"
"buffer_load_dwordx4 v2, s[32:35], s95 offen offset:3072 sc1 lds\n"
"L264_%=:\n"
"v_mfma_f32_16x16x32_bf16 v[76:79], v[40:43], v[236:239], v[76:79]\n"
"ds_read_b128 v[28:31], v3 offset:31744\n"
"v_pk_mul_f32 v[144:145], v[128:129], v[6:7] op_sel_hi:[1,0]\n"
"v_mfma_f32_16x16x32_bf16 v[80:83], v[44:47], v[172:175], v[80:83]\n"
"v_pk_mul_f32 v[146:147], v[130:131], v[6:7] op_sel_hi:[1,0]\n"
"ds_read_b128 v[32:35], v3 offset:32768\n"
"v_pk_mul_f32 v[148:149], v[132:133], v[6:7] op_sel_hi:[1,0]\n"
"v_mfma_f32_16x16x32_bf16 v[84:87], v[44:47], v[236:239], v[84:87]\n"
"ds_read_b128 v[36:39], v3 offset:33792\n"
"v_pk_mul_f32 v[150:151], v[134:135], v[6:7] op_sel_hi:[1,0]\n"
"v_mfma_f32_16x16x32_bf16 v[88:91], v[48:51], v[172:175], v[88:91]\n"
"v_pk_mul_f32 v[152:153], v[136:137], v[6:7] op_sel_hi:[1,0]\n"
"ds_read_b128 v[40:43], v3 offset:34816\n"
"v_mfma_f32_16x16x32_bf16 v[92:95], v[48:51], v[236:239], v[92:95]\n"
"v_pk_mul_f32 v[154:155], v[138:139], v[6:7] op_sel_hi:[1,0]\n"
"v_mfma_f32_16x16x32_bf16 v[96:99], v[52:55], v[172:175], v[96:99]\n"
"v_pk_mul_f32 v[156:157], v[140:141], v[6:7] op_sel_hi:[1,0]\n"
"v_mfma_f32_16x16x32_bf16 v[100:103], v[52:55], v[236:239], v[100:103]\n"
"v_mfma_f32_16x16x32_bf16 v[104:107], v[56:59], v[172:175], v[104:107]\n"
"v_pk_mul_f32 v[158:159], v[142:143], v[6:7] op_sel_hi:[1,0]\n"
"v_mfma_f32_16x16x32_bf16 v[108:111], v[56:59], v[236:239], v[108:111]\n"
"s_lshr_b32 s89, s82, 14\n"
"s_mul_i32 s87, s89, 28672\n"
"s_add_u32 s87, s87, 9437184\n"
"s_lshr_b32 s89, s76, 1\n"
"s_mul_i32 s89, s89, 7168\n"
"s_add_u32 s89, s89, s87\n"
"s_and_b32 s90, s76, 1\n"
"s_mul_i32 s88, s90, 3072\n"
"s_add_u32 s88, s88, s89\n"
"s_lshl_b32 s90, s90, 9\n"
"s_add_u32 s89, s89, s90\n"
"s_add_u32 s77, s89, 6144\n"
"v_mfma_f32_16x16x32_bf16 v[112:115], v[60:63], v[172:175], v[112:115]\n"
"v_lshrrev_b32 v8, 5, v1\n"
"v_and_b32 v11, 0xf0, v2\n"
"v_lshl_or_b32 v11, v8, 8, v11\n"
"s_waitcnt vmcnt(7)\n"
"s_waitcnt lgkmcnt(0)\n"
"v_mfma_f32_16x16x32_bf16 v[116:119], v[60:63], v[236:239], v[116:119]\n"
"s_barrier\n"
"v_mfma_f32_16x16x32_bf16 v[64:67], v[16:19], v[176:179], v[64:67]\n"
"ds_read_b128 v[44:47], v3 offset:57344\n"
"v_mfma_f32_16x16x32_bf16 v[68:71], v[16:19], v[240:243], v[68:71]\n"
"s_mul_i32 s66, s84, 43008\n"
"s_add_u32 s66, s66, s67\n"
"s_mul_i32 s83, s13, 409600\n"
"s_add_u32 s66, s66, s83\n"
"s_mul_i32 s83, s14, 0\n"
"s_add_u32 s66, s66, s83\n"
"s_add_u32 s66, s66, 17825792\n"
"s_add_u32 m0, s67, 0\n"
"s_add_u32 s95, s66, 294912\n"
"buffer_load_dwordx4 v2, s[32:35], s95 offen offset:3072 sc1 lds\n"
"ds_read_b128 v[48:51], v3 offset:58368\n"
"v_mfma_f32_16x16x32_bf16 v[72:75], v[20:23], v[176:179], v[72:75]\n"
"buffer_load_dwordx4 v2, s[32:35], s95 offen offset:1024 sc1 lds\n"
"ds_read_b128 v[52:55], v3 offset:59392\n"
"v_mfma_f32_16x16x32_bf16 v[76:79], v[20:23], v[240:243], v[76:79]\n"
"ds_read_b128 v[56:59], v3 offset:60416\n"
"buffer_load_dwordx4 v2, s[32:35], s95 offen offset:2048 sc1 lds\n"
"v_mfma_f32_16x16x32_bf16 v[80:83], v[24:27], v[176:179], v[80:83]\n"
"ds_read_b128 v[60:63], v3 offset:61440\n"
"s_bitcmp1_b32 s74, 1\n"
"s_cbranch_scc1 L265_%=\n"
"buffer_load_dwordx4 v2, s[32:35], s95 offen offset:0 sc1 lds\n"
"L265_%=:\n"
"v_mfma_f32_16x16x32_bf16 v[84:87], v[24:27], v[240:243], v[84:87]\n"
"ds_read_b128 v[16:19], v3 offset:62464\n"
"v_mfma_f32_16x16x32_bf16 v[88:91], v[28:31], v[176:179], v[88:91]\n"
"v_lshrrev_b32 v8, 4, v1\n"
"v_bfe_u32 v9, v1, 1, 3\n"
"v_lshlrev_b32 v8, 1, v8\n"
"v_sub_u32 v8, v9, v8\n"
"ds_read_b128 v[20:23], v3 offset:63488\n"
"v_mfma_f32_16x16x32_bf16 v[92:95], v[28:31], v[240:243], v[92:95]\n"
"v_and_b32 v9, 1, v1\n"
"v_lshlrev_b32 v9, 4, v9\n"
"v_mov_b32 v160, 0x3f80\n"
"v_lshlrev_b32 v9, v9, v160\n"
"v_mfma_f32_16x16x32_bf16 v[96:99], v[32:35], v[176:179], v[96:99]\n"
"v_mfma_f32_16x16x32_bf16 v[100:103], v[32:35], v[240:243], v[100:103]\n"
"v_mfma_f32_16x16x32_bf16 v[104:107], v[36:39], v[176:179], v[104:107]\n"
"v_cmp_eq_u32 vcc, 0, v8\n"
"v_mov_b64 v[160:161], 0\n"
"v_mov_b64 v[164:165], 0\n"
"v_cndmask_b32 v162, 0, v9, vcc\n"
"v_mfma_f32_16x16x32_bf16 v[108:111], v[36:39], v[240:243], v[108:111]\n"
"v_mfma_f32_16x16x32_bf16 v[112:115], v[40:43], v[176:179], v[112:115]\n"
"v_cmp_eq_u32 vcc, 1, v8\n"
"s_nop 1\n"
"v_cndmask_b32 v163, 0, v9, vcc\n"
"s_waitcnt vmcnt(7)\n"
"s_waitcnt lgkmcnt(0)\n"
"v_mfma_f32_16x16x32_bf16 v[116:119], v[40:43], v[240:243], v[116:119]\n"
"s_barrier\n"
"v_mfma_f32_16x16x32_bf16 v[64:67], v[44:47], v[180:183], v[64:67]\n"
"ds_read_b128 v[24:27], v4 offset:20480\n"
"v_mfma_f32_16x16x32_bf16 v[68:71], v[44:47], v[244:247], v[68:71]\n"
"ds_read_b128 v[28:31], v4 offset:21504\n"
"s_add_u32 m0, s67, 28672\n"
"s_add_u32 s95, s66, 309248\n"
"buffer_load_dwordx4 v2, s[32:35], s95 offen offset:0 sc1 lds\n"
"v_mfma_f32_16x16x32_bf16 v[72:75], v[48:51], v[180:183], v[72:75]\n"
"ds_read_b128 v[32:35], v4 offset:22528\n"
"v_mfma_f32_16x16x32_bf16 v[76:79], v[48:51], v[244:247], v[76:79]\n"
"buffer_load_dwordx4 v2, s[32:35], s95 offen offset:1024 sc1 lds\n"
"ds_read_b128 v[36:39], v4 offset:23552\n"
"v_mfma_f32_16x16x32_bf16 v[80:83], v[52:55], v[180:183], v[80:83]\n"
"ds_read_b128 v[40:43], v4 offset:24576\n"
"buffer_load_dwordx4 v2, s[32:35], s95 offen offset:2048 sc1 lds\n"
"v_mfma_f32_16x16x32_bf16 v[84:87], v[52:55], v[244:247], v[84:87]\n"
"ds_read_b128 v[44:47], v4 offset:25600\n"
"v_mfma_f32_16x16x32_bf16 v[88:91], v[56:59], v[180:183], v[88:91]\n"
"ds_read_b128 v[48:51], v4 offset:26624\n"
"s_bitcmp0_b32 s74, 1\n"
"s_cbranch_scc1 L266_%=\n"
"buffer_load_dwordx4 v2, s[32:35], s95 offen offset:3072 sc1 lds\n"
"L266_%=:\n"
"v_mfma_f32_16x16x32_bf16 v[92:95], v[56:59], v[244:247], v[92:95]\n"
"v_mfma_f32_16x16x32_bf16 v[96:99], v[60:63], v[180:183], v[96:99]\n"
"v_mfma_f32_16x16x32_bf16 v[100:103], v[60:63], v[244:247], v[100:103]\n"
"s_xor_b32 s89, s86, 0\n"
"s_mul_i32 s90, s89, 4608\n"
"s_add_u32 s90, s90, 114688\n"
"v_add_u32 v192, s90, v2\n"
"v_add_u32 v194, s90, v7\n"
"s_mul_i32 s90, s89, 6144\n"
"s_add_u32 s90, s90, 133120\n"
"v_add_u32 v196, s90, v2\n"
"v_mfma_f32_16x16x32_bf16 v[104:107], v[16:19], v[180:183], v[104:107]\n"
"v_mfma_f32_16x16x32_bf16 v[108:111], v[16:19], v[244:247], v[108:111]\n"
"v_mfma_f32_16x16x32_bf16 v[112:115], v[20:23], v[180:183], v[112:115]\n"
"s_xor_b32 s89, s86, 1\n"
"s_mul_i32 s90, s89, 4608\n"
"s_add_u32 s90, s90, 114688\n"
"v_add_u32 v193, s90, v2\n"
"v_add_u32 v195, s90, v7\n"
"s_mul_i32 s90, s89, 6144\n"
"s_add_u32 s90, s90, 133120\n"
"v_add_u32 v197, s90, v2\n"
"s_waitcnt vmcnt(7)\n"
"s_waitcnt lgkmcnt(0)\n"
"v_mfma_f32_16x16x32_bf16 v[116:119], v[20:23], v[244:247], v[116:119]\n"
"s_barrier\n"
"v_mfma_f32_16x16x32_bf16 v[64:67], v[24:27], v[184:187], v[64:67]\n"
"ds_read_b128 v[52:55], v4 offset:49152\n"
"v_mfma_f32_16x16x32_bf16 v[68:71], v[24:27], v[248:251], v[68:71]\n"
"ds_read_b128 v[56:59], v4 offset:50176\n"
"v_mfma_f32_16x16x32_bf16 v[72:75], v[28:31], v[184:187], v[72:75]\n"
"s_sub_u32 s66, s66, s83\n"
"s_sub_u32 s66, s66, s83\n"
"s_add_u32 m0, s67, 57344\n"
"s_add_u32 s95, s66, 323584\n"
"buffer_load_dwordx4 v2, s[32:35], s95 offen offset:3072 sc1 lds\n"
"ds_read_b128 v[60:63], v4 offset:51200\n"
"v_mfma_f32_16x16x32_bf16 v[76:79], v[28:31], v[248:251], v[76:79]\n"
"ds_read_b128 v[16:19], v4 offset:52224\n"
"v_mfma_f32_16x16x32_bf16 v[80:83], v[32:35], v[184:187], v[80:83]\n"
"ds_read_b128 v[20:23], v4 offset:53248\n"
"buffer_load_dwordx4 v2, s[32:35], s95 offen offset:1024 sc1 lds\n"
"v_mfma_f32_16x16x32_bf16 v[84:87], v[32:35], v[248:251], v[84:87]\n"
"ds_read_b128 v[24:27], v4 offset:54272\n"
"v_mfma_f32_16x16x32_bf16 v[88:91], v[36:39], v[184:187], v[88:91]\n"
"ds_read_b128 v[28:31], v4 offset:55296\n"
"v_mfma_f32_16x16x32_bf16 v[92:95], v[36:39], v[248:251], v[92:95]\n"
"buffer_load_dwordx4 v2, s[32:35], s95 offen offset:2048 sc1 lds\n"
"v_mfma_f32_16x16x32_bf16 v[96:99], v[40:43], v[184:187], v[96:99]\n"
"v_mfma_f32_16x16x32_bf16 v[100:103], v[40:43], v[248:251], v[100:103]\n"
"v_mfma_f32_16x16x32_bf16 v[104:107], v[44:47], v[184:187], v[104:107]\n"
"v_mfma_f32_16x16x32_bf16 v[108:111], v[44:47], v[248:251], v[108:111]\n"
"v_mfma_f32_16x16x32_bf16 v[112:115], v[48:51], v[184:187], v[112:115]\n"
"s_bitcmp1_b32 s74, 1\n"
"s_cbranch_scc1 L267_%=\n"
"buffer_load_dwordx4 v2, s[32:35], s95 offen offset:0 sc1 lds\n"
"L267_%=:\n"
"v_mfma_f32_16x16x32_bf16 v[116:119], v[48:51], v[248:251], v[116:119]\n"
"s_cmp_lg_u32 s84, 0\n"
"s_cbranch_scc1 L268_%=\n"
"s_waitcnt lgkmcnt(0)\n"
"s_barrier\n"
"v_mfma_f32_16x16x32_bf16 v[64:67], v[52:55], v[188:191], v[64:67]\n"
"v_mfma_f32_16x16x32_bf16 v[72:75], v[56:59], v[188:191], v[72:75]\n"
"v_mfma_f32_16x16x32_bf16 v[80:83], v[60:63], v[188:191], v[80:83]\n"
"s_add_u32 m0, s67, 86016\n"
"s_add_u32 s95, s66, 337920\n"
"buffer_load_dwordx4 v2, s[32:35], s95 offen offset:0 sc1 lds\n"
"v_mfma_f32_16x16x32_bf16 v[88:91], v[16:19], v[188:191], v[88:91]\n"
"v_mfma_f32_16x16x32_bf16 v[68:71], v[52:55], v[252:255], v[68:71]\n"
"s_nop 0\n"
"v_cvt_pk_bf16_f32 v200, v64, v65\n"
"v_cvt_pk_bf16_f32 v201, v66, v67\n"
"v_cvt_pk_bf16_f32 v202, v72, v73\n"
"v_cvt_pk_bf16_f32 v203, v74, v75\n"
"ds_write_b128 v196, v[200:203] offset:2048\n"
"buffer_load_dwordx4 v2, s[32:35], s95 offen offset:1024 sc1 lds\n"
"v_mfma_f32_16x16x32_bf16 v[76:79], v[56:59], v[252:255], v[76:79]\n"
"v_mfma_f32_16x16x32_bf16 v[84:87], v[60:63], v[252:255], v[84:87]\n"
"v_cvt_pk_bf16_f32 v204, v80, v81\n"
"v_cvt_pk_bf16_f32 v205, v82, v83\n"
"v_cvt_pk_bf16_f32 v206, v88, v89\n"
"v_cvt_pk_bf16_f32 v207, v90, v91\n"
"ds_write_b128 v196, v[204:207] offset:3072\n"
"buffer_load_dwordx4 v2, s[32:35], s95 offen offset:2048 sc1 lds\n"
"v_mfma_f32_16x16x32_bf16 v[92:95], v[16:19], v[252:255], v[92:95]\n"
"v_mfma_f32_16x16x32_bf16 v[100:103], v[20:23], v[252:255], v[100:103]\n"
"v_cvt_pk_bf16_f32 v208, v68, v69\n"
"v_cvt_pk_bf16_f32 v209, v70, v71\n"
"v_cvt_pk_bf16_f32 v210, v76, v77\n"
"v_cvt_pk_bf16_f32 v211, v78, v79\n"
"ds_write_b128 v197, v[208:211] offset:4096\n"
"s_bitcmp0_b32 s74, 1\n"
"s_cbranch_scc1 L270_%=\n"
"buffer_load_dwordx4 v2, s[32:35], s95 offen offset:3072 sc1 lds\n"
"L270_%=:\n"
"v_mfma_f32_16x16x32_bf16 v[108:111], v[24:27], v[252:255], v[108:111]\n"
"v_mfma_f32_16x16x32_bf16 v[116:119], v[28:31], v[252:255], v[116:119]\n"
"v_cvt_pk_bf16_f32 v212, v84, v85\n"
"v_cvt_pk_bf16_f32 v213, v86, v87\n"
"v_cvt_pk_bf16_f32 v214, v92, v93\n"
"v_cvt_pk_bf16_f32 v215, v94, v95\n"
"ds_write_b128 v197, v[212:215] offset:5120\n"
"v_mfma_f32_16x16x32_bf16 v[96:99], v[20:23], v[188:191], v[96:99]\n"
"v_mfma_f32_16x16x32_bf16 v[104:107], v[24:27], v[188:191], v[104:107]\n"
"v_cvt_pk_bf16_f32 v216, v100, v101\n"
"v_cvt_pk_bf16_f32 v217, v102, v103\n"
"v_cvt_pk_bf16_f32 v218, v108, v109\n"
"v_cvt_pk_bf16_f32 v219, v110, v111\n"
"ds_write_b128 v193, v[216:219] offset:0\n"
"v_mfma_f32_16x16x32_bf16 v[112:115], v[28:31], v[188:191], v[112:115]\n"
"v_cvt_pk_bf16_f32 v220, v116, v117\n"
"v_cvt_pk_bf16_f32 v221, v118, v119\n"
"ds_write_b64 v195, v[220:221] offset:1024\n"
"s_waitcnt vmcnt(7)\n"
"s_waitcnt lgkmcnt(0)\n"
"s_barrier\n"
"ds_read_b128 v[16:19], v192 offset:0\n"
"ds_read_b128 v[20:23], v192 offset:1536\n"
"ds_read_b128 v[24:27], v192 offset:3072\n"
"ds_read_b64 v[28:29], v194 offset:1024\n"
"ds_read_b64 v[32:33], v194 offset:2560\n"
"ds_read_b64 v[36:37], v194 offset:4096\n"
"s_waitcnt lgkmcnt(5)\n"
"v_mfma_f32_16x16x32_bf16 v[96:99], v[162:165], v[16:19], v[96:99]\n"
"v_mfma_f32_16x16x32_bf16 v[104:107], v[160:163], v[16:19], v[104:107]\n"
"s_waitcnt lgkmcnt(4)\n"
"v_mfma_f32_16x16x32_bf16 v[96:99], v[162:165], v[20:23], v[96:99]\n"
"v_mfma_f32_16x16x32_bf16 v[104:107], v[160:163], v[20:23], v[104:107]\n"
"s_waitcnt lgkmcnt(3)\n"
"v_mfma_f32_16x16x32_bf16 v[96:99], v[162:165], v[24:27], v[96:99]\n"
"v_mfma_f32_16x16x32_bf16 v[104:107], v[160:163], v[24:27], v[104:107]\n"
"s_waitcnt lgkmcnt(2)\n"
"v_mfma_f32_16x16x32_bf16 v[112:115], v[162:165], v[28:31], v[112:115]\n"
"s_waitcnt lgkmcnt(1)\n"
"v_mfma_f32_16x16x32_bf16 v[112:115], v[162:165], v[32:35], v[112:115]\n"
"s_waitcnt lgkmcnt(0)\n"
"v_mfma_f32_16x16x32_bf16 v[112:115], v[162:165], v[36:39], v[112:115]\n"
"s_nop 0\n"
"v_pk_fma_f32 v[96:97], v[96:97], v[128:129], v[144:145]\n"
"v_pk_fma_f32 v[98:99], v[98:99], v[130:131], v[146:147]\n"
"v_cvt_pk_bf16_f32 v168, v96, v97\n"
"v_cvt_pk_bf16_f32 v169, v98, v99\n"
"v_pk_fma_f32 v[104:105], v[104:105], v[132:133], v[148:149]\n"
"v_pk_fma_f32 v[106:107], v[106:107], v[134:135], v[150:151]\n"
"v_cvt_pk_bf16_f32 v170, v104, v105\n"
"v_cvt_pk_bf16_f32 v171, v106, v107\n"
"buffer_store_dwordx4 v[168:171], v2, s[32:35], s88 offen offset:2048 sc1\n"
"v_pk_fma_f32 v[112:113], v[112:113], v[136:137], v[152:153]\n"
"v_pk_fma_f32 v[114:115], v[114:115], v[138:139], v[154:155]\n"
"v_cvt_pk_bf16_f32 v172, v112, v113\n"
"v_cvt_pk_bf16_f32 v173, v114, v115\n"
"v_mov_b64 v[174:175], v[172:173]\n"
"s_nop 1\n"
"v_permlane16_swap_b32 v172, v174\n"
"v_permlane16_swap_b32 v173, v175\n"
"s_mov_b64 exec, s[80:81]\n"
"buffer_store_dwordx4 v[172:175], v11, s[32:35], s77 offen sc1\n"
"s_mov_b64 exec, -1\n"
"s_nop 15\n"
"s_branch L269_%=\n"
"L268_%=:\n"
"s_waitcnt lgkmcnt(0)\n"
"s_barrier\n"
"v_mfma_f32_16x16x32_bf16 v[96:99], v[20:23], v[188:191], v[96:99]\n"
"v_mfma_f32_16x16x32_bf16 v[104:107], v[24:27], v[188:191], v[104:107]\n"
"v_mfma_f32_16x16x32_bf16 v[112:115], v[28:31], v[188:191], v[112:115]\n"
"s_add_u32 m0, s67, 86016\n"
"s_add_u32 s95, s66, 337920\n"
"buffer_load_dwordx4 v2, s[32:35], s95 offen offset:0 sc1 lds\n"
"v_mfma_f32_16x16x32_bf16 v[100:103], v[20:23], v[252:255], v[100:103]\n"
"v_mfma_f32_16x16x32_bf16 v[108:111], v[24:27], v[252:255], v[108:111]\n"
"s_nop 0\n"
"v_cvt_pk_bf16_f32 v200, v96, v97\n"
"v_cvt_pk_bf16_f32 v201, v98, v99\n"
"v_cvt_pk_bf16_f32 v202, v104, v105\n"
"v_cvt_pk_bf16_f32 v203, v106, v107\n"
"ds_write_b128 v192, v[200:203] offset:1536\n"
"buffer_load_dwordx4 v2, s[32:35], s95 offen offset:1024 sc1 lds\n"
"v_mfma_f32_16x16x32_bf16 v[116:119], v[28:31], v[252:255], v[116:119]\n"
"v_cvt_pk_bf16_f32 v204, v112, v113\n"
"v_cvt_pk_bf16_f32 v205, v114, v115\n"
"ds_write_b64 v194, v[204:205] offset:2560\n"
"v_mfma_f32_16x16x32_bf16 v[68:71], v[52:55], v[252:255], v[68:71]\n"
"buffer_load_dwordx4 v2, s[32:35], s95 offen offset:2048 sc1 lds\n"
"v_mfma_f32_16x16x32_bf16 v[76:79], v[56:59], v[252:255], v[76:79]\n"
"v_cvt_pk_bf16_f32 v208, v100, v101\n"
"v_cvt_pk_bf16_f32 v209, v102, v103\n"
"v_cvt_pk_bf16_f32 v210, v108, v109\n"
"v_cvt_pk_bf16_f32 v211, v110, v111\n"
"ds_write_b128 v193, v[208:211] offset:3072\n"
"v_mfma_f32_16x16x32_bf16 v[84:87], v[60:63], v[252:255], v[84:87]\n"
"s_bitcmp0_b32 s74, 1\n"
"s_cbranch_scc1 L271_%=\n"
"buffer_load_dwordx4 v2, s[32:35], s95 offen offset:3072 sc1 lds\n"
"L271_%=:\n"
"v_cvt_pk_bf16_f32 v212, v116, v117\n"
"v_cvt_pk_bf16_f32 v213, v118, v119\n"
"ds_write_b64 v195, v[212:213] offset:4096\n"
"v_mfma_f32_16x16x32_bf16 v[92:95], v[16:19], v[252:255], v[92:95]\n"
"v_mfma_f32_16x16x32_bf16 v[64:67], v[52:55], v[188:191], v[64:67]\n"
"v_cvt_pk_bf16_f32 v216, v68, v69\n"
"v_cvt_pk_bf16_f32 v217, v70, v71\n"
"v_cvt_pk_bf16_f32 v218, v76, v77\n"
"v_cvt_pk_bf16_f32 v219, v78, v79\n"
"ds_write_b128 v197, v[216:219] offset:0\n"
"v_mfma_f32_16x16x32_bf16 v[72:75], v[56:59], v[188:191], v[72:75]\n"
"v_mfma_f32_16x16x32_bf16 v[80:83], v[60:63], v[188:191], v[80:83]\n"
"v_cvt_pk_bf16_f32 v220, v84, v85\n"
"v_cvt_pk_bf16_f32 v221, v86, v87\n"
"v_cvt_pk_bf16_f32 v222, v92, v93\n"
"v_cvt_pk_bf16_f32 v223, v94, v95\n"
"ds_write_b128 v197, v[220:223] offset:1024\n"
"v_mfma_f32_16x16x32_bf16 v[88:91], v[16:19], v[188:191], v[88:91]\n"
"s_waitcnt vmcnt(7)\n"
"s_waitcnt lgkmcnt(0)\n"
"s_barrier\n"
"ds_read_b128 v[16:19], v196 offset:0\n"
"ds_read_b128 v[20:23], v196 offset:2048\n"
"ds_read_b128 v[24:27], v196 offset:4096\n"
"ds_read_b128 v[28:31], v196 offset:1024\n"
"ds_read_b128 v[32:35], v196 offset:3072\n"
"ds_read_b128 v[36:39], v196 offset:5120\n"
"s_waitcnt lgkmcnt(5)\n"
"v_mfma_f32_16x16x32_bf16 v[64:67], v[162:165], v[16:19], v[64:67]\n"
"v_mfma_f32_16x16x32_bf16 v[72:75], v[160:163], v[16:19], v[72:75]\n"
"s_waitcnt lgkmcnt(4)\n"
"v_mfma_f32_16x16x32_bf16 v[64:67], v[162:165], v[20:23], v[64:67]\n"
"v_mfma_f32_16x16x32_bf16 v[72:75], v[160:163], v[20:23], v[72:75]\n"
"s_waitcnt lgkmcnt(3)\n"
"v_mfma_f32_16x16x32_bf16 v[64:67], v[162:165], v[24:27], v[64:67]\n"
"v_mfma_f32_16x16x32_bf16 v[72:75], v[160:163], v[24:27], v[72:75]\n"
"s_waitcnt lgkmcnt(2)\n"
"v_mfma_f32_16x16x32_bf16 v[80:83], v[162:165], v[28:31], v[80:83]\n"
"v_mfma_f32_16x16x32_bf16 v[88:91], v[160:163], v[28:31], v[88:91]\n"
"s_waitcnt lgkmcnt(1)\n"
"v_mfma_f32_16x16x32_bf16 v[80:83], v[162:165], v[32:35], v[80:83]\n"
"v_mfma_f32_16x16x32_bf16 v[88:91], v[160:163], v[32:35], v[88:91]\n"
"s_waitcnt lgkmcnt(0)\n"
"v_mfma_f32_16x16x32_bf16 v[80:83], v[162:165], v[36:39], v[80:83]\n"
"v_mfma_f32_16x16x32_bf16 v[88:91], v[160:163], v[36:39], v[88:91]\n"
"v_pk_fma_f32 v[64:65], v[64:65], v[128:129], v[144:145]\n"
"v_pk_fma_f32 v[66:67], v[66:67], v[130:131], v[146:147]\n"
"v_cvt_pk_bf16_f32 v168, v64, v65\n"
"v_cvt_pk_bf16_f32 v169, v66, v67\n"
"v_pk_fma_f32 v[72:73], v[72:73], v[132:133], v[148:149]\n"
"v_pk_fma_f32 v[74:75], v[74:75], v[134:135], v[150:151]\n"
"v_cvt_pk_bf16_f32 v170, v72, v73\n"
"v_cvt_pk_bf16_f32 v171, v74, v75\n"
"buffer_store_dwordx4 v[168:171], v2, s[32:35], s88 offen offset:0 sc1\n"
"v_pk_fma_f32 v[80:81], v[80:81], v[136:137], v[152:153]\n"
"v_pk_fma_f32 v[82:83], v[82:83], v[138:139], v[154:155]\n"
"v_cvt_pk_bf16_f32 v172, v80, v81\n"
"v_cvt_pk_bf16_f32 v173, v82, v83\n"
"v_pk_fma_f32 v[88:89], v[88:89], v[140:141], v[156:157]\n"
"v_pk_fma_f32 v[90:91], v[90:91], v[142:143], v[158:159]\n"
"v_cvt_pk_bf16_f32 v174, v88, v89\n"
"v_cvt_pk_bf16_f32 v175, v90, v91\n"
"buffer_store_dwordx4 v[172:175], v2, s[32:35], s88 offen offset:1024 sc1\n"
"s_nop 15\n"
"L269_%=:\n"
"s_mul_i32 s89, s84, 14336\n"
"v_add_u32 v3, s89, v2\n"
"v_add_u32 v4, 0x10000, v3\n"
"s_lshl_b32 s87, s12, 2\n"
"s_add_u32 s87, s87, s86\n"
"s_mul_i32 s87, s87, 28672\n"
"s_add_u32 s87, s87, 9437184\n"
"s_lshr_b32 s79, s13, 2\n"
"s_mul_i32 s89, s84, 7168\n"
"s_add_u32 s87, s87, s89\n"
"s_mul_i32 s89, s79, 14336\n"
"s_sub_u32 s85, s87, s89\n"
"s_add_u32 s85, s85, 14336\n"
"s_add_u32 s87, s87, s89\n"
"ds_read_b128 v[128:131], v3 offset:0\n"
"ds_read_b128 v[132:135], v3 offset:1024\n"
"ds_read_b128 v[136:139], v3 offset:2048\n"
"ds_read_b128 v[140:143], v3 offset:3072\n"
"ds_read_b128 v[144:147], v3 offset:4096\n"
"ds_read_b128 v[148:151], v3 offset:5120\n"
"ds_read_b128 v[152:155], v3 offset:6144\n"
"ds_read_b128 v[156:159], v3 offset:7168\n"
"ds_read_b128 v[160:163], v3 offset:8192\n"
"ds_read_b128 v[164:167], v3 offset:9216\n"
"ds_read_b128 v[168:171], v3 offset:10240\n"
"ds_read_b128 v[172:175], v3 offset:11264\n"
"ds_read_b128 v[176:179], v3 offset:12288\n"
"ds_read_b128 v[180:183], v3 offset:13312\n"
"ds_read_b128 v[184:187], v3 offset:28672\n"
"ds_read_b128 v[188:191], v3 offset:29696\n"
"ds_read_b128 v[192:195], v3 offset:30720\n"
"ds_read_b128 v[196:199], v3 offset:31744\n"
"ds_read_b128 v[200:203], v3 offset:32768\n"
"ds_read_b128 v[204:207], v3 offset:33792\n"
"v_lshrrev_b32 v7, 4, v1\n"
"v_lshlrev_b32 v7, 5, v7\n"
"s_lshl_b32 s89, s84, 7\n"
"v_add_u32 v7, s89, v7\n"
"v_xor_b32 v6, 128, v7\n"
"v_add_u32 v5, 158208, v7\n"
"v_add_u32 v11, 158208, v6\n"
"ds_read_b128 v[88:91], v5\n"
"ds_read_b128 v[92:95], v5 offset:16\n"
"ds_read_b128 v[104:107], v11\n"
"ds_read_b128 v[108:111], v11 offset:16\n"
"v_lshlrev_b32 v96, 16, v12\n"
"v_and_b32 v97, 0xffff0000, v12\n"
"v_lshlrev_b32 v98, 16, v13\n"
"v_and_b32 v99, 0xffff0000, v13\n"
"v_lshlrev_b32 v100, 16, v14\n"
"v_and_b32 v101, 0xffff0000, v14\n"
"v_lshlrev_b32 v102, 16, v15\n"
"v_and_b32 v103, 0xffff0000, v15\n"
"s_mul_i32 s89, s74, 4352\n"
"s_add_u32 s89, s89, 114688\n"
"v_and_b32 v8, 15, v1\n"
"v_mul_u32_u24 v8, 272, v8\n"
"v_add3_u32 v9, v8, v6, s89\n"
"v_add3_u32 v8, v8, v7, s89\n"
"s_mul_i32 s89, s74, 2176\n"
"s_add_u32 s89, s89, 114688\n"
"v_lshrrev_b32 v6, 3, v1\n"
"v_and_b32 v7, 7, v1\n"
"v_mul_u32_u24 v5, 272, v6\n"
"v_lshl_add_u32 v5, v7, 5, v5\n"
"v_add_u32 v5, s89, v5\n"
"v_lshlrev_b32 v6, 10, v6\n"
"v_lshl_or_b32 v10, v7, 4, v6\n"
"s_lshl_b32 s99, s12, 3\n"
"s_add_u32 s99, s99, s74\n"
"s_lshl_b32 s99, s99, 13\n"
"s_lshl_b32 s89, s13, 7\n"
"s_add_u32 s99, s99, s89\n"
"s_waitcnt vmcnt(0)\n"
"s_barrier\n"
"buffer_load_dwordx4 v[16:19], v2, s[32:35], s87 offen offset:0 sc1\n"
"buffer_load_dwordx4 v[20:23], v2, s[32:35], s87 offen offset:1024 sc1\n"
"buffer_load_dwordx4 v[24:27], v2, s[32:35], s87 offen offset:2048 sc1\n"
"buffer_load_dwordx4 v[28:31], v2, s[32:35], s87 offen offset:3072 sc1\n"
"ds_read_b128 v[208:211], v3 offset:34816\n"
"ds_read_b128 v[212:215], v3 offset:35840\n"
"ds_read_b128 v[216:219], v3 offset:36864\n"
"ds_read_b128 v[220:223], v3 offset:37888\n"
"ds_read_b128 v[224:227], v3 offset:38912\n"
"ds_read_b128 v[228:231], v3 offset:39936\n"
"ds_read_b128 v[232:235], v3 offset:40960\n"
"ds_read_b128 v[236:239], v3 offset:41984\n"
"ds_read_b128 v[240:243], v3 offset:57344\n"
"ds_read_b128 v[244:247], v3 offset:58368\n"
"ds_read_b128 v[248:251], v3 offset:59392\n"
"ds_read_b128 v[252:255], v3 offset:60416\n"
"s_waitcnt vmcnt(2)\n"
"v_max3_u32 v0, v17, v19, v21\n"
"v_max_u32 v0, v23, v0\n"
"v_cmp_eq_u32 vcc, -1, v0\n"
"s_nop 0\n"
"s_cbranch_vccnz RT272_%=\n"
"BK273_%=:\n"
"s_waitcnt lgkmcnt(15)\n"
"v_mfma_f32_16x16x32_bf16 v[80:83], v[136:139], v[16:19], 0\n"
"s_add_u32 s94, s87, 4096\n"
"buffer_load_dwordx4 v[32:35], v2, s[32:35], s94 offen offset:0 sc1\n"
"s_waitcnt lgkmcnt(15)\n"
"v_mfma_f32_16x16x32_bf16 v[84:87], v[140:143], v[16:19], 0\n"
"s_add_u32 s94, s87, 4096\n"
"buffer_load_dwordx4 v[36:39], v2, s[32:35], s94 offen offset:1024 sc1\n"
"v_mfma_f32_16x16x32_bf16 v[72:75], v[128:131], v[16:19], 0\n"
"v_mfma_f32_16x16x32_bf16 v[76:79], v[132:135], v[16:19], 0\n"
"ds_read_b128 v[128:131], v3 offset:61440\n"
"ds_read_b128 v[132:135], v3 offset:62464\n"
"ds_read_b128 v[136:139], v3 offset:63488\n"
"ds_read_b128 v[140:143], v3 offset:64512\n"
"s_waitcnt lgkmcnt(15)\n"
"v_mfma_f32_16x16x32_bf16 v[80:83], v[152:155], v[20:23], v[80:83]\n"
"s_add_u32 s94, s87, 4096\n"
"buffer_load_dwordx4 v[40:43], v2, s[32:35], s94 offen offset:2048 sc1\n"
"s_waitcnt lgkmcnt(15)\n"
"v_mfma_f32_16x16x32_bf16 v[84:87], v[156:159], v[20:23], v[84:87]\n"
"buffer_load_dwordx4 v[44:47], v2, s[32:35], s85 offen offset:0 sc1\n"
"v_mfma_f32_16x16x32_bf16 v[72:75], v[144:147], v[20:23], v[72:75]\n"
"v_mfma_f32_16x16x32_bf16 v[76:79], v[148:151], v[20:23], v[76:79]\n"
"ds_read_b128 v[144:147], v4 offset:0\n"
"ds_read_b128 v[148:151], v4 offset:1024\n"
"ds_read_b128 v[152:155], v4 offset:2048\n"
"ds_read_b128 v[156:159], v4 offset:3072\n"
"s_waitcnt vmcnt(4)\n"
"v_max3_u32 v0, v25, v27, v29\n"
"v_max_u32 v0, v31, v0\n"
"v_cmp_eq_u32 vcc, -1, v0\n"
"s_nop 0\n"
"s_cbranch_vccnz RT277_%=\n"
"BK278_%=:\n"
"s_waitcnt lgkmcnt(15)\n"
"v_mfma_f32_16x16x32_bf16 v[80:83], v[168:171], v[24:27], v[80:83]\n"
"buffer_load_dwordx4 v[48:51], v2, s[32:35], s85 offen offset:1024 sc1\n"
"s_waitcnt lgkmcnt(15)\n"
"v_mfma_f32_16x16x32_bf16 v[84:87], v[172:175], v[24:27], v[84:87]\n"
"v_mfma_f32_16x16x32_bf16 v[72:75], v[160:163], v[24:27], v[72:75]\n"
"v_mfma_f32_16x16x32_bf16 v[76:79], v[164:167], v[24:27], v[76:79]\n"
"ds_read_b128 v[160:163], v4 offset:4096\n"
"ds_read_b128 v[164:167], v4 offset:5120\n"
"ds_read_b128 v[168:171], v4 offset:20480\n"
"ds_read_b128 v[172:175], v4 offset:21504\n"
"s_waitcnt lgkmcnt(15)\n"
"v_mfma_f32_16x16x32_bf16 v[80:83], v[184:187], v[28:31], v[80:83]\n"
"buffer_load_dwordx4 v[52:55], v2, s[32:35], s85 offen offset:2048 sc1\n"
"s_waitcnt lgkmcnt(15)\n"
"v_mfma_f32_16x16x32_bf16 v[84:87], v[188:191], v[28:31], v[84:87]\n"
"v_mfma_f32_16x16x32_bf16 v[72:75], v[176:179], v[28:31], v[72:75]\n"
"v_mfma_f32_16x16x32_bf16 v[76:79], v[180:183], v[28:31], v[76:79]\n"
"ds_read_b128 v[176:179], v4 offset:22528\n"
"ds_read_b128 v[180:183], v4 offset:23552\n"
"ds_read_b128 v[184:187], v4 offset:24576\n"
"ds_read_b128 v[188:191], v4 offset:25600\n"
"s_waitcnt vmcnt(4)\n"
"v_max3_u32 v0, v33, v35, v37\n"
"v_max_u32 v0, v39, v0\n"
"v_cmp_eq_u32 vcc, -1, v0\n"
"s_nop 0\n"
"s_cbranch_vccnz RT283_%=\n"
"BK284_%=:\n"
"s_waitcnt lgkmcnt(15)\n"
"v_mfma_f32_16x16x32_bf16 v[80:83], v[200:203], v[32:35], v[80:83]\n"
"buffer_load_dwordx4 v[56:59], v2, s[32:35], s85 offen offset:3072 sc1\n"
"s_waitcnt lgkmcnt(15)\n"
"v_mfma_f32_16x16x32_bf16 v[84:87], v[204:207], v[32:35], v[84:87]\n"
"v_mfma_f32_16x16x32_bf16 v[72:75], v[192:195], v[32:35], v[72:75]\n"
"v_mfma_f32_16x16x32_bf16 v[76:79], v[196:199], v[32:35], v[76:79]\n"
"ds_read_b128 v[192:195], v4 offset:26624\n"
"ds_read_b128 v[196:199], v4 offset:27648\n"
"ds_read_b128 v[200:203], v4 offset:28672\n"
"ds_read_b128 v[204:207], v4 offset:29696\n"
"s_waitcnt lgkmcnt(15)\n"
"v_mfma_f32_16x16x32_bf16 v[80:83], v[216:219], v[36:39], v[80:83]\n"
"s_add_u32 s94, s85, 4096\n"
"buffer_load_dwordx4 v[60:63], v2, s[32:35], s94 offen offset:0 sc1\n"
"s_waitcnt lgkmcnt(15)\n"
"v_mfma_f32_16x16x32_bf16 v[84:87], v[220:223], v[36:39], v[84:87]\n"
"v_mfma_f32_16x16x32_bf16 v[72:75], v[208:211], v[36:39], v[72:75]\n"
"v_mfma_f32_16x16x32_bf16 v[76:79], v[212:215], v[36:39], v[76:79]\n"
"ds_read_b128 v[208:211], v4 offset:30720\n"
"ds_read_b128 v[212:215], v4 offset:31744\n"
"ds_read_b128 v[216:219], v4 offset:32768\n"
"ds_read_b128 v[220:223], v4 offset:33792\n"
"s_waitcnt vmcnt(4)\n"
"v_max3_u32 v0, v41, v43, v45\n"
"v_max_u32 v0, v47, v0\n"
"v_cmp_eq_u32 vcc, -1, v0\n"
"s_nop 0\n"
"s_cbranch_vccnz RT289_%=\n"
"BK290_%=:\n"
"s_waitcnt lgkmcnt(15)\n"
"v_mfma_f32_16x16x32_bf16 v[80:83], v[232:235], v[40:43], v[80:83]\n"
"s_add_u32 s94, s85, 4096\n"
"buffer_load_dwordx4 v[64:67], v2, s[32:35], s94 offen offset:1024 sc1\n"
"s_waitcnt lgkmcnt(15)\n"
"v_mfma_f32_16x16x32_bf16 v[84:87], v[236:239], v[40:43], v[84:87]\n"
"v_mfma_f32_16x16x32_bf16 v[72:75], v[224:227], v[40:43], v[72:75]\n"
"v_mfma_f32_16x16x32_bf16 v[76:79], v[228:231], v[40:43], v[76:79]\n"
"s_waitcnt lgkmcnt(15)\n"
"v_mfma_f32_16x16x32_bf16 v[80:83], v[248:251], v[44:47], v[80:83]\n"
"s_add_u32 s94, s85, 4096\n"
"buffer_load_dwordx4 v[68:71], v2, s[32:35], s94 offen offset:2048 sc1\n"
"s_waitcnt lgkmcnt(15)\n"
"v_mfma_f32_16x16x32_bf16 v[84:87], v[252:255], v[44:47], v[84:87]\n"
"v_mfma_f32_16x16x32_bf16 v[72:75], v[240:243], v[44:47], v[72:75]\n"
"v_mfma_f32_16x16x32_bf16 v[76:79], v[244:247], v[44:47], v[76:79]\n"
"s_waitcnt vmcnt(4)\n"
"v_max3_u32 v0, v49, v51, v53\n"
"v_max_u32 v0, v55, v0\n"
"v_cmp_eq_u32 vcc, -1, v0\n"
"s_nop 0\n"
"s_cbranch_vccnz RT295_%=\n"
"BK296_%=:\n"
"s_waitcnt lgkmcnt(15)\n"
"v_mfma_f32_16x16x32_bf16 v[80:83], v[136:139], v[48:51], v[80:83]\n"
"s_waitcnt lgkmcnt(15)\n"
"v_mfma_f32_16x16x32_bf16 v[84:87], v[140:143], v[48:51], v[84:87]\n"
"v_mfma_f32_16x16x32_bf16 v[72:75], v[128:131], v[48:51], v[72:75]\n"
"v_mfma_f32_16x16x32_bf16 v[76:79], v[132:135], v[48:51], v[76:79]\n"
"s_waitcnt lgkmcnt(15)\n"
"v_mfma_f32_16x16x32_bf16 v[80:83], v[152:155], v[52:55], v[80:83]\n"
"s_waitcnt lgkmcnt(15)\n"
"v_mfma_f32_16x16x32_bf16 v[84:87], v[156:159], v[52:55], v[84:87]\n"
"v_mfma_f32_16x16x32_bf16 v[72:75], v[144:147], v[52:55], v[72:75]\n"
"v_mfma_f32_16x16x32_bf16 v[76:79], v[148:151], v[52:55], v[76:79]\n"
"s_waitcnt vmcnt(2)\n"
"v_max3_u32 v0, v57, v59, v61\n"
"v_max_u32 v0, v63, v0\n"
"v_cmp_eq_u32 vcc, -1, v0\n"
"s_nop 0\n"
"s_cbranch_vccnz RT301_%=\n"
"BK302_%=:\n"
"s_waitcnt lgkmcnt(13)\n"
"v_mfma_f32_16x16x32_bf16 v[80:83], v[168:171], v[56:59], v[80:83]\n"
"s_waitcnt lgkmcnt(12)\n"
"v_mfma_f32_16x16x32_bf16 v[84:87], v[172:175], v[56:59], v[84:87]\n"
"v_mfma_f32_16x16x32_bf16 v[72:75], v[160:163], v[56:59], v[72:75]\n"
"v_mfma_f32_16x16x32_bf16 v[76:79], v[164:167], v[56:59], v[76:79]\n"
"s_waitcnt lgkmcnt(9)\n"
"v_mfma_f32_16x16x32_bf16 v[80:83], v[184:187], v[60:63], v[80:83]\n"
"s_waitcnt lgkmcnt(8)\n"
"v_mfma_f32_16x16x32_bf16 v[84:87], v[188:191], v[60:63], v[84:87]\n"
"v_mfma_f32_16x16x32_bf16 v[72:75], v[176:179], v[60:63], v[72:75]\n"
"v_mfma_f32_16x16x32_bf16 v[76:79], v[180:183], v[60:63], v[76:79]\n"
"s_waitcnt vmcnt(0)\n"
"v_max3_u32 v0, v65, v67, v69\n"
"v_max_u32 v0, v71, v0\n"
"v_cmp_eq_u32 vcc, -1, v0\n"
"s_nop 0\n"
"s_cbranch_vccnz RT306_%=\n"
"BK307_%=:\n"
"s_waitcnt lgkmcnt(5)\n"
"v_mfma_f32_16x16x32_bf16 v[80:83], v[200:203], v[64:67], v[80:83]\n"
"s_waitcnt lgkmcnt(4)\n"
"v_mfma_f32_16x16x32_bf16 v[84:87], v[204:207], v[64:67], v[84:87]\n"
"v_mfma_f32_16x16x32_bf16 v[72:75], v[192:195], v[64:67], v[72:75]\n"
"v_mfma_f32_16x16x32_bf16 v[76:79], v[196:199], v[64:67], v[76:79]\n"
"s_waitcnt lgkmcnt(1)\n"
"v_mfma_f32_16x16x32_bf16 v[80:83], v[216:219], v[68:71], v[80:83]\n"
"s_waitcnt lgkmcnt(0)\n"
"v_mfma_f32_16x16x32_bf16 v[84:87], v[220:223], v[68:71], v[84:87]\n"
"v_mfma_f32_16x16x32_bf16 v[72:75], v[208:211], v[68:71], v[72:75]\n"
"v_mfma_f32_16x16x32_bf16 v[76:79], v[212:215], v[68:71], v[76:79]\n"
"s_nop 3\n"
"v_pk_mul_f32 v[80:81], v[80:81], v[104:105]\n"
"v_pk_mul_f32 v[82:83], v[82:83], v[106:107]\n"
"ds_write_b128 v9, v[80:83] offset:0\n"
"v_pk_mul_f32 v[84:85], v[84:85], v[108:109]\n"
"v_pk_mul_f32 v[86:87], v[86:87], v[110:111]\n"
"ds_write_b128 v9, v[84:87] offset:16\n"
"v_pk_fma_f32 v[72:73], v[72:73], v[88:89], v[96:97]\n"
"v_pk_fma_f32 v[74:75], v[74:75], v[90:91], v[98:99]\n"
"ds_write_b128 v8, v[72:75] offset:0\n"
"v_pk_fma_f32 v[76:77], v[76:77], v[92:93], v[100:101]\n"
"v_pk_fma_f32 v[78:79], v[78:79], v[94:95], v[102:103]\n"
"ds_write_b128 v8, v[76:79] offset:16\n"
"s_waitcnt lgkmcnt(0)\n"
"s_barrier\n"
"ds_read_b128 v[16:19], v5 offset:0\n"
"ds_read_b128 v[20:23], v5 offset:17408\n"
"ds_read_b128 v[24:27], v5 offset:16\n"
"ds_read_b128 v[28:31], v5 offset:17424\n"
"s_waitcnt lgkmcnt(2)\n"
"v_pk_add_f32 v[16:17], v[16:17], v[20:21]\n"
"v_pk_add_f32 v[18:19], v[18:19], v[22:23]\n"
"v_cvt_pk_bf16_f32 v32, v16, v17\n"
"v_cvt_pk_bf16_f32 v33, v18, v19\n"
"s_waitcnt lgkmcnt(0)\n"
"v_pk_add_f32 v[24:25], v[24:25], v[28:29]\n"
"v_pk_add_f32 v[26:27], v[26:27], v[30:31]\n"
"v_cvt_pk_bf16_f32 v34, v24, v25\n"
"v_cvt_pk_bf16_f32 v35, v26, v27\n"
"buffer_store_dwordx4 v[32:35], v10, s[36:39], s99 offen\n"
"LFIN_%=:\n"
"s_endpgm\n"
"RT11_%=:\n"
"s_nop 15\n"
"s_waitcnt vmcnt(0) lgkmcnt(0)\n"
"SP13_%=:\n"
"s_sub_u32 s78, s78, 1\n"
"s_cmp_lt_i32 s78, 0\n"
"s_cbranch_scc1 SD14_%=\n"
"s_sleep 1\n"
"buffer_load_dwordx4 v[16:19], v11, s[32:35], s87 offen sc1\n"
"s_waitcnt vmcnt(0)\n"
"v_cmp_ne_u32 vcc, s72, v17\n"
"v_cmp_ne_u32 s[92:93], s73, v19\n"
"s_nop 0\n"
"s_or_b64 vcc, vcc, s[92:93]\n"
"s_waitcnt vmcnt(0) lgkmcnt(0)\n"
"s_cbranch_vccnz SP13_%=\n"
"SD14_%=:\n"
"s_nop 15\n"
"s_branch BK12_%=\n"
"RT19_%=:\n"
"s_nop 15\n"
"s_waitcnt vmcnt(0) lgkmcnt(0)\n"
"SP21_%=:\n"
"s_sub_u32 s78, s78, 1\n"
"s_cmp_lt_i32 s78, 0\n"
"s_cbranch_scc1 SD22_%=\n"
"s_sleep 1\n"
"s_mov_b64 exec, 0x3fe\n"
"buffer_load_dwordx2 v[12:13], v10, s[32:35], s23 offen sc1\n"
"s_mov_b64 exec, -1\n"
"s_waitcnt vmcnt(0)\n"
"v_cmp_ne_u64 vcc, s[72:73], v[12:13]\n"
"s_waitcnt vmcnt(0) lgkmcnt(0)\n"
"s_cbranch_vccnz SP21_%=\n"
"SD22_%=:\n"
"s_nop 15\n"
"s_branch BK20_%=\n"
"RT24_%=:\n"
"s_nop 15\n"
"s_waitcnt vmcnt(0) lgkmcnt(0)\n"
"SP26_%=:\n"
"s_sub_u32 s78, s78, 1\n"
"s_cmp_lt_i32 s78, 0\n"
"s_cbranch_scc1 SD27_%=\n"
"s_sleep 1\n"
"v_and_b32 v166, 1, v1\n"
"v_lshlrev_b32 v166, 11, v166\n"
"v_and_or_b32 v166, v1, 48, v166\n"
"buffer_load_dwordx4 v[200:203], v166, s[32:35], s67 offen offset:0 sc1\n"
"buffer_load_dwordx4 v[204:207], v166, s[32:35], s67 offen offset:1024 sc1\n"
"s_waitcnt vmcnt(1)\n"
"v_xor_b32 v164, v201, v14\n"
"v_xor_b32 v165, v203, v15\n"
"s_waitcnt vmcnt(0)\n"
"v_bitop3_b32 v164, v205, v14, v164 bitop3:0xbe\n"
"v_bitop3_b32 v165, v207, v15, v165 bitop3:0xbe\n"
"v_cmp_ne_u64 vcc, 0, v[164:165]\n"
"s_waitcnt vmcnt(0) lgkmcnt(0)\n"
"s_cbranch_vccnz SP26_%=\n"
"SD27_%=:\n"
"s_nop 15\n"
"s_branch BK25_%=\n"
"RT28_%=:\n"
"s_nop 15\n"
"s_waitcnt vmcnt(0) lgkmcnt(0)\n"
"SP30_%=:\n"
"s_sub_u32 s78, s78, 1\n"
"s_cmp_lt_i32 s78, 0\n"
"s_cbranch_scc1 SD31_%=\n"
"s_sleep 1\n"
"s_add_u32 s94, s75, 1\n"
"s_and_b32 s94, s94, 31\n"
"s_lshl_b32 s94, s94, 12\n"
"buffer_load_dwordx4 v[84:87], v4, s[32:35], s94 offen sc1\n"
"s_mov_b64 exec, s[80:81]\n"
"buffer_load_dwordx4 v[88:91], v4, s[32:35], s94 offen offset:1024 sc1\n"
"buffer_load_dwordx4 v[92:95], v4, s[32:35], s94 offen offset:1280 sc1\n"
"s_mov_b64 exec, -1\n"
"s_waitcnt vmcnt(1)\n"
"v_max3_u32 v254, v85, v87, v89\n"
"s_waitcnt vmcnt(0)\n"
"v_max3_u32 v254, v91, v93, v254\n"
"v_max_u32 v254, v95, v254\n"
"v_cmp_eq_u32 vcc, -1, v254\n"
"s_waitcnt vmcnt(0) lgkmcnt(0)\n"
"s_cbranch_vccnz SP30_%=\n"
"SD31_%=:\n"
"s_nop 15\n"
"s_branch BK29_%=\n"
"RT32_%=:\n"
"s_nop 15\n"
"s_waitcnt vmcnt(0) lgkmcnt(0)\n"
"SP34_%=:\n"
"s_sub_u32 s78, s78, 1\n"
"s_cmp_lt_i32 s78, 0\n"
"s_cbranch_scc1 SD35_%=\n"
"s_sleep 1\n"
"s_add_u32 s94, s75, 2\n"
"s_and_b32 s94, s94, 31\n"
"s_lshl_b32 s94, s94, 12\n"
"buffer_load_dwordx4 v[48:51], v4, s[32:35], s94 offen sc1\n"
"s_mov_b64 exec, s[80:81]\n"
"buffer_load_dwordx4 v[52:55], v4, s[32:35], s94 offen offset:1024 sc1\n"
"buffer_load_dwordx4 v[56:59], v4, s[32:35], s94 offen offset:1280 sc1\n"
"s_mov_b64 exec, -1\n"
"s_waitcnt vmcnt(1)\n"
"v_max3_u32 v254, v49, v51, v53\n"
"s_waitcnt vmcnt(0)\n"
"v_max3_u32 v254, v55, v57, v254\n"
"v_max_u32 v254, v59, v254\n"
"v_cmp_eq_u32 vcc, -1, v254\n"
"s_waitcnt vmcnt(0) lgkmcnt(0)\n"
"s_cbranch_vccnz SP34_%=\n"
"SD35_%=:\n"
"s_nop 15\n"
"s_branch BK33_%=\n"
"RT36_%=:\n"
"s_nop 15\n"
"s_waitcnt vmcnt(0) lgkmcnt(0)\n"
"SP38_%=:\n"
"s_sub_u32 s78, s78, 1\n"
"s_cmp_lt_i32 s78, 0\n"
"s_cbranch_scc1 SD39_%=\n"
"s_sleep 1\n"
"s_add_u32 s94, s75, 3\n"
"s_and_b32 s94, s94, 31\n"
"s_lshl_b32 s94, s94, 12\n"
"buffer_load_dwordx4 v[192:195], v4, s[32:35], s94 offen sc1\n"
"s_mov_b64 exec, s[80:81]\n"
"buffer_load_dwordx4 v[196:199], v4, s[32:35], s94 offen offset:1024 sc1\n"
"buffer_load_dwordx4 v[200:203], v4, s[32:35], s94 offen offset:1280 sc1\n"
"s_mov_b64 exec, -1\n"
"s_waitcnt vmcnt(1)\n"
"v_max3_u32 v254, v193, v195, v197\n"
"s_waitcnt vmcnt(0)\n"
"v_max3_u32 v254, v199, v201, v254\n"
"v_max_u32 v254, v203, v254\n"
"v_cmp_eq_u32 vcc, -1, v254\n"
"s_waitcnt vmcnt(0) lgkmcnt(0)\n"
"s_cbranch_vccnz SP38_%=\n"
"SD39_%=:\n"
"s_nop 15\n"
"s_branch BK37_%=\n"
"RT41_%=:\n"
"s_nop 15\n"
"s_waitcnt vmcnt(0) lgkmcnt(0)\n"
"SP43_%=:\n"
"s_sub_u32 s78, s78, 1\n"
"s_cmp_lt_i32 s78, 0\n"
"s_cbranch_scc1 SD44_%=\n"
"s_sleep 1\n"
"s_add_u32 s94, s75, 4\n"
"s_and_b32 s94, s94, 31\n"
"s_lshl_b32 s94, s94, 12\n"
"buffer_load_dwordx4 v[224:227], v4, s[32:35], s94 offen sc1\n"
"s_mov_b64 exec, s[80:81]\n"
"buffer_load_dwordx4 v[228:231], v4, s[32:35], s94 offen offset:1024 sc1\n"
"buffer_load_dwordx4 v[232:235], v4, s[32:35], s94 offen offset:1280 sc1\n"
"s_mov_b64 exec, -1\n"
"s_waitcnt vmcnt(1)\n"
"v_max3_u32 v254, v225, v227, v229\n"
"s_waitcnt vmcnt(0)\n"
"v_max3_u32 v254, v231, v233, v254\n"
"v_max_u32 v254, v235, v254\n"
"v_cmp_eq_u32 vcc, -1, v254\n"
"s_waitcnt vmcnt(0) lgkmcnt(0)\n"
"s_cbranch_vccnz SP43_%=\n"
"SD44_%=:\n"
"s_nop 15\n"
"s_branch BK42_%=\n"
"RT46_%=:\n"
"s_nop 15\n"
"s_waitcnt vmcnt(0) lgkmcnt(0)\n"
"SP48_%=:\n"
"s_sub_u32 s78, s78, 1\n"
"s_cmp_lt_i32 s78, 0\n"
"s_cbranch_scc1 SD49_%=\n"
"s_sleep 1\n"
"s_add_u32 s94, s75, 5\n"
"s_and_b32 s94, s94, 31\n"
"s_lshl_b32 s94, s94, 12\n"
"buffer_load_dwordx4 v[236:239], v4, s[32:35], s94 offen sc1\n"
"s_mov_b64 exec, s[80:81]\n"
"buffer_load_dwordx4 v[240:243], v4, s[32:35], s94 offen offset:1024 sc1\n"
"buffer_load_dwordx4 v[244:247], v4, s[32:35], s94 offen offset:1280 sc1\n"
"s_mov_b64 exec, -1\n"
"s_waitcnt vmcnt(1)\n"
"v_max3_u32 v254, v237, v239, v241\n"
"s_waitcnt vmcnt(0)\n"
"v_max3_u32 v254, v243, v245, v254\n"
"v_max_u32 v254, v247, v254\n"
"v_cmp_eq_u32 vcc, -1, v254\n"
"s_waitcnt vmcnt(0) lgkmcnt(0)\n"
"s_cbranch_vccnz SP48_%=\n"
"SD49_%=:\n"
"s_nop 15\n"
"s_branch BK47_%=\n"
"RT52_%=:\n"
"s_nop 15\n"
"s_waitcnt vmcnt(0) lgkmcnt(0)\n"
"SP54_%=:\n"
"s_sub_u32 s78, s78, 1\n"
"s_cmp_lt_i32 s78, 0\n"
"s_cbranch_scc1 SD55_%=\n"
"s_sleep 1\n"
"s_add_u32 s94, s75, 6\n"
"s_and_b32 s94, s94, 31\n"
"s_lshl_b32 s94, s94, 12\n"
"buffer_load_dwordx4 v[72:75], v4, s[32:35], s94 offen sc1\n"
"s_mov_b64 exec, s[80:81]\n"
"buffer_load_dwordx4 v[76:79], v4, s[32:35], s94 offen offset:1024 sc1\n"
"buffer_load_dwordx4 v[80:83], v4, s[32:35], s94 offen offset:1280 sc1\n"
"s_mov_b64 exec, -1\n"
"s_waitcnt vmcnt(1)\n"
"v_max3_u32 v254, v73, v75, v77\n"
"s_waitcnt vmcnt(0)\n"
"v_max3_u32 v254, v79, v81, v254\n"
"v_max_u32 v254, v83, v254\n"
"v_cmp_eq_u32 vcc, -1, v254\n"
"s_waitcnt vmcnt(0) lgkmcnt(0)\n"
"s_cbranch_vccnz SP54_%=\n"
"SD55_%=:\n"
"s_nop 15\n"
"s_branch BK53_%=\n"
"RT57_%=:\n"
"s_nop 15\n"
"s_waitcnt vmcnt(0) lgkmcnt(0)\n"
"SP59_%=:\n"
"s_sub_u32 s78, s78, 1\n"
"s_cmp_lt_i32 s78, 0\n"
"s_cbranch_scc1 SD60_%=\n"
"s_sleep 1\n"
"buffer_load_dwordx2 v[12:13], v8, s[32:35], s69 offen sc1\n"
"s_waitcnt vmcnt(0)\n"
"v_cmp_ne_u64 vcc, s[72:73], v[12:13]\n"
"s_waitcnt vmcnt(0) lgkmcnt(0)\n"
"s_cbranch_vccnz SP59_%=\n"
"SD60_%=:\n"
"s_nop 15\n"
"s_branch BK58_%=\n"
"RT61_%=:\n"
"s_nop 15\n"
"s_waitcnt vmcnt(0) lgkmcnt(0)\n"
"SP63_%=:\n"
"s_sub_u32 s78, s78, 1\n"
"s_cmp_lt_i32 s78, 0\n"
"s_cbranch_scc1 SD64_%=\n"
"s_sleep 1\n"
"s_add_u32 s94, s75, 7\n"
"s_and_b32 s94, s94, 31\n"
"s_lshl_b32 s94, s94, 12\n"
"buffer_load_dwordx4 v[84:87], v4, s[32:35], s94 offen sc1\n"
"s_mov_b64 exec, s[80:81]\n"
"buffer_load_dwordx4 v[88:91], v4, s[32:35], s94 offen offset:1024 sc1\n"
"buffer_load_dwordx4 v[92:95], v4, s[32:35], s94 offen offset:1280 sc1\n"
"s_mov_b64 exec, -1\n"
"s_waitcnt vmcnt(1)\n"
"v_max3_u32 v254, v85, v87, v89\n"
"s_waitcnt vmcnt(0)\n"
"v_max3_u32 v254, v91, v93, v254\n"
"v_max_u32 v254, v95, v254\n"
"v_cmp_eq_u32 vcc, -1, v254\n"
"s_waitcnt vmcnt(0) lgkmcnt(0)\n"
"s_cbranch_vccnz SP63_%=\n"
"SD64_%=:\n"
"s_nop 15\n"
"s_branch BK62_%=\n"
"RT65_%=:\n"
"s_nop 15\n"
"s_waitcnt vmcnt(0) lgkmcnt(0)\n"
"SP67_%=:\n"
"s_sub_u32 s78, s78, 1\n"
"s_cmp_lt_i32 s78, 0\n"
"s_cbranch_scc1 SD68_%=\n"
"s_sleep 1\n"
"s_add_u32 s94, s75, 8\n"
"s_and_b32 s94, s94, 31\n"
"s_lshl_b32 s94, s94, 12\n"
"buffer_load_dwordx4 v[48:51], v4, s[32:35], s94 offen sc1\n"
"s_mov_b64 exec, s[80:81]\n"
"buffer_load_dwordx4 v[52:55], v4, s[32:35], s94 offen offset:1024 sc1\n"
"buffer_load_dwordx4 v[56:59], v4, s[32:35], s94 offen offset:1280 sc1\n"
"s_mov_b64 exec, -1\n"
"s_waitcnt vmcnt(1)\n"
"v_max3_u32 v254, v49, v51, v53\n"
"s_waitcnt vmcnt(0)\n"
"v_max3_u32 v254, v55, v57, v254\n"
"v_max_u32 v254, v59, v254\n"
"v_cmp_eq_u32 vcc, -1, v254\n"
"s_waitcnt vmcnt(0) lgkmcnt(0)\n"
"s_cbranch_vccnz SP67_%=\n"
"SD68_%=:\n"
"s_nop 15\n"
"s_branch BK66_%=\n"
"RT69_%=:\n"
"s_nop 15\n"
"s_waitcnt vmcnt(0) lgkmcnt(0)\n"
"SP71_%=:\n"
"s_sub_u32 s78, s78, 1\n"
"s_cmp_lt_i32 s78, 0\n"
"s_cbranch_scc1 SD72_%=\n"
"s_sleep 1\n"
"s_add_u32 s94, s75, 9\n"
"s_and_b32 s94, s94, 31\n"
"s_lshl_b32 s94, s94, 12\n"
"buffer_load_dwordx4 v[192:195], v4, s[32:35], s94 offen sc1\n"
"s_mov_b64 exec, s[80:81]\n"
"buffer_load_dwordx4 v[196:199], v4, s[32:35], s94 offen offset:1024 sc1\n"
"buffer_load_dwordx4 v[200:203], v4, s[32:35], s94 offen offset:1280 sc1\n"
"s_mov_b64 exec, -1\n"
"s_waitcnt vmcnt(1)\n"
"v_max3_u32 v254, v193, v195, v197\n"
"s_waitcnt vmcnt(0)\n"
"v_max3_u32 v254, v199, v201, v254\n"
"v_max_u32 v254, v203, v254\n"
"v_cmp_eq_u32 vcc, -1, v254\n"
"s_waitcnt vmcnt(0) lgkmcnt(0)\n"
"s_cbranch_vccnz SP71_%=\n"
"SD72_%=:\n"
"s_nop 15\n"
"s_branch BK70_%=\n"
"RT75_%=:\n"
"s_nop 15\n"
"s_waitcnt vmcnt(0) lgkmcnt(0)\n"
"SP77_%=:\n"
"s_sub_u32 s78, s78, 1\n"
"s_cmp_lt_i32 s78, 0\n"
"s_cbranch_scc1 SD78_%=\n"
"s_sleep 1\n"
"buffer_load_dwordx4 v[180:183], v60, s[32:35], s68 offen sc1\n"
"s_waitcnt vmcnt(0)\n"
"v_cmp_ne_u32 vcc, s72, v181\n"
"v_cmp_ne_u32 s[92:93], s73, v183\n"
"s_nop 0\n"
"s_or_b64 vcc, vcc, s[92:93]\n"
"s_waitcnt vmcnt(0) lgkmcnt(0)\n"
"s_cbranch_vccnz SP77_%=\n"
"SD78_%=:\n"
"s_nop 15\n"
"s_branch BK76_%=\n"
"RT79_%=:\n"
"s_nop 15\n"
"s_waitcnt vmcnt(0) lgkmcnt(0)\n"
"SP81_%=:\n"
"s_sub_u32 s78, s78, 1\n"
"s_cmp_lt_i32 s78, 0\n"
"s_cbranch_scc1 SD82_%=\n"
"s_sleep 1\n"
"buffer_load_dwordx2 v[62:63], v60, s[32:35], s99 offen sc1\n"
"s_waitcnt vmcnt(0)\n"
"v_cmp_ne_u64 vcc, s[72:73], v[62:63]\n"
"s_waitcnt vmcnt(0) lgkmcnt(0)\n"
"s_cbranch_vccnz SP81_%=\n"
"SD82_%=:\n"
"s_nop 15\n"
"s_branch BK80_%=\n"
"RT84_%=:\n"
"s_nop 15\n"
"s_waitcnt vmcnt(0) lgkmcnt(0)\n"
"SP86_%=:\n"
"s_sub_u32 s78, s78, 1\n"
"s_cmp_lt_i32 s78, 0\n"
"s_cbranch_scc1 SD87_%=\n"
"s_sleep 1\n"
"buffer_load_dwordx4 v[16:19], v2, s[32:35], s84 offen offset:0 sc1\n"
"buffer_load_dwordx4 v[20:23], v2, s[32:35], s84 offen offset:1024 sc1\n"
"buffer_load_dwordx4 v[24:27], v2, s[32:35], s84 offen offset:2048 sc1\n"
"buffer_load_dwordx4 v[28:31], v2, s[32:35], s84 offen offset:3072 sc1\n"
"buffer_load_dwordx4 v[68:71], v9, s[32:35], s86 offen sc1\n"
"s_waitcnt vmcnt(4)\n"
"v_cmp_ne_u64 vcc, s[72:73], v[16:17]\n"
"v_cmp_ne_u64 s[92:93], s[72:73], v[18:19]\n"
"s_nop 0\n"
"s_or_b64 vcc, vcc, s[92:93]\n"
"s_waitcnt vmcnt(3)\n"
"v_cmp_ne_u64 s[92:93], s[72:73], v[20:21]\n"
"s_nop 0\n"
"s_or_b64 vcc, vcc, s[92:93]\n"
"v_cmp_ne_u64 s[92:93], s[72:73], v[22:23]\n"
"s_nop 0\n"
"s_or_b64 vcc, vcc, s[92:93]\n"
"s_waitcnt vmcnt(2)\n"
"v_cmp_ne_u64 s[92:93], s[72:73], v[24:25]\n"
"s_nop 0\n"
"s_or_b64 vcc, vcc, s[92:93]\n"
"v_cmp_ne_u64 s[92:93], s[72:73], v[26:27]\n"
"s_nop 0\n"
"s_or_b64 vcc, vcc, s[92:93]\n"
"s_waitcnt vmcnt(1)\n"
"v_cmp_ne_u64 s[92:93], s[72:73], v[28:29]\n"
"s_nop 0\n"
"s_or_b64 vcc, vcc, s[92:93]\n"
"v_cmp_ne_u64 s[92:93], s[72:73], v[30:31]\n"
"s_nop 0\n"
"s_or_b64 vcc, vcc, s[92:93]\n"
"s_waitcnt vmcnt(0)\n"
"v_cmp_ne_u64 s[92:93], s[72:73], v[68:69]\n"
"s_nop 0\n"
"s_or_b64 vcc, vcc, s[92:93]\n"
"v_cmp_ne_u64 s[92:93], s[72:73], v[70:71]\n"
"s_nop 0\n"
"s_or_b64 vcc, vcc, s[92:93]\n"
"s_waitcnt vmcnt(0) lgkmcnt(0)\n"
"s_cbranch_vccnz SP86_%=\n"
"SD87_%=:\n"
"s_nop 15\n"
"s_branch BK85_%=\n"
"FB88_%=:\n"
"s_nop 15\n"
"s_waitcnt vmcnt(0) lgkmcnt(0)\n"
"s_lshr_b32 s83, s74, 2\n"
"s_lshl_b32 s83, s83, 11\n"
"v_subrev_u32 v4, s83, v4\n"
"s_mov_b32 s83, 0\n"
"v_mov_b32 v32, 0xff800000\n"
"L90_%=:\n"
"buffer_load_dwordx4 v[72:75], v4, s[32:35], s83 offen sc1\n"
"s_waitcnt vmcnt(0)\n"
"v_mfma_f32_32x32x16_bf16 v[16:31], v[72:75], v[64:67], 0\n"
"s_nop 11\n"
"v_max3_f32 v32, v16, v17, v32\n"
"v_max3_f32 v32, v18, v19, v32\n"
"v_max3_f32 v32, v20, v21, v32\n"
"v_max3_f32 v32, v22, v23, v32\n"
"v_max3_f32 v32, v24, v25, v32\n"
"v_max3_f32 v32, v26, v27, v32\n"
"v_max3_f32 v32, v28, v29, v32\n"
"v_max3_f32 v32, v30, v31, v32\n"
"s_add_u32 s83, s83, 2048\n"
"s_cmp_lt_u32 s83, 0x20000\n"
"s_cbranch_scc1 L90_%=\n"
"v_mov_b32 v33, v32\n"
"s_nop 1\n"
"v_permlane32_swap_b32 v32, v33\n"
"v_max_f32 v32, v32, v33\n"
"v_sub_f32 v32, 0, v32\n"
"v_mov_b32 v33, v32\n"
"v_mov_b32 v34, v32\n"
"v_mov_b32 v35, v32\n"
"v_mov_b32 v36, v32\n"
"v_mov_b32 v37, v32\n"
"v_mov_b32 v38, v32\n"
"v_mov_b32 v39, v32\n"
"v_mov_b32 v40, v32\n"
"v_mov_b32 v41, v32\n"
"v_mov_b32 v42, v32\n"
"v_mov_b32 v43, v32\n"
"v_mov_b32 v44, v32\n"
"v_mov_b32 v45, v32\n"
"v_mov_b32 v46, v32\n"
"v_mov_b32 v47, v32\n"
"v_mov_b32 v96, 0\n"
"v_mov_b32 v97, 0\n"
"v_mov_b32 v98, 0\n"
"v_mov_b32 v99, 0\n"
"v_mov_b32 v100, 0\n"
"v_mov_b32 v101, 0\n"
"v_mov_b32 v102, 0\n"
"v_mov_b32 v103, 0\n"
"v_mov_b32 v104, 0\n"
"v_mov_b32 v105, 0\n"
"v_mov_b32 v106, 0\n"
"v_mov_b32 v107, 0\n"
"v_mov_b32 v108, 0\n"
"v_mov_b32 v109, 0\n"
"v_mov_b32 v110, 0\n"
"v_mov_b32 v111, 0\n"
"s_mov_b32 s83, 0\n"
"L91_%=:\n"
"buffer_load_dwordx4 v[72:75], v4, s[32:35], s83 offen sc1\n"
"s_mov_b64 exec, s[80:81]\n"
"buffer_load_dwordx4 v[76:79], v4, s[32:35], s83 offen offset:1024 sc1\n"
"buffer_load_dwordx4 v[80:83], v4, s[32:35], s83 offen offset:1280 sc1\n"
"s_mov_b64 exec, -1\n"
"s_waitcnt vmcnt(2)\n"
"v_mfma_f32_32x32x16_bf16 v[16:31], v[72:75], v[64:67], v[32:47]\n"
"s_nop 11\n"
"v_exp_f32 v16, v16\n"
"v_exp_f32 v17, v17\n"
"v_exp_f32 v18, v18\n"
"v_exp_f32 v19, v19\n"
"v_exp_f32 v20, v20\n"
"v_exp_f32 v21, v21\n"
"v_exp_f32 v22, v22\n"
"v_exp_f32 v23, v23\n"
"v_exp_f32 v24, v24\n"
"v_exp_f32 v25, v25\n"
"v_exp_f32 v26, v26\n"
"v_exp_f32 v27, v27\n"
"v_exp_f32 v28, v28\n"
"v_exp_f32 v29, v29\n"
"v_exp_f32 v30, v30\n"
"v_exp_f32 v31, v31\n"
"s_waitcnt vmcnt(1)\n"
"v_smfmac_f32_32x32x32_bf16 v[96:111], v[76:79], v[16:23], v0\n"
"s_waitcnt vmcnt(0)\n"
"v_smfmac_f32_32x32x32_bf16 v[96:111], v[80:83], v[24:31], v0\n"
"s_nop 15\n"
"s_add_u32 s83, s83, 2048\n"
"s_cmp_lt_u32 s83, 0x20000\n"
"s_cbranch_scc1 L91_%=\n"
"s_nop 15\n"
"s_branch BK89_%=\n"
"RT95_%=:\n"
"s_nop 15\n"
"s_waitcnt vmcnt(0) lgkmcnt(0)\n"
"SP97_%=:\n"
"s_sub_u32 s78, s78, 1\n"
"s_cmp_lt_i32 s78, 0\n"
"s_cbranch_scc1 SD98_%=\n"
"s_sleep 1\n"
"s_mov_b64 exec, s[66:67]\n"
"buffer_load_dwordx2 v[8:9], v5, s[32:35], s68 offen sc1\n"
"s_mov_b64 exec, -1\n"
"s_waitcnt vmcnt(0)\n"
"v_cmp_ne_u64 vcc, s[72:73], v[8:9]\n"
"s_waitcnt vmcnt(0) lgkmcnt(0)\n"
"s_cbranch_vccnz SP97_%=\n"
"SD98_%=:\n"
"s_nop 15\n"
"s_branch BK96_%=\n"
"RT99_%=:\n"
"s_nop 15\n"
"s_waitcnt vmcnt(0) lgkmcnt(0)\n"
"SP101_%=:\n"
"s_sub_u32 s78, s78, 1\n"
"s_cmp_lt_i32 s78, 0\n"
"s_cbranch_scc1 SD102_%=\n"
"s_sleep 1\n"
"buffer_load_dwordx4 v[16:19], v2, s[32:35], s87 offen offset:0 sc1\n"
"buffer_load_dwordx4 v[20:23], v2, s[32:35], s87 offen offset:1024 sc1\n"
"v_max3_u32 v0, v25, v27, v29\n"
"v_max_u32 v0, v31, v0\n"
"v_cmp_eq_u32 vcc, -1, v0\n"
"s_nop 0\n"
"s_cbranch_vccz L103_%=\n"
"buffer_load_dwordx4 v[24:27], v2, s[32:35], s87 offen offset:2048 sc1\n"
"buffer_load_dwordx4 v[28:31], v2, s[32:35], s87 offen offset:3072 sc1\n"
"L103_%=:\n"
"s_waitcnt vmcnt(0) lgkmcnt(0)\n"
"v_max3_u32 v0, v17, v19, v21\n"
"v_max_u32 v0, v23, v0\n"
"v_cmp_eq_u32 vcc, -1, v0\n"
"s_waitcnt vmcnt(0) lgkmcnt(0)\n"
"s_cbranch_vccnz SP101_%=\n"
"SD102_%=:\n"
"s_nop 15\n"
"s_branch BK100_%=\n"
"RT104_%=:\n"
"s_nop 15\n"
"s_waitcnt vmcnt(0) lgkmcnt(0)\n"
"SP106_%=:\n"
"s_sub_u32 s78, s78, 1\n"
"s_cmp_lt_i32 s78, 0\n"
"s_cbranch_scc1 SD107_%=\n"
"s_sleep 1\n"
"buffer_load_dwordx4 v[24:27], v2, s[32:35], s87 offen offset:2048 sc1\n"
"buffer_load_dwordx4 v[28:31], v2, s[32:35], s87 offen offset:3072 sc1\n"
"v_max3_u32 v0, v33, v35, v37\n"
"v_max_u32 v0, v39, v0\n"
"v_cmp_eq_u32 vcc, -1, v0\n"
"s_nop 0\n"
"s_cbranch_vccz L108_%=\n"
"buffer_load_dwordx4 v[32:35], v2, s[32:35], s88 offen offset:0 sc1\n"
"buffer_load_dwordx4 v[36:39], v2, s[32:35], s88 offen offset:1024 sc1\n"
"L108_%=:\n"
"s_waitcnt vmcnt(0) lgkmcnt(0)\n"
"v_max3_u32 v0, v25, v27, v29\n"
"v_max_u32 v0, v31, v0\n"
"v_cmp_eq_u32 vcc, -1, v0\n"
"s_waitcnt vmcnt(0) lgkmcnt(0)\n"
"s_cbranch_vccnz SP106_%=\n"
"SD107_%=:\n"
"s_nop 15\n"
"s_branch BK105_%=\n"
"RT109_%=:\n"
"s_nop 15\n"
"s_waitcnt vmcnt(0) lgkmcnt(0)\n"
"SP111_%=:\n"
"s_sub_u32 s78, s78, 1\n"
"s_cmp_lt_i32 s78, 0\n"
"s_cbranch_scc1 SD112_%=\n"
"s_sleep 1\n"
"buffer_load_dwordx4 v[32:35], v2, s[32:35], s88 offen offset:0 sc1\n"
"buffer_load_dwordx4 v[36:39], v2, s[32:35], s88 offen offset:1024 sc1\n"
"v_max3_u32 v0, v41, v43, v45\n"
"v_max_u32 v0, v47, v0\n"
"v_cmp_eq_u32 vcc, -1, v0\n"
"s_nop 0\n"
"s_cbranch_vccz L113_%=\n"
"buffer_load_dwordx4 v[40:43], v2, s[32:35], s88 offen offset:2048 sc1\n"
"buffer_load_dwordx4 v[44:47], v2, s[32:35], s88 offen offset:3072 sc1\n"
"L113_%=:\n"
"s_waitcnt vmcnt(0) lgkmcnt(0)\n"
"v_max3_u32 v0, v33, v35, v37\n"
"v_max_u32 v0, v39, v0\n"
"v_cmp_eq_u32 vcc, -1, v0\n"
"s_waitcnt vmcnt(0) lgkmcnt(0)\n"
"s_cbranch_vccnz SP111_%=\n"
"SD112_%=:\n"
"s_nop 15\n"
"s_branch BK110_%=\n"
"RT114_%=:\n"
"s_nop 15\n"
"s_waitcnt vmcnt(0) lgkmcnt(0)\n"
"SP116_%=:\n"
"s_sub_u32 s78, s78, 1\n"
"s_cmp_lt_i32 s78, 0\n"
"s_cbranch_scc1 SD117_%=\n"
"s_sleep 1\n"
"buffer_load_dwordx4 v[40:43], v2, s[32:35], s88 offen offset:2048 sc1\n"
"buffer_load_dwordx4 v[44:47], v2, s[32:35], s88 offen offset:3072 sc1\n"
"s_waitcnt vmcnt(0) lgkmcnt(0)\n"
"v_max3_u32 v0, v41, v43, v45\n"
"v_max_u32 v0, v47, v0\n"
"v_cmp_eq_u32 vcc, -1, v0\n"
"s_waitcnt vmcnt(0) lgkmcnt(0)\n"
"s_cbranch_vccnz SP116_%=\n"
"SD117_%=:\n"
"s_nop 15\n"
"s_branch BK115_%=\n"
"RT120_%=:\n"
"s_nop 15\n"
"s_waitcnt vmcnt(0) lgkmcnt(0)\n"
"SP122_%=:\n"
"s_sub_u32 s78, s78, 1\n"
"s_cmp_lt_i32 s78, 0\n"
"s_cbranch_scc1 SD123_%=\n"
"s_sleep 1\n"
"buffer_load_dwordx4 v[16:19], v2, s[32:35], s87 offen offset:0 sc1\n"
"buffer_load_dwordx4 v[20:23], v2, s[32:35], s87 offen offset:1024 sc1\n"
"v_max3_u32 v0, v25, v27, v29\n"
"v_max_u32 v0, v31, v0\n"
"v_cmp_eq_u32 vcc, -1, v0\n"
"s_nop 0\n"
"s_cbranch_vccz L124_%=\n"
"buffer_load_dwordx4 v[24:27], v2, s[32:35], s87 offen offset:2048 sc1\n"
"buffer_load_dwordx4 v[28:31], v2, s[32:35], s87 offen offset:3072 sc1\n"
"L124_%=:\n"
"s_waitcnt vmcnt(0) lgkmcnt(0)\n"
"v_max3_u32 v0, v17, v19, v21\n"
"v_max_u32 v0, v23, v0\n"
"v_cmp_eq_u32 vcc, -1, v0\n"
"s_waitcnt vmcnt(0) lgkmcnt(0)\n"
"s_cbranch_vccnz SP122_%=\n"
"SD123_%=:\n"
"s_nop 15\n"
"s_branch BK121_%=\n"
"RT125_%=:\n"
"s_nop 15\n"
"s_waitcnt vmcnt(0) lgkmcnt(0)\n"
"SP127_%=:\n"
"s_sub_u32 s78, s78, 1\n"
"s_cmp_lt_i32 s78, 0\n"
"s_cbranch_scc1 SD128_%=\n"
"s_sleep 1\n"
"buffer_load_dwordx4 v[24:27], v2, s[32:35], s87 offen offset:2048 sc1\n"
"buffer_load_dwordx4 v[28:31], v2, s[32:35], s87 offen offset:3072 sc1\n"
"v_max3_u32 v0, v33, v35, v37\n"
"v_max_u32 v0, v39, v0\n"
"v_cmp_eq_u32 vcc, -1, v0\n"
"s_nop 0\n"
"s_cbranch_vccz L129_%=\n"
"s_add_u32 s94, s87, 4096\n"
"buffer_load_dwordx4 v[32:35], v2, s[32:35], s94 offen offset:0 sc1\n"
"s_add_u32 s94, s87, 4096\n"
"buffer_load_dwordx4 v[36:39], v2, s[32:35], s94 offen offset:1024 sc1\n"
"L129_%=:\n"
"s_waitcnt vmcnt(0) lgkmcnt(0)\n"
"v_max3_u32 v0, v25, v27, v29\n"
"v_max_u32 v0, v31, v0\n"
"v_cmp_eq_u32 vcc, -1, v0\n"
"s_waitcnt vmcnt(0) lgkmcnt(0)\n"
"s_cbranch_vccnz SP127_%=\n"
"SD128_%=:\n"
"s_nop 15\n"
"s_branch BK126_%=\n"
"RT130_%=:\n"
"s_nop 15\n"
"s_waitcnt vmcnt(0) lgkmcnt(0)\n"
"SP132_%=:\n"
"s_sub_u32 s78, s78, 1\n"
"s_cmp_lt_i32 s78, 0\n"
"s_cbranch_scc1 SD133_%=\n"
"s_sleep 1\n"
"s_add_u32 s94, s87, 4096\n"
"buffer_load_dwordx4 v[32:35], v2, s[32:35], s94 offen offset:0 sc1\n"
"s_add_u32 s94, s87, 4096\n"
"buffer_load_dwordx4 v[36:39], v2, s[32:35], s94 offen offset:1024 sc1\n"
"s_waitcnt vmcnt(0) lgkmcnt(0)\n"
"v_max3_u32 v0, v33, v35, v37\n"
"v_max_u32 v0, v39, v0\n"
"v_cmp_eq_u32 vcc, -1, v0\n"
"s_waitcnt vmcnt(0) lgkmcnt(0)\n"
"s_cbranch_vccnz SP132_%=\n"
"SD133_%=:\n"
"s_nop 15\n"
"s_branch BK131_%=\n"
"RT134_%=:\n"
"s_nop 15\n"
"s_waitcnt vmcnt(0) lgkmcnt(0)\n"
"SP136_%=:\n"
"s_sub_u32 s78, s78, 1\n"
"s_cmp_lt_i32 s78, 0\n"
"s_cbranch_scc1 SD137_%=\n"
"s_sleep 1\n"
"s_add_u32 s94, s87, 4096\n"
"buffer_load_dwordx4 v[40:43], v2, s[32:35], s94 offen offset:2048 sc1\n"
"s_add_u32 s94, s87, 4096\n"
"buffer_load_dwordx4 v[44:47], v2, s[32:35], s94 offen offset:3072 sc1\n"
"v_max3_u32 v0, v49, v51, v53\n"
"v_max_u32 v0, v55, v0\n"
"v_cmp_eq_u32 vcc, -1, v0\n"
"s_nop 0\n"
"s_cbranch_vccz L138_%=\n"
"buffer_load_dwordx4 v[48:51], v2, s[32:35], s85 offen offset:0 sc1\n"
"buffer_load_dwordx4 v[52:55], v2, s[32:35], s85 offen offset:1024 sc1\n"
"L138_%=:\n"
"s_waitcnt vmcnt(0) lgkmcnt(0)\n"
"v_max3_u32 v0, v41, v43, v45\n"
"v_max_u32 v0, v47, v0\n"
"v_cmp_eq_u32 vcc, -1, v0\n"
"s_waitcnt vmcnt(0) lgkmcnt(0)\n"
"s_cbranch_vccnz SP136_%=\n"
"SD137_%=:\n"
"s_nop 15\n"
"s_branch BK135_%=\n"
"RT139_%=:\n"
"s_nop 15\n"
"s_waitcnt vmcnt(0) lgkmcnt(0)\n"
"SP141_%=:\n"
"s_sub_u32 s78, s78, 1\n"
"s_cmp_lt_i32 s78, 0\n"
"s_cbranch_scc1 SD142_%=\n"
"s_sleep 1\n"
"buffer_load_dwordx4 v[72:75], v2, s[32:35], s77 offen offset:0 sc1\n"
"buffer_load_dwordx4 v[76:79], v2, s[32:35], s77 offen offset:1024 sc1\n"
"s_waitcnt vmcnt(1)\n"
"v_max3_u32 v0, v72, v73, v74\n"
"s_waitcnt vmcnt(0)\n"
"v_max3_u32 v0, v75, v76, v0\n"
"v_max3_u32 v0, v77, v78, v0\n"
"v_max_u32 v0, v79, v0\n"
"v_cmp_eq_u32 vcc, -1, v0\n"
"s_waitcnt vmcnt(0) lgkmcnt(0)\n"
"s_cbranch_vccnz SP141_%=\n"
"SD142_%=:\n"
"s_nop 15\n"
"s_branch BK140_%=\n"
"RT143_%=:\n"
"s_nop 15\n"
"s_waitcnt vmcnt(0) lgkmcnt(0)\n"
"SP145_%=:\n"
"s_sub_u32 s78, s78, 1\n"
"s_cmp_lt_i32 s78, 0\n"
"s_cbranch_scc1 SD146_%=\n"
"s_sleep 1\n"
"buffer_load_dwordx4 v[48:51], v2, s[32:35], s85 offen offset:0 sc1\n"
"buffer_load_dwordx4 v[52:55], v2, s[32:35], s85 offen offset:1024 sc1\n"
"s_waitcnt vmcnt(0) lgkmcnt(0)\n"
"v_max3_u32 v0, v49, v51, v53\n"
"v_max_u32 v0, v55, v0\n"
"v_cmp_eq_u32 vcc, -1, v0\n"
"s_waitcnt vmcnt(0) lgkmcnt(0)\n"
"s_cbranch_vccnz SP145_%=\n"
"SD146_%=:\n"
"s_nop 15\n"
"s_branch BK144_%=\n"
"RT147_%=:\n"
"s_nop 15\n"
"s_waitcnt vmcnt(0) lgkmcnt(0)\n"
"SP149_%=:\n"
"s_sub_u32 s78, s78, 1\n"
"s_cmp_lt_i32 s78, 0\n"
"s_cbranch_scc1 SD150_%=\n"
"s_sleep 1\n"
"buffer_load_dwordx4 v[56:59], v2, s[32:35], s85 offen offset:2048 sc1\n"
"buffer_load_dwordx4 v[60:63], v2, s[32:35], s85 offen offset:3072 sc1\n"
"v_max3_u32 v0, v65, v67, v69\n"
"v_max_u32 v0, v71, v0\n"
"v_cmp_eq_u32 vcc, -1, v0\n"
"s_nop 0\n"
"s_cbranch_vccz L151_%=\n"
"s_add_u32 s94, s85, 4096\n"
"buffer_load_dwordx4 v[64:67], v2, s[32:35], s94 offen offset:0 sc1\n"
"s_add_u32 s94, s85, 4096\n"
"buffer_load_dwordx4 v[68:71], v2, s[32:35], s94 offen offset:1024 sc1\n"
"L151_%=:\n"
"v_max3_u32 v0, v73, v75, v77\n"
"v_max_u32 v0, v79, v0\n"
"v_cmp_eq_u32 vcc, -1, v0\n"
"s_nop 0\n"
"s_cbranch_vccz L152_%=\n"
"s_add_u32 s94, s85, 4096\n"
"buffer_load_dwordx4 v[72:75], v2, s[32:35], s94 offen offset:2048 sc1\n"
"s_add_u32 s94, s85, 4096\n"
"buffer_load_dwordx4 v[76:79], v2, s[32:35], s94 offen offset:3072 sc1\n"
"L152_%=:\n"
"s_waitcnt vmcnt(0) lgkmcnt(0)\n"
"v_max3_u32 v0, v57, v59, v61\n"
"v_max_u32 v0, v63, v0\n"
"v_cmp_eq_u32 vcc, -1, v0\n"
"s_waitcnt vmcnt(0) lgkmcnt(0)\n"
"s_cbranch_vccnz SP149_%=\n"
"SD150_%=:\n"
"s_nop 15\n"
"s_branch BK148_%=\n"
"RT153_%=:\n"
"s_nop 15\n"
"s_waitcnt vmcnt(0) lgkmcnt(0)\n"
"SP155_%=:\n"
"s_sub_u32 s78, s78, 1\n"
"s_cmp_lt_i32 s78, 0\n"
"s_cbranch_scc1 SD156_%=\n"
"s_sleep 1\n"
"s_add_u32 s94, s85, 4096\n"
"buffer_load_dwordx4 v[64:67], v2, s[32:35], s94 offen offset:0 sc1\n"
"s_add_u32 s94, s85, 4096\n"
"buffer_load_dwordx4 v[68:71], v2, s[32:35], s94 offen offset:1024 sc1\n"
"v_max3_u32 v0, v73, v75, v77\n"
"v_max_u32 v0, v79, v0\n"
"v_cmp_eq_u32 vcc, -1, v0\n"
"s_nop 0\n"
"s_cbranch_vccz L157_%=\n"
"s_add_u32 s94, s85, 4096\n"
"buffer_load_dwordx4 v[72:75], v2, s[32:35], s94 offen offset:2048 sc1\n"
"s_add_u32 s94, s85, 4096\n"
"buffer_load_dwordx4 v[76:79], v2, s[32:35], s94 offen offset:3072 sc1\n"
"L157_%=:\n"
"s_waitcnt vmcnt(0) lgkmcnt(0)\n"
"v_max3_u32 v0, v65, v67, v69\n"
"v_max_u32 v0, v71, v0\n"
"v_cmp_eq_u32 vcc, -1, v0\n"
"s_waitcnt vmcnt(0) lgkmcnt(0)\n"
"s_cbranch_vccnz SP155_%=\n"
"SD156_%=:\n"
"s_nop 15\n"
"s_branch BK154_%=\n"
"RT158_%=:\n"
"s_nop 15\n"
"s_waitcnt vmcnt(0) lgkmcnt(0)\n"
"SP160_%=:\n"
"s_sub_u32 s78, s78, 1\n"
"s_cmp_lt_i32 s78, 0\n"
"s_cbranch_scc1 SD161_%=\n"
"s_sleep 1\n"
"s_add_u32 s94, s85, 4096\n"
"buffer_load_dwordx4 v[72:75], v2, s[32:35], s94 offen offset:2048 sc1\n"
"s_add_u32 s94, s85, 4096\n"
"buffer_load_dwordx4 v[76:79], v2, s[32:35], s94 offen offset:3072 sc1\n"
"s_waitcnt vmcnt(0) lgkmcnt(0)\n"
"v_max3_u32 v0, v73, v75, v77\n"
"v_max_u32 v0, v79, v0\n"
"v_cmp_eq_u32 vcc, -1, v0\n"
"s_waitcnt vmcnt(0) lgkmcnt(0)\n"
"s_cbranch_vccnz SP160_%=\n"
"SD161_%=:\n"
"s_nop 15\n"
"s_branch BK159_%=\n"
"RT162_%=:\n"
"s_nop 15\n"
"s_waitcnt vmcnt(0) lgkmcnt(0)\n"
"SP164_%=:\n"
"s_sub_u32 s78, s78, 1\n"
"s_cmp_lt_i32 s78, 0\n"
"s_cbranch_scc1 SD165_%=\n"
"s_sleep 1\n"
"buffer_load_dwordx4 v[16:19], v2, s[32:35], s77 offen offset:0 sc1\n"
"buffer_load_dwordx4 v[20:23], v2, s[32:35], s77 offen offset:1024 sc1\n"
"s_waitcnt vmcnt(1)\n"
"v_max3_u32 v0, v16, v17, v18\n"
"s_waitcnt vmcnt(0)\n"
"v_max3_u32 v0, v19, v20, v0\n"
"v_max3_u32 v0, v21, v22, v0\n"
"v_max_u32 v0, v23, v0\n"
"v_cmp_eq_u32 vcc, -1, v0\n"
"s_waitcnt vmcnt(0) lgkmcnt(0)\n"
"s_cbranch_vccnz SP164_%=\n"
"SD165_%=:\n"
"s_nop 15\n"
"s_branch BK163_%=\n"
"RT170_%=:\n"
"s_nop 15\n"
"s_waitcnt vmcnt(0) lgkmcnt(0)\n"
"SP172_%=:\n"
"s_sub_u32 s78, s78, 1\n"
"s_cmp_lt_i32 s78, 0\n"
"s_cbranch_scc1 SD173_%=\n"
"s_sleep 1\n"
"buffer_load_dwordx4 v[16:19], v2, s[32:35], s87 offen offset:0 sc1\n"
"buffer_load_dwordx4 v[20:23], v2, s[32:35], s87 offen offset:1024 sc1\n"
"v_max3_u32 v0, v25, v27, v29\n"
"v_max_u32 v0, v31, v0\n"
"v_cmp_eq_u32 vcc, -1, v0\n"
"s_nop 0\n"
"s_cbranch_vccz L174_%=\n"
"buffer_load_dwordx4 v[24:27], v2, s[32:35], s87 offen offset:2048 sc1\n"
"buffer_load_dwordx4 v[28:31], v2, s[32:35], s87 offen offset:3072 sc1\n"
"L174_%=:\n"
"s_waitcnt vmcnt(0) lgkmcnt(0)\n"
"v_max3_u32 v0, v17, v19, v21\n"
"v_max_u32 v0, v23, v0\n"
"v_cmp_eq_u32 vcc, -1, v0\n"
"s_waitcnt vmcnt(0) lgkmcnt(0)\n"
"s_cbranch_vccnz SP172_%=\n"
"SD173_%=:\n"
"s_nop 15\n"
"s_branch BK171_%=\n"
"RT175_%=:\n"
"s_nop 15\n"
"s_waitcnt vmcnt(0) lgkmcnt(0)\n"
"SP177_%=:\n"
"s_sub_u32 s78, s78, 1\n"
"s_cmp_lt_i32 s78, 0\n"
"s_cbranch_scc1 SD178_%=\n"
"s_sleep 1\n"
"buffer_load_dwordx4 v[24:27], v2, s[32:35], s87 offen offset:2048 sc1\n"
"buffer_load_dwordx4 v[28:31], v2, s[32:35], s87 offen offset:3072 sc1\n"
"v_max3_u32 v0, v33, v35, v37\n"
"v_max_u32 v0, v39, v0\n"
"v_cmp_eq_u32 vcc, -1, v0\n"
"s_nop 0\n"
"s_cbranch_vccz L179_%=\n"
"s_add_u32 s94, s87, 4096\n"
"buffer_load_dwordx4 v[32:35], v2, s[32:35], s94 offen offset:0 sc1\n"
"s_add_u32 s94, s87, 4096\n"
"buffer_load_dwordx4 v[36:39], v2, s[32:35], s94 offen offset:1024 sc1\n"
"L179_%=:\n"
"s_waitcnt vmcnt(0) lgkmcnt(0)\n"
"v_max3_u32 v0, v25, v27, v29\n"
"v_max_u32 v0, v31, v0\n"
"v_cmp_eq_u32 vcc, -1, v0\n"
"s_waitcnt vmcnt(0) lgkmcnt(0)\n"
"s_cbranch_vccnz SP177_%=\n"
"SD178_%=:\n"
"s_nop 15\n"
"s_branch BK176_%=\n"
"RT180_%=:\n"
"s_nop 15\n"
"s_waitcnt vmcnt(0) lgkmcnt(0)\n"
"SP182_%=:\n"
"s_sub_u32 s78, s78, 1\n"
"s_cmp_lt_i32 s78, 0\n"
"s_cbranch_scc1 SD183_%=\n"
"s_sleep 1\n"
"s_add_u32 s94, s87, 4096\n"
"buffer_load_dwordx4 v[32:35], v2, s[32:35], s94 offen offset:0 sc1\n"
"s_add_u32 s94, s87, 4096\n"
"buffer_load_dwordx4 v[36:39], v2, s[32:35], s94 offen offset:1024 sc1\n"
"s_waitcnt vmcnt(0) lgkmcnt(0)\n"
"v_max3_u32 v0, v33, v35, v37\n"
"v_max_u32 v0, v39, v0\n"
"v_cmp_eq_u32 vcc, -1, v0\n"
"s_waitcnt vmcnt(0) lgkmcnt(0)\n"
"s_cbranch_vccnz SP182_%=\n"
"SD183_%=:\n"
"s_nop 15\n"
"s_branch BK181_%=\n"
"RT184_%=:\n"
"s_nop 15\n"
"s_waitcnt vmcnt(0) lgkmcnt(0)\n"
"SP186_%=:\n"
"s_sub_u32 s78, s78, 1\n"
"s_cmp_lt_i32 s78, 0\n"
"s_cbranch_scc1 SD187_%=\n"
"s_sleep 1\n"
"s_add_u32 s94, s87, 4096\n"
"buffer_load_dwordx4 v[40:43], v2, s[32:35], s94 offen offset:2048 sc1\n"
"s_add_u32 s94, s87, 4096\n"
"buffer_load_dwordx4 v[44:47], v2, s[32:35], s94 offen offset:3072 sc1\n"
"v_max3_u32 v0, v49, v51, v53\n"
"v_max_u32 v0, v55, v0\n"
"v_cmp_eq_u32 vcc, -1, v0\n"
"s_nop 0\n"
"s_cbranch_vccz L188_%=\n"
"buffer_load_dwordx4 v[48:51], v2, s[32:35], s85 offen offset:0 sc1\n"
"buffer_load_dwordx4 v[52:55], v2, s[32:35], s85 offen offset:1024 sc1\n"
"L188_%=:\n"
"s_waitcnt vmcnt(0) lgkmcnt(0)\n"
"v_max3_u32 v0, v41, v43, v45\n"
"v_max_u32 v0, v47, v0\n"
"v_cmp_eq_u32 vcc, -1, v0\n"
"s_waitcnt vmcnt(0) lgkmcnt(0)\n"
"s_cbranch_vccnz SP186_%=\n"
"SD187_%=:\n"
"s_nop 15\n"
"s_branch BK185_%=\n"
"RT189_%=:\n"
"s_nop 15\n"
"s_waitcnt vmcnt(0) lgkmcnt(0)\n"
"SP191_%=:\n"
"s_sub_u32 s78, s78, 1\n"
"s_cmp_lt_i32 s78, 0\n"
"s_cbranch_scc1 SD192_%=\n"
"s_sleep 1\n"
"buffer_load_dwordx4 v[72:75], v2, s[32:35], s77 offen offset:0 sc1\n"
"buffer_load_dwordx4 v[76:79], v2, s[32:35], s77 offen offset:1024 sc1\n"
"s_waitcnt vmcnt(1)\n"
"v_max3_u32 v0, v72, v73, v74\n"
"s_waitcnt vmcnt(0)\n"
"v_max3_u32 v0, v75, v76, v0\n"
"v_max3_u32 v0, v77, v78, v0\n"
"v_max_u32 v0, v79, v0\n"
"v_cmp_eq_u32 vcc, -1, v0\n"
"s_waitcnt vmcnt(0) lgkmcnt(0)\n"
"s_cbranch_vccnz SP191_%=\n"
"SD192_%=:\n"
"s_nop 15\n"
"s_branch BK190_%=\n"
"RT193_%=:\n"
"s_nop 15\n"
"s_waitcnt vmcnt(0) lgkmcnt(0)\n"
"SP195_%=:\n"
"s_sub_u32 s78, s78, 1\n"
"s_cmp_lt_i32 s78, 0\n"
"s_cbranch_scc1 SD196_%=\n"
"s_sleep 1\n"
"buffer_load_dwordx4 v[48:51], v2, s[32:35], s85 offen offset:0 sc1\n"
"buffer_load_dwordx4 v[52:55], v2, s[32:35], s85 offen offset:1024 sc1\n"
"s_waitcnt vmcnt(0) lgkmcnt(0)\n"
"v_max3_u32 v0, v49, v51, v53\n"
"v_max_u32 v0, v55, v0\n"
"v_cmp_eq_u32 vcc, -1, v0\n"
"s_waitcnt vmcnt(0) lgkmcnt(0)\n"
"s_cbranch_vccnz SP195_%=\n"
"SD196_%=:\n"
"s_nop 15\n"
"s_branch BK194_%=\n"
"RT197_%=:\n"
"s_nop 15\n"
"s_waitcnt vmcnt(0) lgkmcnt(0)\n"
"SP199_%=:\n"
"s_sub_u32 s78, s78, 1\n"
"s_cmp_lt_i32 s78, 0\n"
"s_cbranch_scc1 SD200_%=\n"
"s_sleep 1\n"
"buffer_load_dwordx4 v[56:59], v2, s[32:35], s85 offen offset:2048 sc1\n"
"buffer_load_dwordx4 v[60:63], v2, s[32:35], s85 offen offset:3072 sc1\n"
"v_max3_u32 v0, v65, v67, v69\n"
"v_max_u32 v0, v71, v0\n"
"v_cmp_eq_u32 vcc, -1, v0\n"
"s_nop 0\n"
"s_cbranch_vccz L201_%=\n"
"s_add_u32 s94, s85, 4096\n"
"buffer_load_dwordx4 v[64:67], v2, s[32:35], s94 offen offset:0 sc1\n"
"s_add_u32 s94, s85, 4096\n"
"buffer_load_dwordx4 v[68:71], v2, s[32:35], s94 offen offset:1024 sc1\n"
"L201_%=:\n"
"v_max3_u32 v0, v73, v75, v77\n"
"v_max_u32 v0, v79, v0\n"
"v_cmp_eq_u32 vcc, -1, v0\n"
"s_nop 0\n"
"s_cbranch_vccz L202_%=\n"
"s_add_u32 s94, s85, 4096\n"
"buffer_load_dwordx4 v[72:75], v2, s[32:35], s94 offen offset:2048 sc1\n"
"s_add_u32 s94, s85, 4096\n"
"buffer_load_dwordx4 v[76:79], v2, s[32:35], s94 offen offset:3072 sc1\n"
"L202_%=:\n"
"s_waitcnt vmcnt(0) lgkmcnt(0)\n"
"v_max3_u32 v0, v57, v59, v61\n"
"v_max_u32 v0, v63, v0\n"
"v_cmp_eq_u32 vcc, -1, v0\n"
"s_waitcnt vmcnt(0) lgkmcnt(0)\n"
"s_cbranch_vccnz SP199_%=\n"
"SD200_%=:\n"
"s_nop 15\n"
"s_branch BK198_%=\n"
"RT203_%=:\n"
"s_nop 15\n"
"s_waitcnt vmcnt(0) lgkmcnt(0)\n"
"SP205_%=:\n"
"s_sub_u32 s78, s78, 1\n"
"s_cmp_lt_i32 s78, 0\n"
"s_cbranch_scc1 SD206_%=\n"
"s_sleep 1\n"
"s_add_u32 s94, s85, 4096\n"
"buffer_load_dwordx4 v[64:67], v2, s[32:35], s94 offen offset:0 sc1\n"
"s_add_u32 s94, s85, 4096\n"
"buffer_load_dwordx4 v[68:71], v2, s[32:35], s94 offen offset:1024 sc1\n"
"v_max3_u32 v0, v73, v75, v77\n"
"v_max_u32 v0, v79, v0\n"
"v_cmp_eq_u32 vcc, -1, v0\n"
"s_nop 0\n"
"s_cbranch_vccz L207_%=\n"
"s_add_u32 s94, s85, 4096\n"
"buffer_load_dwordx4 v[72:75], v2, s[32:35], s94 offen offset:2048 sc1\n"
"s_add_u32 s94, s85, 4096\n"
"buffer_load_dwordx4 v[76:79], v2, s[32:35], s94 offen offset:3072 sc1\n"
"L207_%=:\n"
"s_waitcnt vmcnt(0) lgkmcnt(0)\n"
"v_max3_u32 v0, v65, v67, v69\n"
"v_max_u32 v0, v71, v0\n"
"v_cmp_eq_u32 vcc, -1, v0\n"
"s_waitcnt vmcnt(0) lgkmcnt(0)\n"
"s_cbranch_vccnz SP205_%=\n"
"SD206_%=:\n"
"s_nop 15\n"
"s_branch BK204_%=\n"
"RT208_%=:\n"
"s_nop 15\n"
"s_waitcnt vmcnt(0) lgkmcnt(0)\n"
"SP210_%=:\n"
"s_sub_u32 s78, s78, 1\n"
"s_cmp_lt_i32 s78, 0\n"
"s_cbranch_scc1 SD211_%=\n"
"s_sleep 1\n"
"s_add_u32 s94, s85, 4096\n"
"buffer_load_dwordx4 v[72:75], v2, s[32:35], s94 offen offset:2048 sc1\n"
"s_add_u32 s94, s85, 4096\n"
"buffer_load_dwordx4 v[76:79], v2, s[32:35], s94 offen offset:3072 sc1\n"
"s_waitcnt vmcnt(0) lgkmcnt(0)\n"
"v_max3_u32 v0, v73, v75, v77\n"
"v_max_u32 v0, v79, v0\n"
"v_cmp_eq_u32 vcc, -1, v0\n"
"s_waitcnt vmcnt(0) lgkmcnt(0)\n"
"s_cbranch_vccnz SP210_%=\n"
"SD211_%=:\n"
"s_nop 15\n"
"s_branch BK209_%=\n"
"RT212_%=:\n"
"s_nop 15\n"
"s_waitcnt vmcnt(0) lgkmcnt(0)\n"
"SP214_%=:\n"
"s_sub_u32 s78, s78, 1\n"
"s_cmp_lt_i32 s78, 0\n"
"s_cbranch_scc1 SD215_%=\n"
"s_sleep 1\n"
"buffer_load_dwordx4 v[16:19], v2, s[32:35], s77 offen offset:0 sc1\n"
"buffer_load_dwordx4 v[20:23], v2, s[32:35], s77 offen offset:1024 sc1\n"
"s_waitcnt vmcnt(1)\n"
"v_max3_u32 v0, v16, v17, v18\n"
"s_waitcnt vmcnt(0)\n"
"v_max3_u32 v0, v19, v20, v0\n"
"v_max3_u32 v0, v21, v22, v0\n"
"v_max_u32 v0, v23, v0\n"
"v_cmp_eq_u32 vcc, -1, v0\n"
"s_waitcnt vmcnt(0) lgkmcnt(0)\n"
"s_cbranch_vccnz SP214_%=\n"
"SD215_%=:\n"
"s_nop 15\n"
"s_branch BK213_%=\n"
"RT221_%=:\n"
"s_nop 15\n"
"s_waitcnt vmcnt(0) lgkmcnt(0)\n"
"SP223_%=:\n"
"s_sub_u32 s78, s78, 1\n"
"s_cmp_lt_i32 s78, 0\n"
"s_cbranch_scc1 SD224_%=\n"
"s_sleep 1\n"
"s_lshr_b32 s87, s75, 3\n"
"s_xor_b32 s87, s87, 0\n"
"s_mul_i32 s87, s87, 114688\n"
"s_add_u32 s87, s87, s85\n"
"s_add_u32 s87, s87, s68\n"
"s_sub_u32 s87, s87, s67\n"
"buffer_load_dwordx4 v[16:19], v2, s[32:35], s87 offen offset:0 sc1\n"
"buffer_load_dwordx4 v[20:23], v2, s[32:35], s87 offen offset:1024 sc1\n"
"buffer_load_dwordx4 v[24:27], v2, s[32:35], s87 offen offset:2048 sc1\n"
"buffer_load_dwordx4 v[28:31], v2, s[32:35], s87 offen offset:3072 sc1\n"
"s_add_u32 s94, s87, 4096\n"
"buffer_load_dwordx4 v[32:35], v2, s[32:35], s94 offen offset:0 sc1\n"
"s_add_u32 s94, s87, 4096\n"
"buffer_load_dwordx4 v[36:39], v2, s[32:35], s94 offen offset:1024 sc1\n"
"s_add_u32 s94, s87, 4096\n"
"buffer_load_dwordx4 v[40:43], v2, s[32:35], s94 offen offset:2048 sc1\n"
"s_waitcnt vmcnt(5)\n"
"v_max3_u32 v0, v17, v19, v21\n"
"s_waitcnt vmcnt(4)\n"
"v_max3_u32 v0, v23, v25, v0\n"
"s_waitcnt vmcnt(3)\n"
"v_max3_u32 v0, v27, v29, v0\n"
"s_waitcnt vmcnt(2)\n"
"v_max3_u32 v0, v31, v33, v0\n"
"s_waitcnt vmcnt(1)\n"
"v_max3_u32 v0, v35, v37, v0\n"
"s_waitcnt vmcnt(0)\n"
"v_max3_u32 v0, v39, v41, v0\n"
"v_max_u32 v0, v43, v0\n"
"v_cmp_eq_u32 vcc, -1, v0\n"
"s_waitcnt vmcnt(0) lgkmcnt(0)\n"
"s_cbranch_vccnz SP223_%=\n"
"SD224_%=:\n"
"s_nop 15\n"
"s_branch BK222_%=\n"
"RT226_%=:\n"
"s_nop 15\n"
"s_waitcnt vmcnt(0) lgkmcnt(0)\n"
"SP228_%=:\n"
"s_sub_u32 s78, s78, 1\n"
"s_cmp_lt_i32 s78, 0\n"
"s_cbranch_scc1 SD229_%=\n"
"s_sleep 1\n"
"s_lshr_b32 s87, s75, 3\n"
"s_xor_b32 s87, s87, 0\n"
"s_mul_i32 s87, s87, 114688\n"
"s_add_u32 s87, s87, s85\n"
"s_add_u32 s87, s87, s68\n"
"s_sub_u32 s87, s87, s67\n"
"s_add_u32 s94, s87, 4096\n"
"buffer_load_dwordx4 v[44:47], v2, s[32:35], s94 offen offset:3072 sc1\n"
"s_add_u32 s94, s87, 8192\n"
"buffer_load_dwordx4 v[48:51], v2, s[32:35], s94 offen offset:0 sc1\n"
"s_add_u32 s94, s87, 8192\n"
"buffer_load_dwordx4 v[52:55], v2, s[32:35], s94 offen offset:1024 sc1\n"
"s_add_u32 s94, s87, 8192\n"
"buffer_load_dwordx4 v[56:59], v2, s[32:35], s94 offen offset:2048 sc1\n"
"s_add_u32 s94, s87, 8192\n"
"buffer_load_dwordx4 v[60:63], v2, s[32:35], s94 offen offset:3072 sc1\n"
"s_add_u32 s94, s87, 12288\n"
"buffer_load_dwordx4 v[16:19], v2, s[32:35], s94 offen offset:0 sc1\n"
"s_add_u32 s94, s87, 12288\n"
"buffer_load_dwordx4 v[20:23], v2, s[32:35], s94 offen offset:1024 sc1\n"
"s_waitcnt vmcnt(5)\n"
"v_max3_u32 v0, v45, v47, v49\n"
"s_waitcnt vmcnt(4)\n"
"v_max3_u32 v0, v51, v53, v0\n"
"s_waitcnt vmcnt(3)\n"
"v_max3_u32 v0, v55, v57, v0\n"
"s_waitcnt vmcnt(2)\n"
"v_max3_u32 v0, v59, v61, v0\n"
"s_waitcnt vmcnt(1)\n"
"v_max3_u32 v0, v63, v17, v0\n"
"s_waitcnt vmcnt(0)\n"
"v_max3_u32 v0, v19, v21, v0\n"
"v_max_u32 v0, v23, v0\n"
"v_cmp_eq_u32 vcc, -1, v0\n"
"s_waitcnt vmcnt(0) lgkmcnt(0)\n"
"s_cbranch_vccnz SP228_%=\n"
"SD229_%=:\n"
"s_nop 15\n"
"s_branch BK227_%=\n"
"RT230_%=:\n"
"s_nop 15\n"
"s_waitcnt vmcnt(0) lgkmcnt(0)\n"
"SP232_%=:\n"
"s_sub_u32 s78, s78, 1\n"
"s_cmp_lt_i32 s78, 0\n"
"s_cbranch_scc1 SD233_%=\n"
"s_sleep 1\n"
"s_lshr_b32 s94, s75, 3\n"
"s_xor_b32 s94, s94, 1\n"
"s_lshl_b32 s94, s94, 10\n"
"s_add_u32 s94, s94, s69\n"
"s_mov_b64 exec, 3\n"
"buffer_load_dwordx2 v[8:9], v11, s[32:35], s94 offen sc1\n"
"s_mov_b64 exec, -1\n"
"s_waitcnt vmcnt(0)\n"
"v_cmp_ne_u64 vcc, s[72:73], v[8:9]\n"
"s_waitcnt vmcnt(0) lgkmcnt(0)\n"
"s_cbranch_vccnz SP232_%=\n"
"SD233_%=:\n"
"s_nop 15\n"
"s_branch BK231_%=\n"
"RT235_%=:\n"
"s_nop 15\n"
"s_waitcnt vmcnt(0) lgkmcnt(0)\n"
"SP237_%=:\n"
"s_sub_u32 s78, s78, 1\n"
"s_cmp_lt_i32 s78, 0\n"
"s_cbranch_scc1 SD238_%=\n"
"s_sleep 1\n"
"s_lshr_b32 s87, s75, 3\n"
"s_xor_b32 s87, s87, 0\n"
"s_mul_i32 s87, s87, 114688\n"
"s_add_u32 s87, s87, s85\n"
"s_add_u32 s87, s87, s68\n"
"s_sub_u32 s87, s87, s67\n"
"s_add_u32 s94, s87, 12288\n"
"buffer_load_dwordx4 v[24:27], v2, s[32:35], s94 offen offset:2048 sc1\n"
"s_add_u32 s94, s87, 12288\n"
"buffer_load_dwordx4 v[28:31], v2, s[32:35], s94 offen offset:3072 sc1\n"
"s_add_u32 s94, s87, 16384\n"
"buffer_load_dwordx4 v[32:35], v2, s[32:35], s94 offen offset:0 sc1\n"
"s_add_u32 s94, s87, 16384\n"
"buffer_load_dwordx4 v[36:39], v2, s[32:35], s94 offen offset:1024 sc1\n"
"s_add_u32 s94, s87, 16384\n"
"buffer_load_dwordx4 v[40:43], v2, s[32:35], s94 offen offset:2048 sc1\n"
"s_add_u32 s94, s87, 16384\n"
"buffer_load_dwordx4 v[44:47], v2, s[32:35], s94 offen offset:3072 sc1\n"
"s_add_u32 s94, s87, 20480\n"
"buffer_load_dwordx4 v[48:51], v2, s[32:35], s94 offen offset:0 sc1\n"
"s_waitcnt vmcnt(5)\n"
"v_max3_u32 v0, v25, v27, v29\n"
"s_waitcnt vmcnt(4)\n"
"v_max3_u32 v0, v31, v33, v0\n"
"s_waitcnt vmcnt(3)\n"
"v_max3_u32 v0, v35, v37, v0\n"
"s_waitcnt vmcnt(2)\n"
"v_max3_u32 v0, v39, v41, v0\n"
"s_waitcnt vmcnt(1)\n"
"v_max3_u32 v0, v43, v45, v0\n"
"s_waitcnt vmcnt(0)\n"
"v_max3_u32 v0, v47, v49, v0\n"
"v_max_u32 v0, v51, v0\n"
"v_cmp_eq_u32 vcc, -1, v0\n"
"s_waitcnt vmcnt(0) lgkmcnt(0)\n"
"s_cbranch_vccnz SP237_%=\n"
"SD238_%=:\n"
"s_nop 15\n"
"s_branch BK236_%=\n"
"RT240_%=:\n"
"s_nop 15\n"
"s_waitcnt vmcnt(0) lgkmcnt(0)\n"
"SP242_%=:\n"
"s_sub_u32 s78, s78, 1\n"
"s_cmp_lt_i32 s78, 0\n"
"s_cbranch_scc1 SD243_%=\n"
"s_sleep 1\n"
"s_lshr_b32 s87, s75, 3\n"
"s_xor_b32 s87, s87, 0\n"
"s_mul_i32 s87, s87, 114688\n"
"s_add_u32 s87, s87, s85\n"
"s_add_u32 s87, s87, s68\n"
"s_sub_u32 s87, s87, s67\n"
"s_add_u32 s94, s87, 20480\n"
"buffer_load_dwordx4 v[52:55], v2, s[32:35], s94 offen offset:1024 sc1\n"
"s_add_u32 s94, s87, 20480\n"
"buffer_load_dwordx4 v[56:59], v2, s[32:35], s94 offen offset:2048 sc1\n"
"s_add_u32 s94, s87, 20480\n"
"buffer_load_dwordx4 v[60:63], v2, s[32:35], s94 offen offset:3072 sc1\n"
"s_add_u32 s94, s87, 24576\n"
"buffer_load_dwordx4 v[16:19], v2, s[32:35], s94 offen offset:0 sc1\n"
"s_add_u32 s94, s87, 24576\n"
"buffer_load_dwordx4 v[20:23], v2, s[32:35], s94 offen offset:1024 sc1\n"
"s_add_u32 s94, s87, 24576\n"
"buffer_load_dwordx4 v[24:27], v2, s[32:35], s94 offen offset:2048 sc1\n"
"s_add_u32 s94, s87, 24576\n"
"buffer_load_dwordx4 v[28:31], v2, s[32:35], s94 offen offset:3072 sc1\n"
"s_waitcnt vmcnt(5)\n"
"v_max3_u32 v0, v53, v55, v57\n"
"s_waitcnt vmcnt(4)\n"
"v_max3_u32 v0, v59, v61, v0\n"
"s_waitcnt vmcnt(3)\n"
"v_max3_u32 v0, v63, v17, v0\n"
"s_waitcnt vmcnt(2)\n"
"v_max3_u32 v0, v19, v21, v0\n"
"s_waitcnt vmcnt(1)\n"
"v_max3_u32 v0, v23, v25, v0\n"
"s_waitcnt vmcnt(0)\n"
"v_max3_u32 v0, v27, v29, v0\n"
"v_max_u32 v0, v31, v0\n"
"v_cmp_eq_u32 vcc, -1, v0\n"
"s_waitcnt vmcnt(0) lgkmcnt(0)\n"
"s_cbranch_vccnz SP242_%=\n"
"SD243_%=:\n"
"s_nop 15\n"
"s_branch BK241_%=\n"
"RT245_%=:\n"
"s_nop 15\n"
"s_waitcnt vmcnt(0) lgkmcnt(0)\n"
"SP247_%=:\n"
"s_sub_u32 s78, s78, 1\n"
"s_cmp_lt_i32 s78, 0\n"
"s_cbranch_scc1 SD248_%=\n"
"s_sleep 1\n"
"s_lshr_b32 s87, s75, 3\n"
"s_xor_b32 s87, s87, 1\n"
"s_mul_i32 s87, s87, 114688\n"
"s_add_u32 s87, s87, s85\n"
"s_add_u32 s87, s87, s68\n"
"s_sub_u32 s87, s87, s67\n"
"buffer_load_dwordx4 v[32:35], v2, s[32:35], s87 offen offset:0 sc1\n"
"buffer_load_dwordx4 v[36:39], v2, s[32:35], s87 offen offset:1024 sc1\n"
"buffer_load_dwordx4 v[40:43], v2, s[32:35], s87 offen offset:2048 sc1\n"
"buffer_load_dwordx4 v[44:47], v2, s[32:35], s87 offen offset:3072 sc1\n"
"s_add_u32 s94, s87, 4096\n"
"buffer_load_dwordx4 v[48:51], v2, s[32:35], s94 offen offset:0 sc1\n"
"s_add_u32 s94, s87, 4096\n"
"buffer_load_dwordx4 v[52:55], v2, s[32:35], s94 offen offset:1024 sc1\n"
"s_add_u32 s94, s87, 4096\n"
"buffer_load_dwordx4 v[56:59], v2, s[32:35], s94 offen offset:2048 sc1\n"
"s_waitcnt vmcnt(5)\n"
"v_max3_u32 v0, v33, v35, v37\n"
"s_waitcnt vmcnt(4)\n"
"v_max3_u32 v0, v39, v41, v0\n"
"s_waitcnt vmcnt(3)\n"
"v_max3_u32 v0, v43, v45, v0\n"
"s_waitcnt vmcnt(2)\n"
"v_max3_u32 v0, v47, v49, v0\n"
"s_waitcnt vmcnt(1)\n"
"v_max3_u32 v0, v51, v53, v0\n"
"s_waitcnt vmcnt(0)\n"
"v_max3_u32 v0, v55, v57, v0\n"
"v_max_u32 v0, v59, v0\n"
"v_cmp_eq_u32 vcc, -1, v0\n"
"s_waitcnt vmcnt(0) lgkmcnt(0)\n"
"s_cbranch_vccnz SP247_%=\n"
"SD248_%=:\n"
"s_nop 15\n"
"s_branch BK246_%=\n"
"RT249_%=:\n"
"s_nop 15\n"
"s_waitcnt vmcnt(0) lgkmcnt(0)\n"
"SP251_%=:\n"
"s_sub_u32 s78, s78, 1\n"
"s_cmp_lt_i32 s78, 0\n"
"s_cbranch_scc1 SD252_%=\n"
"s_sleep 1\n"
"s_lshr_b32 s94, s75, 3\n"
"s_xor_b32 s94, s94, 2\n"
"s_lshl_b32 s94, s94, 10\n"
"s_add_u32 s94, s94, s69\n"
"s_mov_b64 exec, 3\n"
"buffer_load_dwordx2 v[8:9], v11, s[32:35], s94 offen sc1\n"
"s_mov_b64 exec, -1\n"
"s_waitcnt vmcnt(0)\n"
"v_cmp_ne_u64 vcc, s[72:73], v[8:9]\n"
"s_waitcnt vmcnt(0) lgkmcnt(0)\n"
"s_cbranch_vccnz SP251_%=\n"
"SD252_%=:\n"
"s_nop 15\n"
"s_branch BK250_%=\n"
"RT257_%=:\n"
"s_nop 15\n"
"s_waitcnt vmcnt(0) lgkmcnt(0)\n"
"SP259_%=:\n"
"s_sub_u32 s78, s78, 1\n"
"s_cmp_lt_i32 s78, 0\n"
"s_cbranch_scc1 SD260_%=\n"
"s_sleep 1\n"
"s_lshr_b32 s94, s75, 3\n"
"s_xor_b32 s94, s94, 3\n"
"s_lshl_b32 s94, s94, 10\n"
"s_add_u32 s94, s94, s69\n"
"s_mov_b64 exec, 3\n"
"buffer_load_dwordx2 v[8:9], v11, s[32:35], s94 offen sc1\n"
"s_mov_b64 exec, -1\n"
"s_waitcnt vmcnt(0)\n"
"v_cmp_ne_u64 vcc, s[72:73], v[8:9]\n"
"s_waitcnt vmcnt(0) lgkmcnt(0)\n"
"s_cbranch_vccnz SP259_%=\n"
"SD260_%=:\n"
"s_nop 15\n"
"s_branch BK258_%=\n"
"RT272_%=:\n"
"s_nop 15\n"
"s_waitcnt vmcnt(0) lgkmcnt(0)\n"
"SP274_%=:\n"
"s_sub_u32 s78, s78, 1\n"
"s_cmp_lt_i32 s78, 0\n"
"s_cbranch_scc1 SD275_%=\n"
"s_sleep 1\n"
"buffer_load_dwordx4 v[16:19], v2, s[32:35], s87 offen offset:0 sc1\n"
"buffer_load_dwordx4 v[20:23], v2, s[32:35], s87 offen offset:1024 sc1\n"
"v_max3_u32 v0, v25, v27, v29\n"
"v_max_u32 v0, v31, v0\n"
"v_cmp_eq_u32 vcc, -1, v0\n"
"s_nop 0\n"
"s_cbranch_vccz L276_%=\n"
"buffer_load_dwordx4 v[24:27], v2, s[32:35], s87 offen offset:2048 sc1\n"
"buffer_load_dwordx4 v[28:31], v2, s[32:35], s87 offen offset:3072 sc1\n"
"L276_%=:\n"
"s_waitcnt vmcnt(0) lgkmcnt(0)\n"
"v_max3_u32 v0, v17, v19, v21\n"
"v_max_u32 v0, v23, v0\n"
"v_cmp_eq_u32 vcc, -1, v0\n"
"s_waitcnt vmcnt(0) lgkmcnt(0)\n"
"s_cbranch_vccnz SP274_%=\n"
"SD275_%=:\n"
"s_nop 15\n"
"s_branch BK273_%=\n"
"RT277_%=:\n"
"s_nop 15\n"
"s_waitcnt vmcnt(0) lgkmcnt(0)\n"
"SP279_%=:\n"
"s_sub_u32 s78, s78, 1\n"
"s_cmp_lt_i32 s78, 0\n"
"s_cbranch_scc1 SD280_%=\n"
"s_sleep 1\n"
"buffer_load_dwordx4 v[24:27], v2, s[32:35], s87 offen offset:2048 sc1\n"
"buffer_load_dwordx4 v[28:31], v2, s[32:35], s87 offen offset:3072 sc1\n"
"v_max3_u32 v0, v33, v35, v37\n"
"v_max_u32 v0, v39, v0\n"
"v_cmp_eq_u32 vcc, -1, v0\n"
"s_nop 0\n"
"s_cbranch_vccz L281_%=\n"
"s_add_u32 s94, s87, 4096\n"
"buffer_load_dwordx4 v[32:35], v2, s[32:35], s94 offen offset:0 sc1\n"
"s_add_u32 s94, s87, 4096\n"
"buffer_load_dwordx4 v[36:39], v2, s[32:35], s94 offen offset:1024 sc1\n"
"L281_%=:\n"
"v_max3_u32 v0, v41, v43, v45\n"
"v_max_u32 v0, v47, v0\n"
"v_cmp_eq_u32 vcc, -1, v0\n"
"s_nop 0\n"
"s_cbranch_vccz L282_%=\n"
"s_add_u32 s94, s87, 4096\n"
"buffer_load_dwordx4 v[40:43], v2, s[32:35], s94 offen offset:2048 sc1\n"
"buffer_load_dwordx4 v[44:47], v2, s[32:35], s85 offen offset:0 sc1\n"
"L282_%=:\n"
"s_waitcnt vmcnt(0) lgkmcnt(0)\n"
"v_max3_u32 v0, v25, v27, v29\n"
"v_max_u32 v0, v31, v0\n"
"v_cmp_eq_u32 vcc, -1, v0\n"
"s_waitcnt vmcnt(0) lgkmcnt(0)\n"
"s_cbranch_vccnz SP279_%=\n"
"SD280_%=:\n"
"s_nop 15\n"
"s_branch BK278_%=\n"
"RT283_%=:\n"
"s_nop 15\n"
"s_waitcnt vmcnt(0) lgkmcnt(0)\n"
"SP285_%=:\n"
"s_sub_u32 s78, s78, 1\n"
"s_cmp_lt_i32 s78, 0\n"
"s_cbranch_scc1 SD286_%=\n"
"s_sleep 1\n"
"s_add_u32 s94, s87, 4096\n"
"buffer_load_dwordx4 v[32:35], v2, s[32:35], s94 offen offset:0 sc1\n"
"s_add_u32 s94, s87, 4096\n"
"buffer_load_dwordx4 v[36:39], v2, s[32:35], s94 offen offset:1024 sc1\n"
"v_max3_u32 v0, v41, v43, v45\n"
"v_max_u32 v0, v47, v0\n"
"v_cmp_eq_u32 vcc, -1, v0\n"
"s_nop 0\n"
"s_cbranch_vccz L287_%=\n"
"s_add_u32 s94, s87, 4096\n"
"buffer_load_dwordx4 v[40:43], v2, s[32:35], s94 offen offset:2048 sc1\n"
"buffer_load_dwordx4 v[44:47], v2, s[32:35], s85 offen offset:0 sc1\n"
"L287_%=:\n"
"v_max3_u32 v0, v49, v51, v53\n"
"v_max_u32 v0, v55, v0\n"
"v_cmp_eq_u32 vcc, -1, v0\n"
"s_nop 0\n"
"s_cbranch_vccz L288_%=\n"
"buffer_load_dwordx4 v[48:51], v2, s[32:35], s85 offen offset:1024 sc1\n"
"buffer_load_dwordx4 v[52:55], v2, s[32:35], s85 offen offset:2048 sc1\n"
"L288_%=:\n"
"s_waitcnt vmcnt(0) lgkmcnt(0)\n"
"v_max3_u32 v0, v33, v35, v37\n"
"v_max_u32 v0, v39, v0\n"
"v_cmp_eq_u32 vcc, -1, v0\n"
"s_waitcnt vmcnt(0) lgkmcnt(0)\n"
"s_cbranch_vccnz SP285_%=\n"
"SD286_%=:\n"
"s_nop 15\n"
"s_branch BK284_%=\n"
"RT289_%=:\n"
"s_nop 15\n"
"s_waitcnt vmcnt(0) lgkmcnt(0)\n"
"SP291_%=:\n"
"s_sub_u32 s78, s78, 1\n"
"s_cmp_lt_i32 s78, 0\n"
"s_cbranch_scc1 SD292_%=\n"
"s_sleep 1\n"
"s_add_u32 s94, s87, 4096\n"
"buffer_load_dwordx4 v[40:43], v2, s[32:35], s94 offen offset:2048 sc1\n"
"buffer_load_dwordx4 v[44:47], v2, s[32:35], s85 offen offset:0 sc1\n"
"v_max3_u32 v0, v49, v51, v53\n"
"v_max_u32 v0, v55, v0\n"
"v_cmp_eq_u32 vcc, -1, v0\n"
"s_nop 0\n"
"s_cbranch_vccz L293_%=\n"
"buffer_load_dwordx4 v[48:51], v2, s[32:35], s85 offen offset:1024 sc1\n"
"buffer_load_dwordx4 v[52:55], v2, s[32:35], s85 offen offset:2048 sc1\n"
"L293_%=:\n"
"v_max3_u32 v0, v57, v59, v61\n"
"v_max_u32 v0, v63, v0\n"
"v_cmp_eq_u32 vcc, -1, v0\n"
"s_nop 0\n"
"s_cbranch_vccz L294_%=\n"
"buffer_load_dwordx4 v[56:59], v2, s[32:35], s85 offen offset:3072 sc1\n"
"s_add_u32 s94, s85, 4096\n"
"buffer_load_dwordx4 v[60:63], v2, s[32:35], s94 offen offset:0 sc1\n"
"L294_%=:\n"
"s_waitcnt vmcnt(0) lgkmcnt(0)\n"
"v_max3_u32 v0, v41, v43, v45\n"
"v_max_u32 v0, v47, v0\n"
"v_cmp_eq_u32 vcc, -1, v0\n"
"s_waitcnt vmcnt(0) lgkmcnt(0)\n"
"s_cbranch_vccnz SP291_%=\n"
"SD292_%=:\n"
"s_nop 15\n"
"s_branch BK290_%=\n"
"RT295_%=:\n"
"s_nop 15\n"
"s_waitcnt vmcnt(0) lgkmcnt(0)\n"
"SP297_%=:\n"
"s_sub_u32 s78, s78, 1\n"
"s_cmp_lt_i32 s78, 0\n"
"s_cbranch_scc1 SD298_%=\n"
"s_sleep 1\n"
"buffer_load_dwordx4 v[48:51], v2, s[32:35], s85 offen offset:1024 sc1\n"
"buffer_load_dwordx4 v[52:55], v2, s[32:35], s85 offen offset:2048 sc1\n"
"v_max3_u32 v0, v57, v59, v61\n"
"v_max_u32 v0, v63, v0\n"
"v_cmp_eq_u32 vcc, -1, v0\n"
"s_nop 0\n"
"s_cbranch_vccz L299_%=\n"
"buffer_load_dwordx4 v[56:59], v2, s[32:35], s85 offen offset:3072 sc1\n"
"s_add_u32 s94, s85, 4096\n"
"buffer_load_dwordx4 v[60:63], v2, s[32:35], s94 offen offset:0 sc1\n"
"L299_%=:\n"
"v_max3_u32 v0, v65, v67, v69\n"
"v_max_u32 v0, v71, v0\n"
"v_cmp_eq_u32 vcc, -1, v0\n"
"s_nop 0\n"
"s_cbranch_vccz L300_%=\n"
"s_add_u32 s94, s85, 4096\n"
"buffer_load_dwordx4 v[64:67], v2, s[32:35], s94 offen offset:1024 sc1\n"
"s_add_u32 s94, s85, 4096\n"
"buffer_load_dwordx4 v[68:71], v2, s[32:35], s94 offen offset:2048 sc1\n"
"L300_%=:\n"
"s_waitcnt vmcnt(0) lgkmcnt(0)\n"
"v_max3_u32 v0, v49, v51, v53\n"
"v_max_u32 v0, v55, v0\n"
"v_cmp_eq_u32 vcc, -1, v0\n"
"s_waitcnt vmcnt(0) lgkmcnt(0)\n"
"s_cbranch_vccnz SP297_%=\n"
"SD298_%=:\n"
"s_nop 15\n"
"s_branch BK296_%=\n"
"RT301_%=:\n"
"s_nop 15\n"
"s_waitcnt vmcnt(0) lgkmcnt(0)\n"
"SP303_%=:\n"
"s_sub_u32 s78, s78, 1\n"
"s_cmp_lt_i32 s78, 0\n"
"s_cbranch_scc1 SD304_%=\n"
"s_sleep 1\n"
"buffer_load_dwordx4 v[56:59], v2, s[32:35], s85 offen offset:3072 sc1\n"
"s_add_u32 s94, s85, 4096\n"
"buffer_load_dwordx4 v[60:63], v2, s[32:35], s94 offen offset:0 sc1\n"
"v_max3_u32 v0, v65, v67, v69\n"
"v_max_u32 v0, v71, v0\n"
"v_cmp_eq_u32 vcc, -1, v0\n"
"s_nop 0\n"
"s_cbranch_vccz L305_%=\n"
"s_add_u32 s94, s85, 4096\n"
"buffer_load_dwordx4 v[64:67], v2, s[32:35], s94 offen offset:1024 sc1\n"
"s_add_u32 s94, s85, 4096\n"
"buffer_load_dwordx4 v[68:71], v2, s[32:35], s94 offen offset:2048 sc1\n"
"L305_%=:\n"
"s_waitcnt vmcnt(0) lgkmcnt(0)\n"
"v_max3_u32 v0, v57, v59, v61\n"
"v_max_u32 v0, v63, v0\n"
"v_cmp_eq_u32 vcc, -1, v0\n"
"s_waitcnt vmcnt(0) lgkmcnt(0)\n"
"s_cbranch_vccnz SP303_%=\n"
"SD304_%=:\n"
"s_nop 15\n"
"s_branch BK302_%=\n"
"RT306_%=:\n"
"s_nop 15\n"
"s_waitcnt vmcnt(0) lgkmcnt(0)\n"
"SP308_%=:\n"
"s_sub_u32 s78, s78, 1\n"
"s_cmp_lt_i32 s78, 0\n"
"s_cbranch_scc1 SD309_%=\n"
"s_sleep 1\n"
"s_add_u32 s94, s85, 4096\n"
"buffer_load_dwordx4 v[64:67], v2, s[32:35], s94 offen offset:1024 sc1\n"
"s_add_u32 s94, s85, 4096\n"
"buffer_load_dwordx4 v[68:71], v2, s[32:35], s94 offen offset:2048 sc1\n"
"s_waitcnt vmcnt(0) lgkmcnt(0)\n"
"v_max3_u32 v0, v65, v67, v69\n"
"v_max_u32 v0, v71, v0\n"
"v_cmp_eq_u32 vcc, -1, v0\n"
"s_waitcnt vmcnt(0) lgkmcnt(0)\n"
"s_cbranch_vccnz SP308_%=\n"
"SD309_%=:\n"
"s_nop 15\n"
"s_branch BK307_%=\n"
"LEND_%=:\n"
        : "+v"(tid)
        : "s"(__builtin_amdgcn_kernarg_segment_ptr()), "s"(blockIdx.x), "s"((unsigned)(size_t)lds)
        : "memory", "vcc", "scc",
"v1", "v2", "v3", "v4", "v5", "v6", "v7", "v8", "v9", "v10", "v11", "v12", "v13", "v14", "v15", "v16", "v17", "v18", "v19", "v20", "v21", "v22", "v23", "v24", "v25", "v26", "v27", "v28", "v29", "v30", "v31", "v32", "v33", "v34", "v35", "v36", "v37", "v38", "v39", "v40", "v41", "v42", "v43", "v44", "v45", "v46", "v47", "v48", "v49", "v50", "v51", "v52", "v53", "v54", "v55", "v56", "v57", "v58", "v59", "v60", "v61", "v62", "v63", "v64", "v65", "v66", "v67", "v68", "v69", "v70", "v71", "v72", "v73", "v74", "v75", "v76", "v77", "v78", "v79", "v80", "v81", "v82", "v83", "v84", "v85", "v86", "v87", "v88", "v89", "v90", "v91", "v92", "v93", "v94", "v95", "v96", "v97", "v98", "v99", "v100", "v101", "v102", "v103", "v104", "v105", "v106", "v107", "v108", "v109", "v110", "v111", "v112", "v113", "v114", "v115", "v116", "v117", "v118", "v119", "v120", "v121", "v122", "v123", "v124", "v125", "v126", "v127", "v128", "v129", "v130", "v131", "v132", "v133", "v134", "v135", "v136", "v137", "v138", "v139", "v140", "v141", "v142", "v143", "v144", "v145", "v146", "v147", "v148", "v149", "v150", "v151", "v152", "v153", "v154", "v155", "v156", "v157", "v158", "v159", "v160", "v161", "v162", "v163", "v164", "v165", "v166", "v167", "v168", "v169", "v170", "v171", "v172", "v173", "v174", "v175", "v176", "v177", "v178", "v179", "v180", "v181", "v182", "v183", "v184", "v185", "v186", "v187", "v188", "v189", "v190", "v191", "v192", "v193", "v194", "v195", "v196", "v197", "v198", "v199", "v200", "v201", "v202", "v203", "v204", "v205", "v206", "v207", "v208", "v209", "v210", "v211", "v212", "v213", "v214", "v215", "v216", "v217", "v218", "v219", "v220", "v221", "v222", "v223", "v224", "v225", "v226", "v227", "v228", "v229", "v230", "v231", "v232", "v233", "v234", "v235", "v236", "v237", "v238", "v239", "v240", "v241", "v242", "v243", "v244", "v245", "v246", "v247", "v248", "v249", "v250", "v251", "v252", "v253", "v254", "v255", "s8", "s9", "s10", "s11", "s12", "s13", "s14", "s15", "s16", "s17", "s18", "s19", "s20", "s21", "s22", "s23", "s24", "s25", "s26", "s27", "s28", "s29", "s30", "s31", "s33", "s34", "s35", "s36", "s37", "s38", "s39", "s40", "s41", "s42", "s43", "s44", "s45", "s46", "s47", "s48", "s49", "s50", "s51", "s52", "s53", "s54", "s55", "s56", "s57", "s58", "s59", "s60", "s61", "s62", "s63", "s64", "s65", "s66", "s67", "s68", "s69", "s70", "s71", "s72", "s73", "s74", "s75", "s76", "s77", "s78", "s79", "s80", "s81", "s82", "s83", "s84", "s85", "s86", "s87", "s88", "s89", "s90", "s91", "s92", "s93", "s94", "s95", "s96", "s97", "s98", "s99"
    );
#endif
}

extern "C" void kernel_launch(void* const* d_in, const int* in_sizes, int n_in, void* d_out, int out_size, void* d_ws, size_t ws_size, hipStream_t stream) {
    fused_block_kernel<<<256, 512, 0, stream>>>(d_in[8], d_in[9], d_in[0], d_in[1], d_ws, d_in[4], d_in[2], d_in[5], d_in[7], d_in[6], d_out);
}
```
